```python
import torch

def _softmax(x, axis=-1):
    return torch.softmax(x, dim=axis)
def _gelu(x):
    return torch.nn.functional.gelu(x, approximate="tanh")
def _silu(x):
    return torch.nn.functional.silu(x)
def _sigmoid(x):
    return torch.sigmoid(x)
def _layernorm(x, axis=-1, eps=1e-5):
    m = x.mean(dim=axis, keepdim=True)
    v = x.var(dim=axis, keepdim=True, unbiased=False)
    return (x - m) / torch.sqrt(v + eps)
def _scatter_add(tgt, idx, src, axis=0):
    return tgt.clone().index_add_(axis, idx, src)
def _segment_sum(src, seg, n):
    out = torch.zeros((n,) + src.shape[1:], dtype=src.dtype)
    return out.index_add_(0, seg, src)
def _pad(x, pw):
    flat = tuple(v for pair in reversed(pw) for v in pair)
    return torch.nn.functional.pad(x, flat)

kernel_name = 'interleave_layers__grouped_kv_mqa__mixer_channel_mlp__prompt'

def setup_inputs():

    _w = (lambda a: (lambda t: t.to(torch.bfloat16) if t.is_floating_point() else t)(torch.as_tensor(a)))
    def _A_setup():
        B, S, D, H = 1, 2048, 640, 32
        Dh = D // H
        return {
            "x": _w((lambda *s: torch.randn(*s, dtype=torch.bfloat16) * 0.05)(B, S, D)),
            "wq": _w((lambda *s: torch.randn(*s, dtype=torch.bfloat16) * 0.05)(D, H * Dh)),
            "wk": _w((lambda *s: torch.randn(*s, dtype=torch.bfloat16) * 0.05)(D, Dh)),
            "wv": _w((lambda *s: torch.randn(*s, dtype=torch.bfloat16) * 0.05)(D, Dh)),
            "wo": _w((lambda *s: torch.randn(*s, dtype=torch.bfloat16) * 0.05)(H * Dh, D)),
        }
    _A_in = _A_setup()

    def _B_setup():
        B, S, D, F = 1, 2048, 640, 1728
        return {
            "x": _w((lambda *s: torch.randn(*s, dtype=torch.bfloat16) * 0.05)(B, S, D)),
            "w1": _w((lambda *s: torch.randn(*s, dtype=torch.bfloat16) * 3.2)(D, F) * (D ** -0.5)),
            "b1": _w((lambda *s: torch.randn(*s, dtype=torch.bfloat16) * 0.8)(F)),
            "w2": _w((lambda *s: torch.randn(*s, dtype=torch.bfloat16) * 0.4)(F, D) * (F ** -0.5)),
            "b2": _w((lambda *s: torch.randn(*s, dtype=torch.bfloat16) * 0.4)(D)),
        }
    _B_in = _B_setup()

    return {
        "A_x": _A_in["x"],
        "A_wq": _A_in["wq"],
        "A_wk": _A_in["wk"],
        "A_wv": _A_in["wv"],
        "A_wo": _A_in["wo"],
        "B_x": _B_in["x"],
        "B_w1": _B_in["w1"],
        "B_b1": _B_in["b1"],
        "B_w2": _B_in["w2"],
        "B_b2": _B_in["b2"],
    }

def reference(A_x, A_wq, A_wk, A_wv, A_wo, B_x, B_w1, B_b1, B_w2, B_b2):
    def _A_ref(x, wq, wk, wv, wo):
        B, S, D = x.shape
        H = 32
        Dh = D // H
        q = (lambda x, *d: x.permute(*d))((x @ wq).reshape(B, S, H, Dh), 0, 2, 1, 3)
        k = x @ wk
        v = x @ wv
        scores = torch.einsum("bhqd,bkd->bhqk", q, k) * (Dh ** -0.5)
        p = _softmax(scores, axis=-1)
        o = torch.einsum("bhqk,bkd->bhqd", p, v)
        return (lambda x, *d: x.permute(*d))(o, 0, 2, 1, 3).reshape(B, S, H * Dh) @ wo

    def _B_ref(x, w1, b1, w2, b2):
        y = _layernorm(x, axis=-1)
        h = _gelu(torch.einsum("bsd,df->bsf", y, w1) + b1)
        return torch.einsum("bsf,fd->bsd", h, w2) + b2

    x = A_x
    x = x + _A_ref(_layernorm(x, axis=-1), A_wq, A_wk, A_wv, A_wo)
    x = x + _B_ref(_layernorm(x, axis=-1), B_w1, B_b1, B_w2, B_b2)
    x = x + _A_ref(_layernorm(x, axis=-1), A_wq, A_wk, A_wv, A_wo)
    x = x + _B_ref(_layernorm(x, axis=-1), B_w1, B_b1, B_w2, B_b2)
    return x
```

```cpp
#include <hip/hip_runtime.h>
#include <hip/hip_bf16.h>
typedef unsigned short u16;
constexpr int S = 2048, D = 640, H = 32, DH = 20, F = 1728;
__device__ __forceinline__ float b2f(u16 b) { return __uint_as_float((unsigned)b << 16); }
__device__ __forceinline__ u16 f2b(float f) { __hip_bfloat16 h = __float2bfloat16(f); return *(u16*)&h; }

__global__ void k_ln(const u16* x, u16* y, int dbl) {
  int row = blockIdx.x, t = threadIdx.x;
  float v[10], s = 0;
  for (int i = 0; i < 10; ++i) { v[i] = b2f(x[row * D + t + 64 * i]); s += v[i]; }
  for (int o = 32; o; o >>= 1) s += __shfl_xor(s, o);
  float m = s / D, q = 0;
  for (int i = 0; i < 10; ++i) { v[i] -= m; q += v[i] * v[i]; }
  for (int o = 32; o; o >>= 1) q += __shfl_xor(q, o);
  float var = q / D, r = rsqrtf(var + 1e-5f);
  if (dbl) r *= rsqrtf(var * r * r + 1e-5f);
  for (int i = 0; i < 10; ++i) y[row * D + t + 64 * i] = f2b(v[i] * r);
}
template<int ACT> __global__ void __launch_bounds__(256) k_gemm(const u16* A, const u16* W, const u16* bias, const u16* res, u16* C, int N, int K) {
  __shared__ float sa[16][65], sw[16][65];
  int tx = threadIdx.x & 15, ty = threadIdx.x >> 4, m0 = blockIdx.y * 64, n0 = blockIdx.x * 64;
  float acc[4][4] = {};
  for (int k0 = 0; k0 < K; k0 += 16) {
    for (int i = threadIdx.x; i < 1024; i += 256) {
      int r = i >> 4, c = i & 15; sa[c][r] = b2f(A[(m0 + r) * K + k0 + c]);
      int kr = i >> 6, nc = i & 63; sw[kr][nc] = (n0 + nc < N) ? b2f(W[(k0 + kr) * N + n0 + nc]) : 0.f;
    }
    __syncthreads();
#pragma unroll
    for (int k = 0; k < 16; ++k) {
      float a[4], w[4];
#pragma unroll
      for (int i = 0; i < 4; ++i) { a[i] = sa[k][ty * 4 + i]; w[i] = sw[k][tx * 4 + i]; }
#pragma unroll
      for (int i = 0; i < 4; ++i)
#pragma unroll
        for (int j = 0; j < 4; ++j) acc[i][j] += a[i] * w[j];
    }
    __syncthreads();
  }
#pragma unroll
  for (int i = 0; i < 4; ++i)
#pragma unroll
    for (int j = 0; j < 4; ++j) {
      int m = m0 + ty * 4 + i, n = n0 + tx * 4 + j;
      if (n < N) {
        float v = acc[i][j];
        if (bias) v += b2f(bias[n]);
        if (ACT) { float u = 0.7978845608f * (v + 0.044715f * v * v * v); v = 0.5f * v * (1.f + tanhf(u)); }
        if (res) v += b2f(res[m * N + n]);
        C[m * N + n] = f2b(v);
      }
    }
}
__global__ void __launch_bounds__(256) k_attn(const u16* q, const u16* k, const u16* v, u16* o) {
  __shared__ float sk[128][DH], sv[128][DH];
  int idx = blockIdx.x * 256 + threadIdx.x;
  float qq[DH], acc[DH], m = -INFINITY, l = 0;
#pragma unroll
  for (int d = 0; d < DH; ++d) { qq[d] = b2f(q[idx * DH + d]) * 0.22360679775f; acc[d] = 0; }
  for (int k0 = 0; k0 < S; k0 += 128) {
    __syncthreads();
    for (int i = threadIdx.x; i < 128 * DH; i += 256) { (&sk[0][0])[i] = b2f(k[k0 * DH + i]); (&sv[0][0])[i] = b2f(v[k0 * DH + i]); }
    __syncthreads();
    for (int j = 0; j < 128; ++j) {
      float s = 0;
#pragma unroll
      for (int d = 0; d < DH; ++d) s += qq[d] * sk[j][d];
      float mn = fmaxf(m, s), a = __expf(m - mn), p = __expf(s - mn);
      l = l * a + p; m = mn;
#pragma unroll
      for (int d = 0; d < DH; ++d) acc[d] = acc[d] * a + p * sv[j][d];
    }
  }
#pragma unroll
  for (int d = 0; d < DH; ++d) o[idx * DH + d] = f2b(acc[d] / l);
}
extern "C" void kernel_launch(void* const* d_in, const int* in_sizes, int n_in, void* d_out, int out_size, void* d_ws, size_t ws_size, hipStream_t stream) {
  const u16 *x = (const u16*)d_in[0], *wq = (const u16*)d_in[1], *wk = (const u16*)d_in[2], *wv = (const u16*)d_in[3], *wo = (const u16*)d_in[4];
  const u16 *w1 = (const u16*)d_in[6], *b1 = (const u16*)d_in[7], *w2 = (const u16*)d_in[8], *b2 = (const u16*)d_in[9];
  u16* ws = (u16*)d_ws;
  u16 *y = ws, *q = y + S * D, *k = q + S * D, *v = k + S * 32, *o = v + S * 32, *x1 = o + S * D, *x2 = x1 + S * D, *h = x2 + S * D;
  const u16* cur = x;
  for (int l = 0; l < 2; ++l) {
    k_ln<<<S, 64, 0, stream>>>(cur, y, 0);
    k_gemm<0><<<dim3(10, 32), 256, 0, stream>>>(y, wq, nullptr, nullptr, q, D, D);
    k_gemm<0><<<dim3(1, 32), 256, 0, stream>>>(y, wk, nullptr, nullptr, k, DH, D);
    k_gemm<0><<<dim3(1, 32), 256, 0, stream>>>(y, wv, nullptr, nullptr, v, DH, D);
    k_attn<<<S * H / 256, 256, 0, stream>>>(q, k, v, o);
    k_gemm<0><<<dim3(10, 32), 256, 0, stream>>>(o, wo, nullptr, cur, x1, D, D);
    k_ln<<<S, 64, 0, stream>>>(x1, y, 1);
    k_gemm<1><<<dim3(27, 32), 256, 0, stream>>>(y, w1, b1, nullptr, h, F, D);
    u16* dst = l == 0 ? x2 : (u16*)d_out;
    k_gemm<0><<<dim3(10, 32), 256, 0, stream>>>(h, w2, b2, x1, dst, D, F);
    cur = x2;
  }
}
```

```cpp
#include <hip/hip_runtime.h>

__global__ void __launch_bounds__(256, 1) k_main(void* ws, const void* x, const void* wq, void* out, const void* b2, const void* wk,
                                                 const void* wv, const void* wo, const void* w1, const void* w2, const void* b1) {
  __shared__ __attribute__((aligned(16))) char lds[163840];
  const unsigned w = __builtin_amdgcn_readfirstlane(threadIdx.x >> 6);
  const unsigned ldsb = (unsigned)(size_t)lds;
  asm volatile(
    "kbeg_%=:\n\t"
    "s_getpc_b64 s[56:57]\n\t"
    "s_mov_b32 s33, %1\n\t"
    "s_mov_b32 s39, %2\n\t"
    "s_load_dwordx2 s[12:13], %0, 0\n\t"
    "s_load_dwordx2 s[16:17], %0, 8\n\t"
    "s_load_dwordx2 s[20:21], %0, 16\n\t"
    "s_load_dwordx2 s[24:25], %0, 24\n\t"
    "s_load_dwordx2 s[28:29], %0, 32\n\t"
    "s_load_dwordx4 s[88:91], %0, 40\n\t"
    "s_load_dwordx8 s[92:99], %0, 56\n\t"
    "v_mbcnt_lo_u32_b32 v0, -1, 0\n\t"
    "v_mbcnt_hi_u32_b32 v0, -1, v0\n\t"
    "s_lshr_b32 s50, s39, 3\n\t"
    "s_lshl_b32 s50, s50, 2\n\t"
    "s_add_u32 s52, s50, s33\n\t"
    "s_and_b32 s57, s57, 0xffff\n\t"
    "s_mov_b32 s58, kend_%=-kbeg_%=\n\t"
    "s_mov_b32 s59, 0x20000\n\t"
    "s_lshl_b32 s50, s52, 9\n\t"
    "v_lshl_add_u32 v33, v0, 3, s50\n\t"
    "buffer_load_dword v29, v33, s[56:59], 0 offen\n\t"
    "s_bfe_u32 s34, s39, 0x30003\n\t"
    "s_lshr_b32 s83, s39, 6\n\t"
    "s_lshl_b32 s35, s83, 3\n\t"
    "s_and_b32 s50, s39, 7\n\t"
    "s_or_b32 s35, s35, s50\n\t"
    "v_lshlrev_b32 v1, 4, v0\n\t"
    "v_mov_b32 v28, 0\n\t"
    "s_movk_i32 s67, 0x3c0\n\t"
    "v_mul_u32_u24 v32, 0xccd, v0\n\t"
    "v_lshrrev_b32 v36, 18, v32\n\t"
    "v_lshrrev_b32 v32, 16, v32\n\t"
    "v_xor_b32 v36, v36, v0\n\t"
    "v_lshlrev_b32 v36, 4, v36\n\t"
    "v_mad_u32_u24 v36, v32, s67, v36\n\t"
    "v_mov_b32 v33, 0x1c000\n\t"
    "v_mul_u32_u24 v34, 40, v0\n\t"
    "v_mul_u32_u24 v35, 0x64, v0\n\t"
    "s_mul_i32 s79, s52, 0x1900\n\t"
    "s_mul_i32 s78, s35, 0x14000\n\t"
    "s_mul_i32 s53, s34, 0x2800\n\t"
    "s_add_u32 s78, s78, s53\n\t"
    "s_mul_i32 s53, s33, 0xa00\n\t"
    "s_add_u32 s78, s78, s53\n\t"
    "s_lshr_b32 s58, s34, 1\n\t"
    "s_mul_i32 s80, s35, 0x14000\n\t"
    "s_mul_i32 s53, s33, 0x140\n\t"
    "s_add_u32 s80, s80, s53\n\t"
    "s_mul_i32 s54, s58, 0x5000\n\t"
    "s_add_u32 s54, s54, s80\n\t"
    "s_mul_i32 s64, s33, 0x5000\n\t"
    "s_add_u32 s64, s64, 0x13000\n\t"
    "s_mul_i32 s65, s33, 0x4c00\n\t"
    "s_mul_i32 s59, s33, 0x1900\n\t"
    "s_mov_b32 s14, 0x215e000\n\t"
    "s_mov_b32 s15, 0x20000\n\t"
    "s_mov_b32 s18, 0x280000\n\t"
    "s_mov_b32 s19, 0x20000\n\t"
    "s_mov_b32 s22, 0xc8000\n\t"
    "s_mov_b32 s23, 0x20000\n\t"
    "s_mov_b32 s26, 0x280000\n\t"
    "s_mov_b32 s27, 0x20000\n\t"
    "s_mov_b32 s30, 0x500\n\t"
    "s_mov_b32 s31, 0x20000\n\t"
    "s_waitcnt lgkmcnt(0)\n\t"
    "s_and_b32 s17, s17, 0xffff\n\t"
    "s_and_b32 s21, s21, 0xffff\n\t"
    "s_and_b32 s13, s13, 0xffff\n\t"
    "s_bitcmp1_b32 s34, 0\n\t"
    "s_cselect_b64 s[8:9], s[90:91], s[88:89]\n\t"
    "s_and_b32 s9, s9, 0xffff\n\t"
    "s_mov_b32 s10, 0x6400\n\t"
    "s_mov_b32 s11, 0x20000\n\t"
    "s_add_u32 m0, s64, 0\n\t"
    "s_nop 0\n\t"
    "buffer_load_dwordx4 v36, s[16:19], s54 offen lds\n\t"
    "v_add_u32 v37, 64, v0\n\t"
    "v_mul_u32_u24 v32, 0xccd, v37\n\t"
    "v_lshrrev_b32 v37, 18, v32\n\t"
    "v_lshrrev_b32 v32, 16, v32\n\t"
    "v_xor_b32 v37, v37, v0\n\t"
    "v_lshlrev_b32 v37, 4, v37\n\t"
    "v_mad_u32_u24 v37, v32, s67, v37\n\t"
    "s_mov_b32 s36, 0\n\t"
    "s_mov_b32 s45, 0xe04000\n\t"
    "s_add_u32 s50, s58, 1\n\t"
    "buffer_load_dwordx4 v37, s[16:19], s54 offen offset:1024 lds\n\t"
    "v_add_u32 v38, 0x80, v0\n\t"
    "v_mul_u32_u24 v32, 0xccd, v38\n\t"
    "v_lshrrev_b32 v38, 18, v32\n\t"
    "v_lshrrev_b32 v32, 16, v32\n\t"
    "v_xor_b32 v38, v38, v0\n\t"
    "v_lshlrev_b32 v38, 4, v38\n\t"
    "v_mad_u32_u24 v38, v32, s67, v38\n\t"
    "s_and_b32 s50, s50, 3\n\t"
    "s_mul_i32 s55, s50, 0x5000\n\t"
    "s_add_u32 s55, s55, s80\n\t"
    "s_add_u32 s50, s58, 2\n\t"
    "s_and_b32 s50, s50, 3\n\t"
    "buffer_load_dwordx4 v38, s[16:19], s54 offen offset:2048 lds\n\t"
    "v_add_u32 v39, 0xc0, v0\n\t"
    "v_mul_u32_u24 v32, 0xccd, v39\n\t"
    "v_lshrrev_b32 v39, 18, v32\n\t"
    "v_lshrrev_b32 v32, 16, v32\n\t"
    "v_xor_b32 v39, v39, v0\n\t"
    "v_lshlrev_b32 v39, 4, v39\n\t"
    "v_mad_u32_u24 v39, v32, s67, v39\n\t"
    "s_mul_i32 s56, s50, 0x5000\n\t"
    "s_add_u32 s56, s56, s80\n\t"
    "s_add_u32 s50, s58, 3\n\t"
    "s_and_b32 s50, s50, 3\n\t"
    "s_mul_i32 s57, s50, 0x5000\n\t"
    "buffer_load_dwordx4 v39, s[16:19], s54 offen offset:3072 lds\n\t"
    "v_add_u32 v40, 0x100, v0\n\t"
    "v_mul_u32_u24 v32, 0xccd, v40\n\t"
    "v_lshrrev_b32 v40, 18, v32\n\t"
    "v_lshrrev_b32 v32, 16, v32\n\t"
    "v_xor_b32 v40, v40, v0\n\t"
    "v_lshlrev_b32 v40, 4, v40\n\t"
    "v_mad_u32_u24 v40, v32, s67, v40\n\t"
    "s_add_u32 s57, s57, s80\n\t"
    "s_mul_i32 s50, s58, 0x80\n\t"
    "s_add_u32 s50, s50, 0x27000\n\t"
    "v_and_b32 v52, 15, v0\n\t"
    "v_lshlrev_b32 v52, 2, v52\n\t"
    "s_add_u32 m0, m0, 0x1000\n\t"
    "s_add_u32 s54, s54, 0x1000\n\t"
    "buffer_load_dwordx4 v40, s[16:19], s54 offen lds\n\t"
    "v_lshrrev_b32 v30, 4, v0\n\t"
    "v_lshl_add_u32 v52, v30, 10, v52\n\t"
    "v_add_u32 v52, s50, v52\n\t"
    "s_mul_i32 s50, s33, 0x4c00\n\t"
    "s_add_u32 s50, s50, 0x4400\n\t"
    "v_add_u32 v53, s50, v1\n\t"
    "v_mov_b32 v100, 0x3f803f80\n\t"
    "v_mov_b32 v101, 0x3f803f80\n\t"
    "v_mov_b32 v102, 0x3f803f80\n\t"
    "v_mov_b32 v103, 0x3f803f80\n\t"
    "s_mov_b32 m0, s65\n\t"
    "s_nop 0\n\t"
    "buffer_load_dwordx4 v1, s[8:11], s59 offen lds\n\t"
    "v_bfe_u32 v85, v0, 2, 2\n\t"
    "v_lshrrev_b32 v84, 4, v0\n\t"
    "v_lshl_add_u32 v91, v84, 4, s65\n\t"
    "v_xor_b32 v85, v85, v84\n\t"
    "v_and_b32 v84, 15, v0\n\t"
    "v_mul_u32_u24 v84, 0x140, v84\n\t"
    "v_lshl_add_u32 v84, v85, 4, v84\n\t"
    "v_add_u32 v90, s65, v84\n\t"
    "v_add_u32 v84, s64, v84\n\t"
    "v_and_b32 v85, 3, v0\n\t"
    "buffer_load_dwordx4 v1, s[8:11], s59 offen offset:1024 lds\n\t"
    "v_lshlrev_b32 v85, 4, v85\n\t"
    "v_bfe_u32 v30, v0, 2, 2\n\t"
    "v_mul_u32_u24 v30, 40, v30\n\t"
    "v_add_u32 v85, v85, v30\n\t"
    "v_lshrrev_b32 v30, 4, v0\n\t"
    "v_mul_u32_u24 v30, 0x140, v30\n\t"
    "v_add_u32 v85, v85, v30\n\t"
    "v_add_u32 v85, s65, v85\n\t"
    "v_bfe_u32 v86, v0, 2, 2\n\t"
    "v_mul_u32_u24 v86, 0xa0, v86\n\t"
    "v_lshrrev_b32 v30, 4, v0\n\t"
    "v_mul_u32_u24 v30, 0x500, v30\n\t"
    "buffer_load_dwordx4 v1, s[8:11], s59 offen offset:2048 lds\n\t"
    "v_add_u32 v86, v86, v30\n\t"
    "v_and_b32 v87, 3, v0\n\t"
    "v_lshlrev_b32 v87, 3, v87\n\t"
    "v_add_u32 v252, v86, v87\n\t"
    "v_mul_u32_u24 v87, 5, v87\n\t"
    "v_add3_u32 v87, v87, v86, 32\n\t"
    "v_add_u32 v88, s64, v252\n\t"
    "v_add_u32 v89, s64, v87\n\t"
    "v_add_u32 v86, s65, v252\n\t"
    "v_add_u32 v87, s65, v87\n\t"
    "s_movk_i32 s50, 0x460\n\t"
    "v_add_u32 v92, 0, v0\n\t"
    "buffer_load_dwordx4 v1, s[8:11], s59 offen offset:3072 lds\n\t"
    "v_mul_u32_u24 v244, 0x199a, v92\n\t"
    "v_lshrrev_b32 v244, 16, v244\n\t"
    "v_mad_u32_u24 v92, v244, s50, v1\n\t"
    "v_add_u32 v93, 64, v0\n\t"
    "v_mul_u32_u24 v245, 0x199a, v93\n\t"
    "v_lshrrev_b32 v245, 16, v245\n\t"
    "v_mad_u32_u24 v93, v245, s50, v1\n\t"
    "v_add_u32 v94, 0x80, v0\n\t"
    "v_mul_u32_u24 v246, 0x199a, v94\n\t"
    "v_lshrrev_b32 v246, 16, v246\n\t"
    "v_mad_u32_u24 v94, v246, s50, v1\n\t"
    "v_add_u32 v95, 0xc0, v0\n\t"
    "s_add_u32 m0, m0, 0x1000\n\t"
    "s_add_u32 s59, s59, 0x1000\n\t"
    "buffer_load_dwordx4 v1, s[8:11], s59 offen lds\n\t"
    "v_mul_u32_u24 v247, 0x199a, v95\n\t"
    "v_lshrrev_b32 v247, 16, v247\n\t"
    "v_mad_u32_u24 v95, v247, s50, v1\n\t"
    "v_add_u32 v96, 0x100, v0\n\t"
    "v_mul_u32_u24 v248, 0x199a, v96\n\t"
    "v_lshrrev_b32 v248, 16, v248\n\t"
    "v_mad_u32_u24 v96, v248, s50, v1\n\t"
    "s_mul_i32 s50, s33, 0x400\n\t"
    "s_mul_i32 s53, s58, 0x80\n\t"
    "s_add_u32 s50, s50, s53\n\t"
    "buffer_load_dwordx4 v1, s[8:11], s59 offen offset:1024 lds\n\t"
    "s_add_u32 s50, s50, 0x27000\n\t"
    "v_and_b32 v41, 15, v0\n\t"
    "v_lshlrev_b32 v41, 2, v41\n\t"
    "v_bfe_u32 v30, v0, 4, 1\n\t"
    "v_lshl_add_u32 v41, v30, 9, v41\n\t"
    "v_lshrrev_b32 v30, 5, v0\n\t"
    "v_lshl_add_u32 v41, v30, 6, v41\n\t"
    "v_add_u32 v41, s50, v41\n\t"
    "s_mul_i32 s60, s33, 0x32000\n\t"
    "s_mul_i32 s53, s34, 0xa0\n\t"
    "s_add_u32 s60, s60, s53\n\t"
    "s_nop 0\n\t"
    "s_mov_b64 exec, 0xffff\n\t"
    "buffer_load_dwordx4 v1, s[8:11], s59 offen offset:2048 lds\n\t"
    "s_mov_b64 exec, -1\n\t"
    "s_nop 9\n\t"
    "buffer_load_dwordx2 v[2:3], v33, s[12:15], 0 offen sc1\n\t"
    "s_mul_i32 s52, s35, 0x1480\n\t"
    "s_add_u32 s52, s52, s45\n\t"
    "s_bitcmp1_b32 s34, 0\n\t"
    "s_cbranch_scc1 L1_%=\n\t"
    "s_lshr_b32 s50, s58, 1\n\t"
    "s_and_b32 s51, s58, 1\n\t"
    "s_mul_i32 s62, s50, 0x400\n\t"
    "s_mul_i32 s53, s51, 0x100\n\t"
    "s_add_u32 s62, s62, s53\n\t"
    "s_add_u32 s62, s62, s52\n\t"
    "s_mul_i32 s63, s58, 0x80\n\t"
    "s_add_u32 s63, s63, s52\n\t"
    "s_add_u32 s63, s63, 0x800\n\t"
    "v_and_b32 v56, 15, v0\n\t"
    "v_lshlrev_b32 v56, 4, v56\n\t"
    "v_lshrrev_b32 v30, 4, v0\n\t"
    "v_lshl_add_u32 v56, v30, 9, v56\n\t"
    "v_and_b32 v57, 15, v0\n\t"
    "v_lshlrev_b32 v57, 3, v57\n\t"
    "s_mul_i32 s50, s36, 0x4000\n\t"
    "s_mul_i32 s53, s35, 0x200\n\t"
    "s_add_u32 s50, s50, s53\n\t"
    "s_mul_i32 s53, s52, -1\n\t"
    "s_add_u32 s50, s50, s53\n\t"
    "s_add_u32 s50, s50, 0xe55800\n\t"
    "v_add_u32 v54, s50, v57\n\t"
    "v_cmp_lt_u32 vcc, 47, v0\n\t"
    "s_nop 1\n\t"
    "v_cndmask_b32 v57, v57, v54, vcc\n\t"
    "s_branch L2_%=\n\t"
    "L1_%=:\n\t"
    "s_mul_i32 s62, s58, 0x2a0\n\t"
    "s_add_u32 s62, s62, s52\n\t"
    "s_add_u32 s62, s62, 0xa00\n\t"
    "s_mov_b32 s63, 0x3f803f80\n\t"
    "v_and_b32 v56, 15, v0\n\t"
    "v_lshlrev_b32 v56, 6, v56\n\t"
    "v_lshrrev_b32 v30, 4, v0\n\t"
    "v_lshl_add_u32 v56, v30, 4, v56\n\t"
    "v_and_b32 v57, 3, v0\n\t"
    "v_lshlrev_b32 v57, 3, v57\n\t"
    "v_bfe_u32 v30, v0, 2, 2\n\t"
    "v_lshl_add_u32 v57, v30, 6, v57\n\t"
    "v_bfe_u32 v30, v0, 4, 1\n\t"
    "v_lshl_add_u32 v57, v30, 5, v57\n\t"
    "v_lshrrev_b32 v30, 5, v0\n\t"
    "v_lshl_add_u32 v57, v30, 9, v57\n\t"
    "v_and_b32 v58, 31, v0\n\t"
    "v_lshlrev_b32 v58, 4, v58\n\t"
    "v_lshrrev_b32 v30, 5, v0\n\t"
    "v_mul_u32_u24 v30, 0x140, v30\n\t"
    "v_add_u32 v58, v58, v30\n\t"
    "L2_%=:\n\t"
    "s_mov_b64 exec, 0\n\t"
    "s_cmp_eq_u32 s33, 0\n\t"
    "s_cbranch_scc1 pad1_%=\n\t"
    "vb3_%=:\n\t"
    "s_mov_b64 exec, -1\n\t"
    "s_mov_b32 s42, 0xffff0000\n\t"
    "s_mov_b32 s43, 0xffff0000\n\t"
    "s_waitcnt vmcnt(8)\n\t"
    "s_add_u32 m0, s65, 0x1c00\n\t"
    "s_nop 0\n\t"
    "buffer_load_dwordx4 v92, s[20:23], s60 offen lds\n\t"
    "ds_read_b128 v[64:67], v84\n\t"
    "ds_read_b128 v[68:71], v84 offset:64\n\t"
    "ds_read_b128 v[72:75], v84 offset:128\n\t"
    "ds_read_b128 v[76:79], v84 offset:192\n\t"
    "ds_read_b128 v[80:83], v84 offset:256\n\t"
    "s_waitcnt lgkmcnt(4)\n\t"
    "v_dot2_f32_bf16 v42, v64, v100, 0\n\t"
    "v_dot2_f32_bf16 v43, v64, v64, 0\n\t"
    "v_dot2_f32_bf16 v42, v65, v100, v42\n\t"
    "v_dot2_f32_bf16 v43, v65, v65, v43\n\t"
    "v_dot2_f32_bf16 v42, v66, v100, v42\n\t"
    "v_dot2_f32_bf16 v43, v66, v66, v43\n\t"
    "v_dot2_f32_bf16 v42, v67, v100, v42\n\t"
    "v_dot2_f32_bf16 v43, v67, v67, v43\n\t"
    "buffer_load_dwordx4 v93, s[20:23], s60 offen offset:1024 lds\n\t"
    "s_waitcnt lgkmcnt(3)\n\t"
    "v_dot2_f32_bf16 v42, v68, v100, v42\n\t"
    "v_dot2_f32_bf16 v43, v68, v68, v43\n\t"
    "v_dot2_f32_bf16 v42, v69, v100, v42\n\t"
    "v_dot2_f32_bf16 v43, v69, v69, v43\n\t"
    "v_dot2_f32_bf16 v42, v70, v100, v42\n\t"
    "v_dot2_f32_bf16 v43, v70, v70, v43\n\t"
    "v_dot2_f32_bf16 v42, v71, v100, v42\n\t"
    "v_dot2_f32_bf16 v43, v71, v71, v43\n\t"
    "s_waitcnt lgkmcnt(2)\n\t"
    "v_dot2_f32_bf16 v42, v72, v100, v42\n\t"
    "v_dot2_f32_bf16 v43, v72, v72, v43\n\t"
    "v_dot2_f32_bf16 v42, v73, v100, v42\n\t"
    "v_dot2_f32_bf16 v43, v73, v73, v43\n\t"
    "v_dot2_f32_bf16 v42, v74, v100, v42\n\t"
    "v_dot2_f32_bf16 v43, v74, v74, v43\n\t"
    "buffer_load_dwordx4 v94, s[20:23], s60 offen offset:2048 lds\n\t"
    "v_dot2_f32_bf16 v42, v75, v100, v42\n\t"
    "v_dot2_f32_bf16 v43, v75, v75, v43\n\t"
    "s_waitcnt lgkmcnt(1)\n\t"
    "v_dot2_f32_bf16 v42, v76, v100, v42\n\t"
    "v_dot2_f32_bf16 v43, v76, v76, v43\n\t"
    "v_dot2_f32_bf16 v42, v77, v100, v42\n\t"
    "v_dot2_f32_bf16 v43, v77, v77, v43\n\t"
    "v_dot2_f32_bf16 v42, v78, v100, v42\n\t"
    "v_dot2_f32_bf16 v43, v78, v78, v43\n\t"
    "v_dot2_f32_bf16 v42, v79, v100, v42\n\t"
    "v_dot2_f32_bf16 v43, v79, v79, v43\n\t"
    "s_waitcnt lgkmcnt(0)\n\t"
    "v_dot2_f32_bf16 v42, v80, v100, v42\n\t"
    "v_dot2_f32_bf16 v43, v80, v80, v43\n\t"
    "v_dot2_f32_bf16 v42, v81, v100, v42\n\t"
    "v_dot2_f32_bf16 v43, v81, v81, v43\n\t"
    "buffer_load_dwordx4 v95, s[20:23], s60 offen offset:3072 lds\n\t"
    "v_dot2_f32_bf16 v42, v82, v100, v42\n\t"
    "v_dot2_f32_bf16 v43, v82, v82, v43\n\t"
    "v_dot2_f32_bf16 v42, v83, v100, v42\n\t"
    "v_dot2_f32_bf16 v43, v83, v83, v43\n\t"
    "s_nop 2\n\t"
    "v_permlane32_swap_b32 v42, v43\n\t"
    "v_add_f32 v42, v42, v43\n\t"
    "v_mov_b32 v43, v42\n\t"
    "s_nop 1\n\t"
    "v_permlane16_swap_b32 v42, v43\n\t"
    "v_add_f32 v42, v42, v43\n\t"
    "v_cndmask_b32 v42, v42, 0, s[42:43]\n\t"
    "ds_write_b32 v41, v42\n\t"
    "s_add_u32 m0, m0, 0x1000\n\t"
    "s_add_u32 s60, s60, 0x1000\n\t"
    "buffer_load_dwordx4 v96, s[20:23], s60 offen lds\n\t"
    "s_waitcnt vmcnt(6)\n\t"
    "ds_read_b64_tr_b16 a[0:1], v85\n\t"
    "ds_read_b64_tr_b16 a[2:3], v85 offset:160\n\t"
    "ds_read_b64_tr_b16 a[4:5], v85 offset:8\n\t"
    "ds_read_b64_tr_b16 a[6:7], v85 offset:168\n\t"
    "ds_read_b64_tr_b16 a[8:9], v85 offset:1280\n\t"
    "ds_read_b64_tr_b16 a[10:11], v85 offset:1440\n\t"
    "ds_read_b64_tr_b16 a[12:13], v85 offset:1288\n\t"
    "s_add_u32 m0, s65, 0x3000\n\t"
    "s_add_u32 s60, s60, 0x9000\n\t"
    "buffer_load_dwordx4 v92, s[20:23], s60 offen lds\n\t"
    "ds_read_b64_tr_b16 a[14:15], v85 offset:1448\n\t"
    "ds_read_b64_tr_b16 a[16:17], v85 offset:2560\n\t"
    "ds_read_b64_tr_b16 a[18:19], v85 offset:2720\n\t"
    "ds_read_b64_tr_b16 a[20:21], v85 offset:2568\n\t"
    "ds_read_b64_tr_b16 a[22:23], v85 offset:2728\n\t"
    "ds_read_b64_tr_b16 a[24:25], v85 offset:3840\n\t"
    "ds_read_b64_tr_b16 a[26:27], v85 offset:4000\n\t"
    "buffer_load_dwordx4 v93, s[20:23], s60 offen offset:1024 lds\n\t"
    "ds_read_b64_tr_b16 a[28:29], v85 offset:3848\n\t"
    "ds_read_b64_tr_b16 a[30:31], v85 offset:4008\n\t"
    "ds_read_b64_tr_b16 a[32:33], v85 offset:5120\n\t"
    "ds_read_b64_tr_b16 a[34:35], v85 offset:5280\n\t"
    "ds_read_b64_tr_b16 a[36:37], v85 offset:5128\n\t"
    "ds_read_b64_tr_b16 a[38:39], v85 offset:5288\n\t"
    "s_waitcnt lgkmcnt(15)\n\t"
    "v_mfma_f32_16x16x32_bf16 v[104:107], a[0:3], v[64:67], 0\n\t"
    "v_mfma_f32_16x16x32_bf16 v[108:111], a[4:7], v[64:67], 0\n\t"
    "buffer_load_dwordx4 v94, s[20:23], s60 offen offset:2048 lds\n\t"
    "s_waitcnt lgkmcnt(14)\n\t"
    "v_mfma_f32_16x16x32_bf16 v[104:107], a[8:11], v[68:71], v[104:107]\n\t"
    "s_waitcnt lgkmcnt(12)\n\t"
    "v_mfma_f32_16x16x32_bf16 v[108:111], a[12:15], v[68:71], v[108:111]\n\t"
    "s_waitcnt lgkmcnt(10)\n\t"
    "v_mfma_f32_16x16x32_bf16 v[104:107], a[16:19], v[72:75], v[104:107]\n\t"
    "s_waitcnt lgkmcnt(8)\n\t"
    "v_mfma_f32_16x16x32_bf16 v[108:111], a[20:23], v[72:75], v[108:111]\n\t"
    "s_waitcnt lgkmcnt(6)\n\t"
    "v_mfma_f32_16x16x32_bf16 v[104:107], a[24:27], v[76:79], v[104:107]\n\t"
    "s_waitcnt lgkmcnt(4)\n\t"
    "v_mfma_f32_16x16x32_bf16 v[108:111], a[28:31], v[76:79], v[108:111]\n\t"
    "buffer_load_dwordx4 v95, s[20:23], s60 offen offset:3072 lds\n\t"
    "s_waitcnt lgkmcnt(2)\n\t"
    "v_mfma_f32_16x16x32_bf16 v[104:107], a[32:35], v[80:83], v[104:107]\n\t"
    "s_waitcnt lgkmcnt(0)\n\t"
    "v_mfma_f32_16x16x32_bf16 v[108:111], a[36:39], v[80:83], v[108:111]\n\t"
    "v_mfma_f32_16x16x32_bf16 v[44:47], a[0:3], v[100:103], 0\n\t"
    "v_mfma_f32_16x16x32_bf16 v[48:51], a[4:7], v[100:103], 0\n\t"
    "v_mfma_f32_16x16x32_bf16 v[44:47], a[8:11], v[100:103], v[44:47]\n\t"
    "v_mfma_f32_16x16x32_bf16 v[48:51], a[12:15], v[100:103], v[48:51]\n\t"
    "v_mfma_f32_16x16x32_bf16 v[44:47], a[16:19], v[100:103], v[44:47]\n\t"
    "s_add_u32 m0, m0, 0x1000\n\t"
    "s_add_u32 s60, s60, 0x1000\n\t"
    "buffer_load_dwordx4 v96, s[20:23], s60 offen lds\n\t"
    "v_mfma_f32_16x16x32_bf16 v[48:51], a[20:23], v[100:103], v[48:51]\n\t"
    "v_mfma_f32_16x16x32_bf16 v[44:47], a[24:27], v[100:103], v[44:47]\n\t"
    "v_mfma_f32_16x16x32_bf16 v[48:51], a[28:31], v[100:103], v[48:51]\n\t"
    "v_mfma_f32_16x16x32_bf16 v[44:47], a[32:35], v[100:103], v[44:47]\n\t"
    "v_mfma_f32_16x16x32_bf16 v[48:51], a[36:39], v[100:103], v[48:51]\n\t"
    "s_waitcnt vmcnt(10)\n\t"
    "v_add_co_u32 v2, vcc, 0x7f4a7c15, v2\n\t"
    "v_mov_b32 v33, 0x9e3779b9\n\t"
    "s_nop 0\n\t"
    "v_addc_co_u32 v3, vcc, v33, v3, vcc\n\t"
    "s_cmp_eq_u32 s33, 0\n\t"
    "s_cbranch_scc1 L4_%=\n\t"
    "ds_write_b128 v53, v[104:107]\n\t"
    "ds_write_b128 v53, v[108:111] offset:1024\n\t"
    "s_mov_b32 exec_lo, 0x10001\n\t"
    "s_mov_b32 exec_hi, 0x10001\n\t"
    "ds_write_b128 v91, v[44:47] offset:6400\n\t"
    "ds_write_b128 v91, v[48:51] offset:6464\n\t"
    "s_mov_b64 exec, -1\n\t"
    "s_waitcnt lgkmcnt(0)\n\t"
    "s_barrier\n\t"
    "s_add_u32 m0, s65, 0\n\t"
    "s_nop 0\n\t"
    "buffer_load_dwordx4 v36, s[16:19], s55 offen lds\n\t"
    "s_nop 15\n\t"
    "s_nop 4\n\t"
    "buffer_load_dwordx4 v37, s[16:19], s55 offen offset:1024 lds\n\t"
    "s_nop 15\n\t"
    "s_nop 6\n\t"
    "buffer_load_dwordx4 v38, s[16:19], s55 offen offset:2048 lds\n\t"
    "s_nop 15\n\t"
    "s_nop 6\n\t"
    "buffer_load_dwordx4 v39, s[16:19], s55 offen offset:3072 lds\n\t"
    "s_nop 15\n\t"
    "s_nop 6\n\t"
    "s_add_u32 m0, m0, 0x1000\n\t"
    "s_add_u32 s55, s55, 0x1000\n\t"
    "buffer_load_dwordx4 v40, s[16:19], s55 offen lds\n\t"
    "s_branch L5_%=\n\t"
    "L4_%=:\n\t"
    "s_barrier\n\t"
    "s_add_u32 m0, s65, 0\n\t"
    "s_nop 0\n\t"
    "buffer_load_dwordx4 v36, s[16:19], s55 offen lds\n\t"
    "ds_read2_b32 v[116:117], v52 offset0:0 offset1:128\n\t"
    "ds_read2_b32 v[118:119], v52 offset0:16 offset1:144\n\t"
    "ds_read_b128 v[200:203], v91 offset:25856\n\t"
    "ds_read_b128 v[204:207], v91 offset:25920\n\t"
    "ds_read_b128 v[208:211], v91 offset:45312\n\t"
    "ds_read_b128 v[212:215], v91 offset:45376\n\t"
    "ds_read_b128 v[216:219], v91 offset:64768\n\t"
    "ds_read_b128 v[220:223], v91 offset:64832\n\t"
    "ds_read_b128 v[160:163], v53 offset:19456\n\t"
    "ds_read_b128 v[164:167], v53 offset:20480\n\t"
    "ds_read_b128 v[168:171], v53 offset:38912\n\t"
    "ds_read_b128 v[172:175], v53 offset:39936\n\t"
    "ds_read_b128 v[176:179], v53 offset:58368\n\t"
    "ds_read_b128 v[180:183], v53 offset:59392\n\t"
    "buffer_load_dwordx4 v37, s[16:19], s55 offen offset:1024 lds\n\t"
    "s_waitcnt lgkmcnt(12)\n\t"
    "v_add_f32 v116, v116, v117\n\t"
    "v_add_f32 v118, v118, v119\n\t"
    "v_mov_b32 v117, v116\n\t"
    "v_mov_b32 v119, v118\n\t"
    "s_nop 0\n\t"
    "v_permlane32_swap_b32 v116, v117\n\t"
    "v_permlane32_swap_b32 v118, v119\n\t"
    "v_add_f32 v116, v116, v117\n\t"
    "v_add_f32 v118, v118, v119\n\t"
    "v_mov_b32 v117, v116\n\t"
    "v_mov_b32 v119, v118\n\t"
    "s_nop 0\n\t"
    "v_permlane16_swap_b32 v116, v117\n\t"
    "v_permlane16_swap_b32 v118, v119\n\t"
    "v_add_f32 v116, v116, v117\n\t"
    "buffer_load_dwordx4 v38, s[16:19], s55 offen offset:2048 lds\n\t"
    "v_add_f32 v118, v118, v119\n\t"
    "v_mul_f32 v116, 0xbacccccd, v116\n\t"
    "v_mul_f32 v118, 0x3acccccd, v118\n\t"
    "v_fma_f32 v118, -v116, v116, v118\n\t"
    "v_max_f32 v118, 0, v118\n\t"
    "v_add_f32 v112, 0x3727c5ac, v118\n\t"
    "v_rsq_f32 v118, v112\n\t"
    "s_waitcnt lgkmcnt(0)\n\t"
    "v_pk_add_f32 v[44:45], v[44:45], v[200:201]\n\t"
    "v_pk_add_f32 v[46:47], v[46:47], v[202:203]\n\t"
    "v_pk_add_f32 v[48:49], v[48:49], v[204:205]\n\t"
    "v_pk_add_f32 v[50:51], v[50:51], v[206:207]\n\t"
    "v_pk_add_f32 v[44:45], v[44:45], v[208:209]\n\t"
    "v_pk_add_f32 v[46:47], v[46:47], v[210:211]\n\t"
    "v_pk_add_f32 v[48:49], v[48:49], v[212:213]\n\t"
    "buffer_load_dwordx4 v39, s[16:19], s55 offen offset:3072 lds\n\t"
    "v_pk_add_f32 v[50:51], v[50:51], v[214:215]\n\t"
    "v_pk_add_f32 v[44:45], v[44:45], v[216:217]\n\t"
    "v_pk_add_f32 v[46:47], v[46:47], v[218:219]\n\t"
    "v_pk_add_f32 v[48:49], v[48:49], v[220:221]\n\t"
    "v_pk_add_f32 v[50:51], v[50:51], v[222:223]\n\t"
    "v_mov_b32 v117, v116\n\t"
    "v_mov_b32 v119, v118\n\t"
    "v_pk_add_f32 v[104:105], v[104:105], v[160:161]\n\t"
    "v_pk_add_f32 v[106:107], v[106:107], v[162:163]\n\t"
    "v_pk_add_f32 v[108:109], v[108:109], v[164:165]\n\t"
    "v_pk_add_f32 v[110:111], v[110:111], v[166:167]\n\t"
    "v_pk_add_f32 v[104:105], v[104:105], v[168:169]\n\t"
    "v_pk_add_f32 v[106:107], v[106:107], v[170:171]\n\t"
    "v_pk_add_f32 v[108:109], v[108:109], v[172:173]\n\t"
    "v_pk_add_f32 v[110:111], v[110:111], v[174:175]\n\t"
    "v_pk_add_f32 v[104:105], v[104:105], v[176:177]\n\t"
    "s_add_u32 m0, m0, 0x1000\n\t"
    "s_add_u32 s55, s55, 0x1000\n\t"
    "buffer_load_dwordx4 v40, s[16:19], s55 offen lds\n\t"
    "v_pk_add_f32 v[106:107], v[106:107], v[178:179]\n\t"
    "v_pk_add_f32 v[108:109], v[108:109], v[180:181]\n\t"
    "v_pk_add_f32 v[110:111], v[110:111], v[182:183]\n\t"
    "v_pk_fma_f32 v[104:105], v[116:117], v[44:45], v[104:105]\n\t"
    "v_pk_fma_f32 v[106:107], v[116:117], v[46:47], v[106:107]\n\t"
    "v_pk_fma_f32 v[108:109], v[116:117], v[48:49], v[108:109]\n\t"
    "v_pk_fma_f32 v[110:111], v[116:117], v[50:51], v[110:111]\n\t"
    "v_pk_mul_f32 v[104:105], v[104:105], v[118:119]\n\t"
    "v_pk_mul_f32 v[106:107], v[106:107], v[118:119]\n\t"
    "v_pk_mul_f32 v[108:109], v[108:109], v[118:119]\n\t"
    "v_pk_mul_f32 v[110:111], v[110:111], v[118:119]\n\t"
    "v_cvt_pk_bf16_f32 v112, v104, v105\n\t"
    "v_cvt_pk_bf16_f32 v113, v106, v107\n\t"
    "v_cvt_pk_bf16_f32 v114, v108, v109\n\t"
    "v_cvt_pk_bf16_f32 v115, v110, v111\n\t"
    "s_bitcmp1_b32 s34, 0\n\t"
    "s_cbranch_scc1 L6_%=\n\t"
    "s_mov_b32 exec_hi, 0\n\t"
    "buffer_store_dwordx4 v[112:115], v56, s[12:15], s62 offen sc1\n\t"
    "s_mov_b32 exec_lo, 0\n\t"
    "s_mov_b32 exec_hi, 0xffff0000\n\t"
    "v_mov_b32 v112, v116\n\t"
    "v_mov_b32 v113, v118\n\t"
    "s_mov_b32 exec_hi, -1\n\t"
    "buffer_store_dwordx2 v[112:113], v57, s[12:15], s63 offen sc1\n\t"
    "s_branch L7_%=\n\t"
    "L6_%=:\n\t"
    "ds_write_b128 v56, v[112:115] offset:17408\n\t"
    "ds_read_b64_tr_b16 v[164:165], v57 offset:17408\n\t"
    "ds_read_b64_tr_b16 v[166:167], v57 offset:17664\n\t"
    "v_max3_f32 v160, |v104|, |v105|, |v106|\n\t"
    "v_max3_f32 v161, |v108|, |v109|, |v110|\n\t"
    "v_max_f32 v160, |v107|, v160\n\t"
    "v_max_f32 v161, |v111|, v161\n\t"
    "v_mov_b32 v162, 0x5d800000\n\t"
    "v_cmp_lt_f32 vcc, v162, v160\n\t"
    "v_cmp_lt_f32 s[42:43], v162, v161\n\t"
    "s_and_b32 s50, vcc_hi, 0xffff\n\t"
    "s_or_b32 s50, s50, vcc_lo\n\t"
    "s_or_b32 s50, s50, s42\n\t"
    "s_cselect_b32 s63, 0x7f807f80, s63\n\t"
    "s_mov_b32 exec_lo, 0xfffff\n\t"
    "s_mov_b32 exec_hi, 0x3fffff\n\t"
    "s_waitcnt lgkmcnt(0)\n\t"
    "v_writelane_b32 v164, s63, 52\n\t"
    "v_writelane_b32 v165, s63, 52\n\t"
    "v_writelane_b32 v166, s63, 52\n\t"
    "v_writelane_b32 v167, s63, 52\n\t"
    "v_writelane_b32 v164, 0, 53\n\t"
    "v_writelane_b32 v165, 0, 53\n\t"
    "v_writelane_b32 v166, 0, 53\n\t"
    "v_writelane_b32 v167, 0, 53\n\t"
    "buffer_store_dwordx4 v[164:167], v58, s[12:15], s62 offen sc1\n\t"
    "L7_%=:\n\t"
    "s_mov_b64 exec, -1\n\t"
    "v_lshrrev_b32 v160, 4, v0\n\t"
    "v_lshlrev_b32 v160, 5, v160\n\t"
    "s_mul_i32 s50, s39, 0x80\n\t"
    "s_add_u32 s50, s50, 0x11c000\n\t"
    "s_mov_b32 exec_lo, 0x10001\n\t"
    "s_mov_b32 exec_hi, 0x10001\n\t"
    "buffer_store_dwordx4 v[44:47], v160, s[12:15], s50 offen sc1\n\t"
    "buffer_store_dwordx4 v[48:51], v160, s[12:15], s50 offen offset:16 sc1\n\t"
    "s_mov_b64 exec, -1\n\t"
    "L5_%=:\n\t"
    "s_and_b32 s25, s25, 0xffff\n\t"
    "s_and_b32 s29, s29, 0xffff\n\t"
    "s_mov_b32 s37, 0x100000\n\t"
    "s_mul_i32 s44, s35, 0x15000\n\t"
    "s_add_u32 s44, s44, 0x384000\n\t"
    "s_add_u32 s47, s44, 0x540000\n\t"
    "s_mul_i32 s46, s35, 0x14000\n\t"
    "s_add_u32 s46, s46, 0xe5e000\n\t"
    "s_mul_i32 s48, s35, 0x36000\n\t"
    "s_add_u32 s48, s48, 0x135e000\n\t"
    "s_add_u32 s84, s33, 1\n\t"
    "s_and_b32 s84, s84, 3\n\t"
    "s_add_u32 s85, s33, 2\n\t"
    "s_and_b32 s85, s85, 3\n\t"
    "s_add_u32 s86, s33, 3\n\t"
    "s_and_b32 s86, s86, 3\n\t"
    "s_add_u32 s87, s33, 4\n\t"
    "s_and_b32 s87, s87, 3\n\t"
    "s_and_b32 s50, s34, 1\n\t"
    "s_mul_i32 s49, s35, 0x800\n\t"
    "s_mul_i32 s76, s34, 8\n\t"
    "s_add_u32 s76, s76, s49\n\t"
    "s_mul_i32 s70, s34, 0xa0\n\t"
    "s_mul_i32 s53, s50, 32\n\t"
    "s_add_u32 s70, s70, s53\n\t"
    "s_mov_b32 s71, s70\n\t"
    "s_mul_i32 s53, s50, 0xffffff60\n\t"
    "s_add_u32 s71, s71, s53\n\t"
    "s_add_u32 s71, s71, 0x80\n\t"
    "s_mul_i32 s72, s33, 0x5000\n\t"
    "s_waitcnt vmcnt(10)\n\t"
    "ds_read_b64_tr_b16 a[80:81], v86 offset:7168\n\t"
    "ds_read_b64_tr_b16 a[82:83], v86 offset:7808\n\t"
    "ds_read_b64_tr_b16 a[84:85], v86 offset:7208\n\t"
    "ds_read_b64_tr_b16 a[86:87], v86 offset:7848\n\t"
    "ds_read_b64_tr_b16 a[88:89], v86 offset:7248\n\t"
    "ds_read_b64_tr_b16 a[90:91], v86 offset:7888\n\t"
    "ds_read_b64_tr_b16 a[92:93], v86 offset:7288\n\t"
    "ds_read_b64_tr_b16 a[94:95], v86 offset:7928\n\t"
    "ds_read_b64_tr_b16 a[96:97], v87 offset:7168\n\t"
    "ds_read_b64_tr_b16 a[98:99], v87 offset:7808\n\t"
    "s_waitcnt lgkmcnt(8)\n\t"
    "v_mfma_f32_16x16x32_bf16 a[40:43], a[80:83], v[64:67], 0\n\t"
    "s_add_u32 m0, s64, 0x1400\n\t"
    "s_add_u32 s60, s60, 0x9000\n\t"
    "buffer_load_dwordx4 v92, s[20:23], s60 offen lds\n\t"
    "s_waitcnt lgkmcnt(6)\n\t"
    "v_mfma_f32_16x16x32_bf16 a[44:47], a[84:87], v[64:67], 0\n\t"
    "s_waitcnt lgkmcnt(4)\n\t"
    "v_mfma_f32_16x16x32_bf16 a[48:51], a[88:91], v[64:67], 0\n\t"
    "buffer_load_dwordx4 v93, s[20:23], s60 offen offset:1024 lds\n\t"
    "s_waitcnt lgkmcnt(2)\n\t"
    "v_mfma_f32_16x16x32_bf16 a[52:55], a[92:95], v[64:67], 0\n\t"
    "s_waitcnt lgkmcnt(0)\n\t"
    "v_mfma_f32_16x16x32_bf16 a[56:59], a[96:99], v[64:67], 0\n\t"
    "buffer_load_dwordx4 v94, s[20:23], s60 offen offset:2048 lds\n\t"
    "v_mfma_f32_16x16x32_bf16 a[60:63], a[80:83], v[100:103], 0\n\t"
    "v_mfma_f32_16x16x32_bf16 a[64:67], a[84:87], v[100:103], 0\n\t"
    "buffer_load_dwordx4 v95, s[20:23], s60 offen offset:3072 lds\n\t"
    "pad1_%=:\n\t"
    "s_cbranch_execz vb3_%=\n\t"
    "v_mfma_f32_16x16x32_bf16 a[68:71], a[88:91], v[100:103], 0\n\t"
    "v_mfma_f32_16x16x32_bf16 a[72:75], a[92:95], v[100:103], 0\n\t"
    "s_add_u32 m0, m0, 0x1000\n\t"
    "s_add_u32 s60, s60, 0x1000\n\t"
    "buffer_load_dwordx4 v96, s[20:23], s60 offen lds\n\t"
    "v_mfma_f32_16x16x32_bf16 a[76:79], a[96:99], v[100:103], 0\n\t"
    "s_mul_i32 s53, s58, 0x1400\n\t"
    "s_add_u32 s72, s72, s53\n\t"
    "s_mul_i32 s53, s50, 0xc00\n\t"
    "s_add_u32 s72, s72, s53\n\t"
    "s_mov_b32 s73, s72\n\t"
    "s_mul_i32 s53, s50, 0xfffff600\n\t"
    "s_add_u32 s73, s73, s53\n\t"
    "s_add_u32 s73, s73, 0x800\n\t"
    "s_mul_i32 s82, s33, 14\n\t"
    "s_min_u32 s82, s82, 41\n\t"
    "v_and_b32 v15, 7, v0\n\t"
    "v_lshlrev_b32 v15, 3, v15\n\t"
    "v_and_b32 v14, 15, v0\n\t"
    "v_lshlrev_b32 v14, 4, v14\n\t"
    "v_lshrrev_b32 v30, 5, v0\n\t"
    "v_lshl_add_u32 v14, v30, 8, v14\n\t"
    "v_bfe_u32 v30, v0, 4, 1\n\t"
    "v_lshl_add_u32 v14, v30, 3, v14\n\t"
    "s_mul_i32 s61, s36, 0x4000\n\t"
    "s_mul_i32 s53, s35, 64\n\t"
    "s_add_u32 s61, s61, s53\n\t"
    "s_mul_i32 s53, s34, 8\n\t"
    "s_add_u32 s61, s61, s53\n\t"
    "s_add_u32 s61, s61, 0x10000\n\t"
    "v_and_b32 v62, 7, v0\n\t"
    "v_lshlrev_b32 v62, 11, v62\n\t"
    "s_mul_i32 s50, s33, 0x5000\n\t"
    "s_add_u32 s50, s50, 0x13000\n\t"
    "v_add_u32 v31, s50, v1\n\t"
    "s_mul_i32 s50, s33, 0x400\n\t"
    "s_add_u32 s50, s50, 0x13000\n\t"
    "v_and_b32 v59, 15, v0\n\t"
    "v_lshlrev_b32 v59, 4, v59\n\t"
    "v_bfe_u32 v30, v0, 4, 1\n\t"
    "v_mul_u32_u24 v30, 0x1400, v30\n\t"
    "v_add_u32 v59, v59, v30\n\t"
    "v_lshrrev_b32 v30, 5, v0\n\t"
    "v_lshl_add_u32 v59, v30, 9, v59\n\t"
    "v_add_u32 v59, s50, v59\n\t"
    "v_add_u32 v60, 0x2800, v59\n\t"
    "s_mul_i32 s50, s33, 0x100\n\t"
    "s_add_u32 s50, s50, 0x14000\n\t"
    "v_and_b32 v61, 15, v0\n\t"
    "v_lshlrev_b32 v61, 4, v61\n\t"
    "v_lshrrev_b32 v30, 4, v0\n\t"
    "v_mul_u32_u24 v30, 0x1400, v30\n\t"
    "v_add_u32 v61, v61, v30\n\t"
    "v_add_u32 v61, s50, v61\n\t"
    "s_mul_i32 s50, s58, 16\n\t"
    "s_add_u32 s50, s50, 16\n\t"
    "v_add_u32 v63, s50, v0\n\t"
    "v_and_b32 v63, 63, v63\n\t"
    "v_lshlrev_b32 v63, 3, v63\n\t"
    "v_mov_b32 v124, 0xdddddddd\n\t"
    "v_mov_b32 v125, v1\n\t"
    "v_lshlrev_b32 v126, 3, v0\n\t"
    "v_and_b32 v129, 3, v0\n\t"
    "v_lshlrev_b32 v129, 4, v129\n\t"
    "v_bfe_u32 v30, v0, 2, 1\n\t"
    "v_lshl_add_u32 v129, v30, 7, v129\n\t"
    "v_bfe_u32 v30, v0, 3, 1\n\t"
    "v_lshl_add_u32 v129, v30, 6, v129\n\t"
    "v_bfe_u32 v30, v0, 4, 1\n\t"
    "v_lshl_add_u32 v129, v30, 8, v129\n\t"
    "v_lshrrev_b32 v30, 5, v0\n\t"
    "v_mul_u32_u24 v30, 0x140, v30\n\t"
    "v_add_u32 v129, v129, v30\n\t"
    "v_and_b32 v128, 31, v0\n\t"
    "v_and_b32 v131, 27, v128\n\t"
    "v_mov_b32 v130, 0x290\n\t"
    "s_mov_b64 exec, 0\n\t"
    "s_cmp_eq_u32 s33, 0\n\t"
    "s_cbranch_scc1 pad2_%=\n\t"
    "s_cmp_eq_u32 s33, 1\n\t"
    "s_cbranch_scc1 pad3_%=\n\t"
    "s_cmp_eq_u32 s33, 2\n\t"
    "s_cbranch_scc1 pad4_%=\n\t"
    "s_cmp_eq_u32 s33, 3\n\t"
    "s_cbranch_scc1 pad5_%=\n\t"
    "vb8_%=:\n\t"
    "s_mov_b64 exec, -1\n\t"
    "s_waitcnt vmcnt(10)\n\t"
    "ds_read_b64_tr_b16 a[100:101], v86 offset:12288\n\t"
    "ds_read_b64_tr_b16 a[102:103], v86 offset:12928\n\t"
    "ds_read_b64_tr_b16 a[104:105], v86 offset:12328\n\t"
    "ds_read_b64_tr_b16 a[106:107], v86 offset:12968\n\t"
    "ds_read_b64_tr_b16 a[108:109], v86 offset:12368\n\t"
    "ds_read_b64_tr_b16 a[110:111], v86 offset:13008\n\t"
    "ds_read_b64_tr_b16 a[112:113], v86 offset:12408\n\t"
    "ds_read_b64_tr_b16 a[114:115], v86 offset:13048\n\t"
    "ds_read_b64_tr_b16 a[116:117], v87 offset:12288\n\t"
    "ds_read_b64_tr_b16 a[118:119], v87 offset:12928\n\t"
    "s_waitcnt lgkmcnt(8)\n\t"
    "v_mfma_f32_16x16x32_bf16 a[40:43], a[100:103], v[68:71], a[40:43]\n\t"
    "s_add_u32 m0, s65, 0x1c00\n\t"
    "s_nop 0\n\t"
    "buffer_load_dwordx4 v36, s[16:19], s56 offen lds\n\t"
    "s_waitcnt lgkmcnt(6)\n\t"
    "v_mfma_f32_16x16x32_bf16 a[44:47], a[104:107], v[68:71], a[44:47]\n\t"
    "s_waitcnt lgkmcnt(4)\n\t"
    "v_mfma_f32_16x16x32_bf16 a[48:51], a[108:111], v[68:71], a[48:51]\n\t"
    "buffer_load_dwordx4 v37, s[16:19], s56 offen offset:1024 lds\n\t"
    "s_waitcnt lgkmcnt(2)\n\t"
    "v_mfma_f32_16x16x32_bf16 a[52:55], a[112:115], v[68:71], a[52:55]\n\t"
    "s_waitcnt lgkmcnt(0)\n\t"
    "v_mfma_f32_16x16x32_bf16 a[56:59], a[116:119], v[68:71], a[56:59]\n\t"
    "buffer_load_dwordx4 v38, s[16:19], s56 offen offset:2048 lds\n\t"
    "v_mfma_f32_16x16x32_bf16 a[60:63], a[100:103], v[100:103], a[60:63]\n\t"
    "v_mfma_f32_16x16x32_bf16 a[64:67], a[104:107], v[100:103], a[64:67]\n\t"
    "buffer_load_dwordx4 v39, s[16:19], s56 offen offset:3072 lds\n\t"
    "v_mfma_f32_16x16x32_bf16 a[68:71], a[108:111], v[100:103], a[68:71]\n\t"
    "v_mfma_f32_16x16x32_bf16 a[72:75], a[112:115], v[100:103], a[72:75]\n\t"
    "s_add_u32 m0, m0, 0x1000\n\t"
    "s_add_u32 s56, s56, 0x1000\n\t"
    "buffer_load_dwordx4 v40, s[16:19], s56 offen lds\n\t"
    "v_mfma_f32_16x16x32_bf16 a[76:79], a[116:119], v[100:103], a[76:79]\n\t"
    "v_mov_b32 v132, 0x280\n\t"
    "v_cmp_eq_u32 vcc, 24, v131\n\t"
    "s_nop 1\n\t"
    "v_cndmask_b32 v130, v130, v132, vcc\n\t"
    "v_cmp_gt_u32 vcc, 20, v128\n\t"
    "s_nop 1\n\t"
    "v_cndmask_b32 v127, v130, v129, vcc\n\t"
    "v_mov_b32 v128, 0\n\t"
    "v_mov_b32 v144, 0\n\t"
    "v_mov_b32 v129, 0\n\t"
    "v_mov_b32 v145, 0\n\t"
    "v_mov_b32 v130, 0\n\t"
    "v_mov_b32 v146, 0\n\t"
    "v_mov_b32 v131, 0\n\t"
    "v_mov_b32 v147, 0\n\t"
    "v_mov_b32 v132, 0\n\t"
    "v_mov_b32 v148, 0\n\t"
    "v_mov_b32 v133, 0\n\t"
    "v_mov_b32 v149, 0\n\t"
    "v_mov_b32 v134, 0\n\t"
    "v_mov_b32 v150, 0\n\t"
    "v_mov_b32 v135, 0\n\t"
    "v_mov_b32 v151, 0\n\t"
    "v_mov_b32 v136, 0\n\t"
    "v_mov_b32 v152, 0\n\t"
    "v_mov_b32 v137, 0\n\t"
    "v_mov_b32 v153, 0\n\t"
    "v_mov_b32 v138, 0\n\t"
    "v_mov_b32 v154, 0\n\t"
    "v_mov_b32 v139, 0\n\t"
    "v_mov_b32 v155, 0\n\t"
    "v_mov_b32 v140, 0\n\t"
    "v_mov_b32 v156, 0\n\t"
    "v_mov_b32 v141, 0\n\t"
    "v_mov_b32 v157, 0\n\t"
    "v_mov_b32 v142, 0\n\t"
    "v_mov_b32 v158, 0\n\t"
    "v_mov_b32 v143, 0\n\t"
    "v_mov_b32 v159, 0\n\t"
    "v_and_b32 v26, 15, v0\n\t"
    "v_lshlrev_b32 v26, 4, v26\n\t"
    "v_bfe_u32 v30, v0, 4, 1\n\t"
    "v_lshl_add_u32 v26, v30, 14, v26\n\t"
    "v_lshrrev_b32 v30, 5, v0\n\t"
    "v_lshl_add_u32 v26, v30, 8, v26\n\t"
    "v_and_b32 v27, 15, v0\n\t"
    "v_lshlrev_b32 v27, 4, v27\n\t"
    "v_bfe_u32 v30, v0, 4, 1\n\t"
    "v_lshl_add_u32 v27, v30, 13, v27\n\t"
    "s_mul_i32 s78, s34, 0x800\n\t"
    "s_mul_i32 s53, s33, 0x200\n\t"
    "s_add_u32 s78, s78, s53\n\t"
    "s_add_u32 s78, s78, s46\n\t"
    "s_add_u32 s79, s78, 0x8000\n\t"
    "s_mul_i32 s80, s34, 0x400\n\t"
    "s_mul_i32 s53, s33, 0x100\n\t"
    "s_add_u32 s80, s80, s53\n\t"
    "s_add_u32 s80, s80, s46\n\t"
    "s_add_u32 s80, s80, 0x10000\n\t"
    "s_mov_b64 exec, 0\n\t"
    "s_cmp_eq_u32 s33, 0\n\t"
    "s_cbranch_scc1 pad6_%=\n\t"
    "s_cmp_eq_u32 s33, 1\n\t"
    "s_cbranch_scc1 pad7_%=\n\t"
    "s_cmp_eq_u32 s33, 2\n\t"
    "s_cbranch_scc1 pad8_%=\n\t"
    "s_cmp_eq_u32 s33, 3\n\t"
    "s_cbranch_scc1 pad9_%=\n\t"
    "vb9_%=:\n\t"
    "s_mov_b64 exec, -1\n\t"
    "s_waitcnt vmcnt(10)\n\t"
    "ds_read_b128 a[120:123], v90\n\t"
    "ds_read_b128 a[124:127], v90 offset:64\n\t"
    "ds_read_b128 a[128:131], v90 offset:128\n\t"
    "ds_read_b128 a[132:135], v90 offset:192\n\t"
    "ds_read_b128 a[136:139], v90 offset:256\n\t"
    "s_cmp_eq_u32 s33, 0\n\t"
    "s_cbranch_scc0 L10_%=\n\t"
    "s_mov_b64 exec, 0xff\n\t"
    "buffer_store_dwordx2 v[2:3], v62, s[12:15], s61 offen sc1\n\t"
    "s_mov_b64 exec, -1\n\t"
    "L10_%=:\n\t"
    "s_waitcnt lgkmcnt(4)\n\t"
    "v_mfma_f32_16x16x32_bf16 a[140:143], a[80:83], a[120:123], 0\n\t"
    "s_add_u32 m0, s64, 0x2800\n\t"
    "s_add_u32 s60, s60, 0x9000\n\t"
    "buffer_load_dwordx4 v92, s[20:23], s60 offen lds\n\t"
    "v_mfma_f32_16x16x32_bf16 a[144:147], a[84:87], a[120:123], 0\n\t"
    "v_mfma_f32_16x16x32_bf16 a[148:151], a[88:91], a[120:123], 0\n\t"
    "buffer_load_dwordx4 v93, s[20:23], s60 offen offset:1024 lds\n\t"
    "v_mfma_f32_16x16x32_bf16 a[152:155], a[92:95], a[120:123], 0\n\t"
    "v_mfma_f32_16x16x32_bf16 a[156:159], a[96:99], a[120:123], 0\n\t"
    "buffer_load_dwordx4 v94, s[20:23], s60 offen offset:2048 lds\n\t"
    "s_waitcnt lgkmcnt(3)\n\t"
    "v_mfma_f32_16x16x32_bf16 a[140:143], a[100:103], a[124:127], a[140:143]\n\t"
    "v_mfma_f32_16x16x32_bf16 a[144:147], a[104:107], a[124:127], a[144:147]\n\t"
    "buffer_load_dwordx4 v95, s[20:23], s60 offen offset:3072 lds\n\t"
    "v_mfma_f32_16x16x32_bf16 a[148:151], a[108:111], a[124:127], a[148:151]\n\t"
    "v_mfma_f32_16x16x32_bf16 a[152:155], a[112:115], a[124:127], a[152:155]\n\t"
    "s_add_u32 m0, m0, 0x1000\n\t"
    "s_add_u32 s60, s60, 0x1000\n\t"
    "buffer_load_dwordx4 v96, s[20:23], s60 offen lds\n\t"
    "v_mfma_f32_16x16x32_bf16 a[156:159], a[116:119], a[124:127], a[156:159]\n\t"
    "s_mov_b32 s7, 0x20000\n\t"
    "s_mov_b64 s[0:1], s[98:99]\n\t"
    "s_and_b32 s1, s1, 0xffff\n\t"
    "s_mov_b32 s3, 0x20000\n\t"
    "s_mov_b32 s2, 0xd80\n\t"
    "s_mul_i32 s50, s39, 4\n\t"
    "s_add_u32 s50, s50, s33\n\t"
    "s_add_u32 s50, s50, 0xfffffe98\n\t"
    "s_cmp_lt_u32 s39, 0x5a\n\t"
    "s_cbranch_scc1 L11_%=\n\t"
    "s_mov_b32 s89, 0\n\t"
    "s_mov_b32 s90, 0\n\t"
    "s_mov_b32 s91, 0\n\t"
    "s_cmp_lt_u32 s50, 0x250\n\t"
    "s_cselect_b32 s38, 1, 0\n\t"
    "s_cmp_lt_u32 s50, 0x1b0\n\t"
    "s_cbranch_scc0 L12_%=\n\t"
    "s_mov_b64 s[4:5], s[96:97]\n\t"
    "s_mov_b32 s6, 0x21c000\n\t"
    "s_mul_i32 s88, s50, 0x4be\n\t"
    "s_lshr_b32 s88, s88, 16\n\t"
    "s_mul_i32 s81, s50, 0x1400\n\t"
    "s_add_u32 s81, s81, 0x408000\n\t"
    "s_mul_i32 s77, s50, 0xa000\n\t"
    "s_mul_i32 s53, s88, 0xffde40a0\n\t"
    "s_add_u32 s77, s77, s53\n\t"
    "s_branch L13_%=\n\t"
    "L12_%=:\n\t"
    "s_mov_b64 s[4:5], s[92:93]\n\t"
    "s_mov_b32 s6, 0xc8000\n\t"
    "s_sub_u32 s50, s50, 0x1b0\n\t"
    "s_mul_i32 s81, s50, 0x1400\n\t"
    "s_add_u32 s81, s81, 0x124000\n\t"
    "s_mul_i32 s88, s50, 0xccd\n\t"
    "s_lshr_b32 s88, s88, 16\n\t"
    "s_mul_i32 s53, s88, 0xffffffec\n\t"
    "s_add_u32 s50, s50, s53\n\t"
    "s_mul_i32 s51, s50, 52\n\t"
    "s_lshr_b32 s51, s51, 8\n\t"
    "s_mul_i32 s53, s51, -5\n\t"
    "s_add_u32 s50, s50, s53\n\t"
    "s_mul_i32 s52, s50, 40\n\t"
    "s_cmp_eq_u32 s50, 4\n\t"
    "s_cselect_b32 s52, 16, s52\n\t"
    "s_cselect_b32 s89, 0x17c00, 0\n\t"
    "s_cselect_b32 s90, 0x3c00, 0\n\t"
    "s_cselect_b32 s91, 0x6400, 0\n\t"
    "s_add_u32 s91, s91, 0x1400\n\t"
    "s_mul_i32 s77, s51, 0x32000\n\t"
    "s_mul_i32 s53, s52, 0x500\n\t"
    "s_add_u32 s77, s77, s53\n\t"
    "s_mul_i32 s53, s88, 0xa0\n\t"
    "s_add_u32 s77, s77, s53\n\t"
    "L13_%=:\n\t"
    "s_and_b32 s88, s88, 1\n\t"
    "v_bfe_u32 v30, v244, 2, 1\n\t"
    "v_mad_u32_u24 v249, v30, s89, v92\n\t"
    "v_bfe_u32 v30, v244, 3, 1\n\t"
    "v_mad_u32_u24 v249, v30, s90, v249\n\t"
    "v_bfe_u32 v30, v244, 4, 1\n\t"
    "v_mad_u32_u24 v244, v30, s91, v249\n\t"
    "v_bfe_u32 v30, v245, 2, 1\n\t"
    "v_mad_u32_u24 v249, v30, s89, v93\n\t"
    "v_bfe_u32 v30, v245, 3, 1\n\t"
    "v_mad_u32_u24 v249, v30, s90, v249\n\t"
    "v_bfe_u32 v30, v245, 4, 1\n\t"
    "v_mad_u32_u24 v245, v30, s91, v249\n\t"
    "v_bfe_u32 v30, v246, 2, 1\n\t"
    "v_mad_u32_u24 v249, v30, s89, v94\n\t"
    "v_bfe_u32 v30, v246, 3, 1\n\t"
    "v_mad_u32_u24 v249, v30, s90, v249\n\t"
    "v_bfe_u32 v30, v246, 4, 1\n\t"
    "v_mad_u32_u24 v246, v30, s91, v249\n\t"
    "v_bfe_u32 v30, v247, 2, 1\n\t"
    "v_mad_u32_u24 v249, v30, s89, v95\n\t"
    "v_bfe_u32 v30, v247, 3, 1\n\t"
    "v_mad_u32_u24 v249, v30, s90, v249\n\t"
    "v_bfe_u32 v30, v247, 4, 1\n\t"
    "v_mad_u32_u24 v247, v30, s91, v249\n\t"
    "v_bfe_u32 v30, v248, 2, 1\n\t"
    "v_mad_u32_u24 v249, v30, s89, v96\n\t"
    "v_bfe_u32 v30, v248, 3, 1\n\t"
    "v_mad_u32_u24 v249, v30, s90, v249\n\t"
    "v_bfe_u32 v30, v248, 4, 1\n\t"
    "v_mad_u32_u24 v248, v30, s91, v249\n\t"
    "s_mul_i32 s6, s6, s38\n\t"
    "s_movk_i32 s89, 0x800\n\t"
    "s_mul_i32 s50, s88, 0xffffff80\n\t"
    "s_mul_i32 s53, s33, 0x1800\n\t"
    "s_add_u32 s50, s50, s53\n\t"
    "s_add_u32 s50, s50, 0x22080\n\t"
    "v_add_u32 v252, s50, v252\n\t"
    "v_add_u32 v253, 0x280, v252\n\t"
    "s_mul_i32 s50, s88, 32\n\t"
    "s_mul_i32 s53, s33, 0x1800\n\t"
    "s_add_u32 s50, s50, s53\n\t"
    "s_add_u32 s50, s50, 0x22000\n\t"
    "v_and_b32 v250, 3, v0\n\t"
    "v_lshlrev_b32 v250, 4, v250\n\t"
    "v_bfe_u32 v30, v0, 2, 2\n\t"
    "v_mul_u32_u24 v30, 0xa0, v30\n\t"
    "v_add_u32 v250, v250, v30\n\t"
    "v_lshrrev_b32 v30, 4, v0\n\t"
    "v_mul_u32_u24 v30, 0x500, v30\n\t"
    "v_add_u32 v250, v250, v30\n\t"
    "v_add_u32 v250, s50, v250\n\t"
    "v_add_u32 v251, 0x280, v250\n\t"
    "s_branch L14_%=\n\t"
    "L11_%=:\n\t"
    "s_mov_b64 s[4:5], s[94:95]\n\t"
    "s_mov_b32 s6, 0x21c000\n\t"
    "s_mov_b32 s38, 2\n\t"
    "s_add_u32 s50, s50, 0x168\n\t"
    "s_mul_i32 s51, s50, 0xe39\n\t"
    "s_lshr_b32 s51, s51, 16\n\t"
    "s_mul_i32 s53, s51, 0xffffffee\n\t"
    "s_add_u32 s50, s50, s53\n\t"
    "s_mul_i32 s77, s51, 0x1b000\n\t"
    "s_mul_i32 s53, s50, 0xc0\n\t"
    "s_add_u32 s77, s77, s53\n\t"
    "s_mul_i32 s81, s50, 0x1e000\n\t"
    "s_mul_i32 s53, s51, 0x800\n\t"
    "s_add_u32 s81, s81, s53\n\t"
    "s_add_u32 s81, s81, 0x1ec000\n\t"
    "s_mov_b32 s89, 0xa000\n\t"
    "s_movk_i32 s50, 0xcc0\n\t"
    "v_add_u32 v30, 0, v0\n\t"
    "v_mul_u32_u24 v30, 0x1556, v30\n\t"
    "v_lshrrev_b32 v30, 16, v30\n\t"
    "v_mad_u32_u24 v244, v30, s50, v1\n\t"
    "v_add_u32 v30, 64, v0\n\t"
    "v_mul_u32_u24 v30, 0x1556, v30\n\t"
    "v_lshrrev_b32 v30, 16, v30\n\t"
    "v_mad_u32_u24 v245, v30, s50, v1\n\t"
    "v_add_u32 v30, 0x80, v0\n\t"
    "v_mul_u32_u24 v30, 0x1556, v30\n\t"
    "v_lshrrev_b32 v30, 16, v30\n\t"
    "v_mad_u32_u24 v246, v30, s50, v1\n\t"
    "v_add_u32 v30, 0xc0, v0\n\t"
    "v_mul_u32_u24 v30, 0x1556, v30\n\t"
    "v_lshrrev_b32 v30, 16, v30\n\t"
    "v_mad_u32_u24 v247, v30, s50, v1\n\t"
    "v_add_u32 v30, 0x100, v0\n\t"
    "v_mul_u32_u24 v30, 0x1556, v30\n\t"
    "v_lshrrev_b32 v30, 16, v30\n\t"
    "v_mad_u32_u24 v248, v30, s50, v1\n\t"
    "v_add_u32 v30, 0x140, v0\n\t"
    "v_mul_u32_u24 v30, 0x1556, v30\n\t"
    "v_lshrrev_b32 v30, 16, v30\n\t"
    "v_mad_u32_u24 v249, v30, s50, v1\n\t"
    "s_mul_i32 s50, s33, 0x1800\n\t"
    "s_add_u32 s50, s50, 0x22000\n\t"
    "v_and_b32 v250, 3, v0\n\t"
    "v_lshlrev_b32 v250, 4, v250\n\t"
    "v_bfe_u32 v30, v0, 2, 2\n\t"
    "v_mul_u32_u24 v30, 0xc0, v30\n\t"
    "v_add_u32 v250, v250, v30\n\t"
    "v_lshrrev_b32 v30, 4, v0\n\t"
    "v_mul_u32_u24 v30, 0x600, v30\n\t"
    "v_add_u32 v250, v250, v30\n\t"
    "v_add_u32 v250, s50, v250\n\t"
    "v_add_u32 v251, 0x300, v250\n\t"
    "v_add_u32 v252, 0x80, v250\n\t"
    "v_add_u32 v253, 0x380, v250\n\t"
    "L14_%=:\n\t"
    "s_and_b32 s5, s5, 0xffff\n\t"
    "s_mov_b64 s[92:93], s[12:13]\n\t"
    "s_mov_b32 s95, 0x20000\n\t"
    "s_cmp_eq_u32 s38, 0\n\t"
    "s_cselect_b32 s94, 0, 0x215e000\n\t"
    "v_and_b32 v254, 15, v0\n\t"
    "v_mul_u32_u24 v254, 0x500, v254\n\t"
    "v_lshrrev_b32 v30, 4, v0\n\t"
    "v_lshl_add_u32 v254, v30, 4, v254\n\t"
    "v_and_b32 v255, 15, v0\n\t"
    "v_mul_u32_u24 v255, 0x500, v255\n\t"
    "v_lshrrev_b32 v30, 4, v0\n\t"
    "v_lshl_add_u32 v255, v30, 3, v255\n\t"
    "s_mul_i32 s90, s35, 0x14000\n\t"
    "s_mul_i32 s53, s33, 0x5000\n\t"
    "s_add_u32 s90, s90, s53\n\t"
    "s_add_u32 s91, s90, s71\n\t"
    "s_add_u32 s90, s90, s70\n\t"
    "s_waitcnt vmcnt(10)\n\t"
    "ds_read_b64_tr_b16 a[120:121], v88 offset:5120\n\t"
    "ds_read_b64_tr_b16 a[122:123], v88 offset:5760\n\t"
    "ds_read_b64_tr_b16 a[124:125], v88 offset:5160\n\t"
    "ds_read_b64_tr_b16 a[126:127], v88 offset:5800\n\t"
    "ds_read_b64_tr_b16 a[160:161], v88 offset:5200\n\t"
    "ds_read_b64_tr_b16 a[162:163], v88 offset:5840\n\t"
    "ds_read_b64_tr_b16 a[164:165], v88 offset:5240\n\t"
    "ds_read_b64_tr_b16 a[166:167], v88 offset:5880\n\t"
    "ds_read_b64_tr_b16 a[168:169], v89 offset:5120\n\t"
    "ds_read_b64_tr_b16 a[170:171], v89 offset:5760\n\t"
    "s_waitcnt lgkmcnt(8)\n\t"
    "v_mfma_f32_16x16x32_bf16 a[40:43], a[120:123], v[72:75], a[40:43]\n\t"
    "s_add_u32 m0, s64, 0x3c00\n\t"
    "s_add_u32 s60, s60, 0x9000\n\t"
    "buffer_load_dwordx4 v92, s[20:23], s60 offen lds\n\t"
    "s_waitcnt lgkmcnt(6)\n\t"
    "v_mfma_f32_16x16x32_bf16 a[44:47], a[124:127], v[72:75], a[44:47]\n\t"
    "s_waitcnt lgkmcnt(4)\n\t"
    "v_mfma_f32_16x16x32_bf16 a[48:51], a[160:163], v[72:75], a[48:51]\n\t"
    "buffer_load_dwordx4 v93, s[20:23], s60 offen offset:1024 lds\n\t"
    "s_waitcnt lgkmcnt(2)\n\t"
    "v_mfma_f32_16x16x32_bf16 a[52:55], a[164:167], v[72:75], a[52:55]\n\t"
    "s_waitcnt lgkmcnt(0)\n\t"
    "v_mfma_f32_16x16x32_bf16 a[56:59], a[168:171], v[72:75], a[56:59]\n\t"
    "buffer_load_dwordx4 v94, s[20:23], s60 offen offset:2048 lds\n\t"
    "v_mfma_f32_16x16x32_bf16 a[60:63], a[120:123], v[100:103], a[60:63]\n\t"
    "v_mfma_f32_16x16x32_bf16 a[64:67], a[124:127], v[100:103], a[64:67]\n\t"
    "buffer_load_dwordx4 v95, s[20:23], s60 offen offset:3072 lds\n\t"
    "v_mfma_f32_16x16x32_bf16 a[68:71], a[160:163], v[100:103], a[68:71]\n\t"
    "v_mfma_f32_16x16x32_bf16 a[72:75], a[164:167], v[100:103], a[72:75]\n\t"
    "s_add_u32 m0, m0, 0x1000\n\t"
    "s_add_u32 s60, s60, 0x1000\n\t"
    "buffer_load_dwordx4 v96, s[20:23], s60 offen lds\n\t"
    "v_mfma_f32_16x16x32_bf16 a[76:79], a[168:171], v[100:103], a[76:79]\n\t"
    "v_mfma_f32_16x16x32_bf16 a[140:143], a[120:123], a[128:131], a[140:143]\n\t"
    "v_mfma_f32_16x16x32_bf16 a[144:147], a[124:127], a[128:131], a[144:147]\n\t"
    "v_mfma_f32_16x16x32_bf16 a[148:151], a[160:163], a[128:131], a[148:151]\n\t"
    "v_mfma_f32_16x16x32_bf16 a[152:155], a[164:167], a[128:131], a[152:155]\n\t"
    "v_mfma_f32_16x16x32_bf16 a[156:159], a[168:171], a[128:131], a[156:159]\n\t"
    "s_mov_b64 exec, 0\n\t"
    "s_cmp_eq_u32 s33, 0\n\t"
    "s_cbranch_scc1 pad10_%=\n\t"
    "s_cmp_eq_u32 s33, 1\n\t"
    "s_cbranch_scc1 pad11_%=\n\t"
    "s_cmp_eq_u32 s33, 2\n\t"
    "s_cbranch_scc1 pad12_%=\n\t"
    "s_cmp_eq_u32 s33, 3\n\t"
    "s_cbranch_scc1 pad13_%=\n\t"
    "vb15_%=:\n\t"
    "s_mov_b64 exec, -1\n\t"
    "s_waitcnt vmcnt(10)\n\t"
    "ds_read_b128 a[128:131], v90 offset:7168\n\t"
    "ds_read_b128 a[172:175], v90 offset:7232\n\t"
    "ds_read_b128 a[176:179], v90 offset:7296\n\t"
    "ds_read_b128 a[180:183], v90 offset:7360\n\t"
    "ds_read_b128 a[184:187], v90 offset:7424\n\t"
    "s_waitcnt lgkmcnt(4)\n\t"
    "v_mfma_f32_16x16x32_bf16 a[188:191], a[80:83], a[128:131], 0\n\t"
    "s_add_u32 m0, s65, 0x3000\n\t"
    "s_nop 0\n\t"
    "buffer_load_dwordx4 v36, s[16:19], s57 offen lds\n\t"
    "v_mfma_f32_16x16x32_bf16 a[192:195], a[84:87], a[128:131], 0\n\t"
    "v_mfma_f32_16x16x32_bf16 a[196:199], a[88:91], a[128:131], 0\n\t"
    "buffer_load_dwordx4 v37, s[16:19], s57 offen offset:1024 lds\n\t"
    "v_mfma_f32_16x16x32_bf16 a[200:203], a[92:95], a[128:131], 0\n\t"
    "v_mfma_f32_16x16x32_bf16 a[204:207], a[96:99], a[128:131], 0\n\t"
    "buffer_load_dwordx4 v38, s[16:19], s57 offen offset:2048 lds\n\t"
    "s_waitcnt lgkmcnt(3)\n\t"
    "v_mfma_f32_16x16x32_bf16 a[188:191], a[100:103], a[172:175], a[188:191]\n\t"
    "v_mfma_f32_16x16x32_bf16 a[192:195], a[104:107], a[172:175], a[192:195]\n\t"
    "buffer_load_dwordx4 v39, s[16:19], s57 offen offset:3072 lds\n\t"
    "v_mfma_f32_16x16x32_bf16 a[196:199], a[108:111], a[172:175], a[196:199]\n\t"
    "v_mfma_f32_16x16x32_bf16 a[200:203], a[112:115], a[172:175], a[200:203]\n\t"
    "s_add_u32 m0, m0, 0x1000\n\t"
    "s_add_u32 s57, s57, 0x1000\n\t"
    "buffer_load_dwordx4 v40, s[16:19], s57 offen lds\n\t"
    "v_mfma_f32_16x16x32_bf16 a[204:207], a[116:119], a[172:175], a[204:207]\n\t"
    "s_waitcnt lgkmcnt(2)\n\t"
    "v_mfma_f32_16x16x32_bf16 a[188:191], a[120:123], a[176:179], a[188:191]\n\t"
    "v_mfma_f32_16x16x32_bf16 a[192:195], a[124:127], a[176:179], a[192:195]\n\t"
    "v_mfma_f32_16x16x32_bf16 a[196:199], a[160:163], a[176:179], a[196:199]\n\t"
    "v_mfma_f32_16x16x32_bf16 a[200:203], a[164:167], a[176:179], a[200:203]\n\t"
    "v_mfma_f32_16x16x32_bf16 a[204:207], a[168:171], a[176:179], a[204:207]\n\t"
    "s_mul_i32 s51, s58, 0x80\n\t"
    "s_add_u32 s51, s51, 0x27080\n\t"
    "s_mul_i32 s50, s33, 0x400\n\t"
    "s_add_u32 s50, s50, s51\n\t"
    "v_lshrrev_b32 v91, 4, v0\n\t"
    "v_lshlrev_b32 v91, 4, v91\n\t"
    "v_add_u32 v91, s50, v91\n\t"
    "s_mul_i32 s50, s33, 64\n\t"
    "s_add_u32 s50, s50, s51\n\t"
    "v_lshrrev_b32 v52, 5, v0\n\t"
    "v_lshlrev_b32 v52, 5, v52\n\t"
    "v_add_u32 v52, s50, v52\n\t"
    "s_mul_i32 s50, s33, 16\n\t"
    "s_add_u32 s50, s50, s51\n\t"
    "s_add_u32 s50, s50, 0x100\n\t"
    "v_mov_b32 v53, s50\n\t"
    "s_waitcnt vmcnt(10)\n\t"
    "ds_read_b64_tr_b16 a[128:129], v88 offset:10240\n\t"
    "ds_read_b64_tr_b16 a[130:131], v88 offset:10880\n\t"
    "ds_read_b64_tr_b16 a[172:173], v88 offset:10280\n\t"
    "ds_read_b64_tr_b16 a[174:175], v88 offset:10920\n\t"
    "ds_read_b64_tr_b16 a[176:177], v88 offset:10320\n\t"
    "ds_read_b64_tr_b16 a[178:179], v88 offset:10960\n\t"
    "ds_read_b64_tr_b16 a[208:209], v88 offset:10360\n\t"
    "ds_read_b64_tr_b16 a[210:211], v88 offset:11000\n\t"
    "ds_read_b64_tr_b16 a[212:213], v89 offset:10240\n\t"
    "ds_read_b64_tr_b16 a[214:215], v89 offset:10880\n\t"
    "s_waitcnt lgkmcnt(8)\n\t"
    "v_mfma_f32_16x16x32_bf16 a[40:43], a[128:131], v[76:79], a[40:43]\n\t"
    "s_and_b32 s50, s39, 7\n\t"
    "s_mul_i32 s60, s36, 0x4000\n\t"
    "s_waitcnt lgkmcnt(6)\n\t"
    "v_mfma_f32_16x16x32_bf16 a[44:47], a[172:175], v[76:79], a[44:47]\n\t"
    "s_mul_i32 s53, s50, 0x800\n\t"
    "s_add_u32 s60, s60, s53\n\t"
    "s_waitcnt lgkmcnt(4)\n\t"
    "v_mfma_f32_16x16x32_bf16 a[48:51], a[176:179], v[76:79], a[48:51]\n\t"
    "s_add_u32 s60, s60, 0x10000\n\t"
    "s_waitcnt lgkmcnt(2)\n\t"
    "v_mfma_f32_16x16x32_bf16 a[52:55], a[208:211], v[76:79], a[52:55]\n\t"
    "s_waitcnt lgkmcnt(0)\n\t"
    "v_mfma_f32_16x16x32_bf16 a[56:59], a[212:215], v[76:79], a[56:59]\n\t"
    "v_mfma_f32_16x16x32_bf16 a[60:63], a[128:131], v[100:103], a[60:63]\n\t"
    "v_mfma_f32_16x16x32_bf16 a[64:67], a[172:175], v[100:103], a[64:67]\n\t"
    "v_mfma_f32_16x16x32_bf16 a[68:71], a[176:179], v[100:103], a[68:71]\n\t"
    "v_mfma_f32_16x16x32_bf16 a[72:75], a[208:211], v[100:103], a[72:75]\n\t"
    "v_mfma_f32_16x16x32_bf16 a[76:79], a[212:215], v[100:103], a[76:79]\n\t"
    "v_mfma_f32_16x16x32_bf16 a[140:143], a[128:131], a[132:135], a[140:143]\n\t"
    "v_mfma_f32_16x16x32_bf16 a[144:147], a[172:175], a[132:135], a[144:147]\n\t"
    "v_mfma_f32_16x16x32_bf16 a[148:151], a[176:179], a[132:135], a[148:151]\n\t"
    "v_mfma_f32_16x16x32_bf16 a[152:155], a[208:211], a[132:135], a[152:155]\n\t"
    "v_mfma_f32_16x16x32_bf16 a[156:159], a[212:215], a[132:135], a[156:159]\n\t"
    "v_mfma_f32_16x16x32_bf16 a[188:191], a[128:131], a[180:183], a[188:191]\n\t"
    "v_mfma_f32_16x16x32_bf16 a[192:195], a[172:175], a[180:183], a[192:195]\n\t"
    "v_mfma_f32_16x16x32_bf16 a[196:199], a[176:179], a[180:183], a[196:199]\n\t"
    "v_mfma_f32_16x16x32_bf16 a[200:203], a[208:211], a[180:183], a[200:203]\n\t"
    "v_mfma_f32_16x16x32_bf16 a[204:207], a[212:215], a[180:183], a[204:207]\n\t"
    "s_waitcnt vmcnt(5)\n\t"
    "ds_read_b64_tr_b16 a[132:133], v88 offset:15360\n\t"
    "ds_read_b64_tr_b16 a[134:135], v88 offset:16000\n\t"
    "ds_read_b64_tr_b16 a[180:181], v88 offset:15400\n\t"
    "ds_read_b64_tr_b16 a[182:183], v88 offset:16040\n\t"
    "ds_read_b64_tr_b16 a[216:217], v88 offset:15440\n\t"
    "ds_read_b64_tr_b16 a[218:219], v88 offset:16080\n\t"
    "ds_read_b64_tr_b16 a[220:221], v88 offset:15480\n\t"
    "ds_read_b64_tr_b16 a[222:223], v88 offset:16120\n\t"
    "ds_read_b64_tr_b16 a[224:225], v89 offset:15360\n\t"
    "ds_read_b64_tr_b16 a[226:227], v89 offset:16000\n\t"
    "s_waitcnt lgkmcnt(8)\n\t"
    "v_mfma_f32_16x16x32_bf16 a[60:63], a[132:135], v[100:103], a[60:63]\n\t"
    "s_waitcnt lgkmcnt(6)\n\t"
    "v_mfma_f32_16x16x32_bf16 a[64:67], a[180:183], v[100:103], a[64:67]\n\t"
    "s_waitcnt lgkmcnt(4)\n\t"
    "v_mfma_f32_16x16x32_bf16 a[68:71], a[216:219], v[100:103], a[68:71]\n\t"
    "s_waitcnt lgkmcnt(2)\n\t"
    "v_mfma_f32_16x16x32_bf16 a[72:75], a[220:223], v[100:103], a[72:75]\n\t"
    "s_waitcnt lgkmcnt(0)\n\t"
    "v_mfma_f32_16x16x32_bf16 a[76:79], a[224:227], v[100:103], a[76:79]\n\t"
    "v_mfma_f32_16x16x32_bf16 a[40:43], a[132:135], v[80:83], a[40:43]\n\t"
    "v_mfma_f32_16x16x32_bf16 a[44:47], a[180:183], v[80:83], a[44:47]\n\t"
    "v_mfma_f32_16x16x32_bf16 a[48:51], a[216:219], v[80:83], a[48:51]\n\t"
    "s_mov_b32 exec_lo, 0x10001\n\t"
    "s_mov_b32 exec_hi, 0x10001\n\t"
    "ds_write_b128 v91, a[60:63]\n\t"
    "ds_write_b128 v91, a[64:67] offset:64\n\t"
    "ds_write_b128 v91, a[68:71] offset:128\n\t"
    "ds_write_b128 v91, a[72:75] offset:192\n\t"
    "ds_write_b128 v91, a[76:79] offset:256\n\t"
    "s_mov_b64 exec, -1\n\t"
    "v_mfma_f32_16x16x32_bf16 a[52:55], a[220:223], v[80:83], a[52:55]\n\t"
    "v_mfma_f32_16x16x32_bf16 a[56:59], a[224:227], v[80:83], a[56:59]\n\t"
    "v_mfma_f32_16x16x32_bf16 a[140:143], a[132:135], a[136:139], a[140:143]\n\t"
    "ds_write_b128 v31, a[40:43] offset:15360\n\t"
    "v_mfma_f32_16x16x32_bf16 a[144:147], a[180:183], a[136:139], a[144:147]\n\t"
    "v_mfma_f32_16x16x32_bf16 a[148:151], a[216:219], a[136:139], a[148:151]\n\t"
    "ds_write_b128 v31, a[44:47] offset:16384\n\t"
    "v_mfma_f32_16x16x32_bf16 a[152:155], a[220:223], a[136:139], a[152:155]\n\t"
    "v_mfma_f32_16x16x32_bf16 a[156:159], a[224:227], a[136:139], a[156:159]\n\t"
    "ds_write_b128 v31, a[48:51] offset:17408\n\t"
    "v_mfma_f32_16x16x32_bf16 a[188:191], a[132:135], a[184:187], a[188:191]\n\t"
    "v_mfma_f32_16x16x32_bf16 a[192:195], a[180:183], a[184:187], a[192:195]\n\t"
    "s_waitcnt vmcnt(0)\n\t"
    "ds_read_b128 a[228:231], v90 offset:12288\n\t"
    "v_mfma_f32_16x16x32_bf16 a[196:199], a[216:219], a[184:187], a[196:199]\n\t"
    "ds_read_b128 a[232:235], v90 offset:12352\n\t"
    "ds_read_b128 a[236:239], v90 offset:12416\n\t"
    "v_mfma_f32_16x16x32_bf16 a[200:203], a[220:223], a[184:187], a[200:203]\n\t"
    "ds_read_b128 a[240:243], v90 offset:12480\n\t"
    "ds_read_b128 a[244:247], v90 offset:12544\n\t"
    "v_mfma_f32_16x16x32_bf16 a[204:207], a[224:227], a[184:187], a[204:207]\n\t"
    "s_cmp_eq_u32 s33, 0\n\t"
    "s_cbranch_scc0 L16_%=\n\t"
    "buffer_load_dwordx4 v[44:47], v1, s[12:15], s60 offen sc1\n\t"
    "buffer_load_dwordx4 v[48:51], v1, s[12:15], s60 offen offset:1024 sc1\n\t"
    "L16_%=:\n\t"
    "s_waitcnt lgkmcnt(4)\n\t"
    "v_mfma_f32_16x16x32_bf16 a[60:63], a[80:83], a[228:231], 0\n\t"
    "ds_write_b128 v31, a[52:55] offset:18432\n\t"
    "v_mfma_f32_16x16x32_bf16 a[64:67], a[84:87], a[228:231], 0\n\t"
    "buffer_load_dwordx4 v[4:7], v254, s[16:19], s90 offen\n\t"
    "v_mfma_f32_16x16x32_bf16 a[68:71], a[88:91], a[228:231], 0\n\t"
    "ds_write_b128 v31, a[56:59] offset:19456\n\t"
    "v_mfma_f32_16x16x32_bf16 a[72:75], a[92:95], a[228:231], 0\n\t"
    "v_mfma_f32_16x16x32_bf16 a[76:79], a[96:99], a[228:231], 0\n\t"
    "ds_write_b128 v31, a[140:143]\n\t"
    "s_waitcnt lgkmcnt(6)\n\t"
    "v_mfma_f32_16x16x32_bf16 a[60:63], a[100:103], a[232:235], a[60:63]\n\t"
    "v_mfma_f32_16x16x32_bf16 a[64:67], a[104:107], a[232:235], a[64:67]\n\t"
    "ds_write_b128 v31, a[144:147] offset:1024\n\t"
    "v_mfma_f32_16x16x32_bf16 a[68:71], a[108:111], a[232:235], a[68:71]\n\t"
    "buffer_load_dwordx4 v[8:11], v254, s[16:19], s90 offen offset:64\n\t"
    "v_mfma_f32_16x16x32_bf16 a[72:75], a[112:115], a[232:235], a[72:75]\n\t"
    "ds_write_b128 v31, a[148:151] offset:2048\n\t"
    "v_mfma_f32_16x16x32_bf16 a[76:79], a[116:119], a[232:235], a[76:79]\n\t"
    "s_waitcnt lgkmcnt(7)\n\t"
    "v_mfma_f32_16x16x32_bf16 a[60:63], a[120:123], a[236:239], a[60:63]\n\t"
    "ds_write_b128 v31, a[152:155] offset:3072\n\t"
    "v_mfma_f32_16x16x32_bf16 a[64:67], a[124:127], a[236:239], a[64:67]\n\t"
    "v_mfma_f32_16x16x32_bf16 a[68:71], a[160:163], a[236:239], a[68:71]\n\t"
    "ds_write_b128 v31, a[156:159] offset:4096\n\t"
    "v_mfma_f32_16x16x32_bf16 a[72:75], a[164:167], a[236:239], a[72:75]\n\t"
    "buffer_load_dwordx2 v[12:13], v255, s[16:19], s91 offen\n\t"
    "v_mfma_f32_16x16x32_bf16 a[76:79], a[168:171], a[236:239], a[76:79]\n\t"
    "ds_write_b128 v31, a[188:191] offset:5120\n\t"
    "s_waitcnt lgkmcnt(9)\n\t"
    "v_mfma_f32_16x16x32_bf16 a[60:63], a[128:131], a[240:243], a[60:63]\n\t"
    "v_mfma_f32_16x16x32_bf16 a[64:67], a[172:175], a[240:243], a[64:67]\n\t"
    "ds_write_b128 v31, a[192:195] offset:6144\n\t"
    "v_mfma_f32_16x16x32_bf16 a[68:71], a[176:179], a[240:243], a[68:71]\n\t"
    "v_mfma_f32_16x16x32_bf16 a[72:75], a[208:211], a[240:243], a[72:75]\n\t"
    "ds_write_b128 v31, a[196:199] offset:7168\n\t"
    "pad2_%=:\n\t"
    "s_cbranch_execz vb8_%=\n\t"
    "v_mfma_f32_16x16x32_bf16 a[76:79], a[212:215], a[240:243], a[76:79]\n\t"
    "s_waitcnt lgkmcnt(10)\n\t"
    "v_mfma_f32_16x16x32_bf16 a[60:63], a[132:135], a[244:247], a[60:63]\n\t"
    "ds_write_b128 v31, a[200:203] offset:8192\n\t"
    "v_mfma_f32_16x16x32_bf16 a[64:67], a[180:183], a[244:247], a[64:67]\n\t"
    "v_mfma_f32_16x16x32_bf16 a[68:71], a[216:219], a[244:247], a[68:71]\n\t"
    "ds_write_b128 v31, a[204:207] offset:9216\n\t"
    "v_mfma_f32_16x16x32_bf16 a[72:75], a[220:223], a[244:247], a[72:75]\n\t"
    "v_mfma_f32_16x16x32_bf16 a[76:79], a[224:227], a[244:247], a[76:79]\n\t"
    "s_mul_i32 s55, s33, 0xc00\n\t"
    "s_add_u32 s55, s55, s45\n\t"
    "s_mul_i32 s54, s33, 0x1000\n\t"
    "s_add_u32 s54, s54, s45\n\t"
    "s_add_u32 s54, s54, 0x3000\n\t"
    "ds_write_b128 v31, a[60:63] offset:10240\n\t"
    "ds_write_b128 v31, a[64:67] offset:11264\n\t"
    "ds_write_b128 v31, a[68:71] offset:12288\n\t"
    "ds_write_b128 v31, a[72:75] offset:13312\n\t"
    "ds_write_b128 v31, a[76:79] offset:14336\n\t"
    "s_mul_i32 m0, s33, 0xc00\n\t"
    "s_mul_i32 s57, s36, 0x4000\n\t"
    "s_mul_i32 s53, s35, 0x200\n\t"
    "s_add_u32 s57, s57, s53\n\t"
    "s_add_u32 s57, s57, 0xe56000\n\t"
    "s_waitcnt lgkmcnt(0)\n\t"
    "s_cmp_eq_u32 s33, 0\n\t"
    "s_cbranch_scc0 L17_%=\n\t"
    "s_waitcnt vmcnt(3)\n\t"
    "v_cmp_ne_u64 vcc, v[2:3], v[44:45]\n\t"
    "v_cmp_ne_u64 s[42:43], v[2:3], v[46:47]\n\t"
    "s_nop 0\n\t"
    "s_or_b64 vcc, vcc, s[42:43]\n\t"
    "v_cmp_ne_u64 s[42:43], v[2:3], v[48:49]\n\t"
    "s_nop 0\n\t"
    "s_or_b64 vcc, vcc, s[42:43]\n\t"
    "v_cmp_ne_u64 s[42:43], v[2:3], v[50:51]\n\t"
    "s_nop 0\n\t"
    "s_or_b64 vcc, vcc, s[42:43]\n\t"
    "s_cbranch_vccz L18_%=\n\t"
    "poll19_%=:\n\t"
    "s_sleep 2\n\t"
    "buffer_load_dwordx4 v[44:47], v1, s[12:15], s60 offen sc1\n\t"
    "buffer_load_dwordx4 v[48:51], v1, s[12:15], s60 offen offset:1024 sc1\n\t"
    "s_waitcnt vmcnt(0)\n\t"
    "v_cmp_ne_u64 vcc, v[2:3], v[44:45]\n\t"
    "v_cmp_ne_u64 s[42:43], v[2:3], v[46:47]\n\t"
    "s_nop 0\n\t"
    "s_or_b64 vcc, vcc, s[42:43]\n\t"
    "v_cmp_ne_u64 s[42:43], v[2:3], v[48:49]\n\t"
    "s_nop 0\n\t"
    "s_or_b64 vcc, vcc, s[42:43]\n\t"
    "v_cmp_ne_u64 s[42:43], v[2:3], v[50:51]\n\t"
    "s_nop 0\n\t"
    "s_or_b64 vcc, vcc, s[42:43]\n\t"
    "s_cbranch_vccz L18_%=\n\t"
    "s_sub_u32 s37, s37, 1\n\t"
    "s_cbranch_scc0 poll19_%=\n\t"
    "s_mov_b32 s37, 0\n\t"
    "L18_%=:\n\t"
    "L17_%=:\n\t"
    "s_barrier\n\t"
    "buffer_load_dwordx2 v[232:233], v63, s[12:15], s57 offen sc1\n\t"
    "ds_read_b128 v[220:223], v52\n\t"
    "ds_read_b128 v[224:227], v52 offset:16\n\t"
    "ds_read_b128 v[228:231], v53\n\t"
    "ds_read_b128 v[104:107], v52 offset:1024\n\t"
    "ds_read_b128 v[108:111], v52 offset:1040\n\t"
    "ds_read_b128 v[112:115], v53 offset:1024\n\t"
    "buffer_load_dwordx4 v1, s[12:15], s55 offen sc1 lds\n\t"
    "ds_read_b128 v[116:119], v52 offset:2048\n\t"
    "ds_read_b128 v[120:123], v52 offset:2064\n\t"
    "ds_read_b128 v[160:163], v53 offset:2048\n\t"
    "ds_read_b128 v[164:167], v52 offset:3072\n\t"
    "ds_read_b128 v[168:171], v52 offset:3088\n\t"
    "ds_read_b128 v[172:175], v53 offset:3072\n\t"
    "buffer_load_dwordx4 v1, s[12:15], s55 offen offset:1024 sc1 lds\n\t"
    "ds_read_b128 v[200:203], v59\n\t"
    "ds_read_b128 v[204:207], v59 offset:256\n\t"
    "ds_read_b128 v[208:211], v60\n\t"
    "buffer_load_dwordx4 v1, s[12:15], s55 offen offset:2048 sc1 lds\n\t"
    "ds_read_b128 v[212:215], v60 offset:256\n\t"
    "ds_read_b128 v[216:219], v61\n\t"
    "ds_read_b128 v[32:35], v59 offset:20480\n\t"
    "s_lshl_b32 m0, s33, 12\n\t"
    "s_add_u32 m0, m0, 0x3000\n\t"
    "s_nop 0\n\t"
    "buffer_load_dwordx4 v1, s[12:15], s54 offen sc1 lds\n\t"
    "ds_read_b128 v[36:39], v59 offset:20736\n\t"
    "ds_read_b128 v[40:43], v60 offset:20480\n\t"
    "ds_read_b128 v[44:47], v60 offset:20736\n\t"
    "buffer_load_dwordx4 v1, s[12:15], s54 offen offset:1024 sc1 lds\n\t"
    "ds_read_b128 v[48:51], v61 offset:20480\n\t"
    "ds_read_b128 v[64:67], v59 offset:40960\n\t"
    "ds_read_b128 v[68:71], v59 offset:41216\n\t"
    "buffer_load_dwordx4 v1, s[12:15], s54 offen offset:2048 sc1 lds\n\t"
    "ds_read_b128 v[72:75], v60 offset:40960\n\t"
    "ds_read_b128 v[76:79], v60 offset:41216\n\t"
    "ds_read_b128 v[80:83], v61 offset:40960\n\t"
    "buffer_load_dwordx4 v1, s[12:15], s54 offen offset:3072 sc1 lds\n\t"
    "ds_read_b128 v[84:87], v59 offset:61440\n\t"
    "ds_read_b128 v[88:91], v59 offset:61696\n\t"
    "ds_read_b128 v[92:95], v60 offset:61440\n\t"
    "s_add_u32 m0, m0, 0x4000\n\t"
    "s_add_u32 s54, s54, 0x4000\n\t"
    "buffer_load_dwordx4 v1, s[12:15], s54 offen sc1 lds\n\t"
    "ds_read_b128 v[96:99], v60 offset:61696\n\t"
    "ds_read_b128 v[100:103], v61 offset:61440\n\t"
    "s_waitcnt lgkmcnt(15)\n\t"
    "v_pk_add_f32 v[220:221], v[220:221], v[104:105]\n\t"
    "buffer_load_dwordx4 v1, s[12:15], s54 offen offset:1024 sc1 lds\n\t"
    "v_pk_add_f32 v[222:223], v[222:223], v[106:107]\n\t"
    "v_pk_add_f32 v[224:225], v[224:225], v[108:109]\n\t"
    "v_pk_add_f32 v[226:227], v[226:227], v[110:111]\n\t"
    "v_pk_add_f32 v[228:229], v[228:229], v[112:113]\n\t"
    "v_pk_add_f32 v[230:231], v[230:231], v[114:115]\n\t"
    "v_pk_add_f32 v[220:221], v[220:221], v[116:117]\n\t"
    "v_pk_add_f32 v[222:223], v[222:223], v[118:119]\n\t"
    "v_pk_add_f32 v[224:225], v[224:225], v[120:121]\n\t"
    "v_pk_add_f32 v[226:227], v[226:227], v[122:123]\n\t"
    "v_pk_add_f32 v[228:229], v[228:229], v[160:161]\n\t"
    "v_pk_add_f32 v[230:231], v[230:231], v[162:163]\n\t"
    "v_pk_add_f32 v[220:221], v[220:221], v[164:165]\n\t"
    "buffer_load_dwordx4 v1, s[12:15], s54 offen offset:2048 sc1 lds\n\t"
    "v_pk_add_f32 v[222:223], v[222:223], v[166:167]\n\t"
    "v_pk_add_f32 v[224:225], v[224:225], v[168:169]\n\t"
    "v_pk_add_f32 v[226:227], v[226:227], v[170:171]\n\t"
    "v_pk_add_f32 v[228:229], v[228:229], v[172:173]\n\t"
    "v_pk_add_f32 v[230:231], v[230:231], v[174:175]\n\t"
    "s_waitcnt lgkmcnt(10)\n\t"
    "v_pk_add_f32 v[200:201], v[200:201], v[32:33]\n\t"
    "v_pk_add_f32 v[202:203], v[202:203], v[34:35]\n\t"
    "v_pk_add_f32 v[204:205], v[204:205], v[36:37]\n\t"
    "v_pk_add_f32 v[206:207], v[206:207], v[38:39]\n\t"
    "v_pk_add_f32 v[208:209], v[208:209], v[40:41]\n\t"
    "v_pk_add_f32 v[210:211], v[210:211], v[42:43]\n\t"
    "buffer_load_dwordx4 v1, s[12:15], s54 offen offset:3072 sc1 lds\n\t"
    "v_pk_add_f32 v[212:213], v[212:213], v[44:45]\n\t"
    "v_pk_add_f32 v[214:215], v[214:215], v[46:47]\n\t"
    "v_pk_add_f32 v[216:217], v[216:217], v[48:49]\n\t"
    "v_pk_add_f32 v[218:219], v[218:219], v[50:51]\n\t"
    "s_waitcnt lgkmcnt(5)\n\t"
    "v_pk_add_f32 v[200:201], v[200:201], v[64:65]\n\t"
    "v_pk_add_f32 v[202:203], v[202:203], v[66:67]\n\t"
    "v_pk_add_f32 v[204:205], v[204:205], v[68:69]\n\t"
    "v_pk_add_f32 v[206:207], v[206:207], v[70:71]\n\t"
    "v_pk_add_f32 v[208:209], v[208:209], v[72:73]\n\t"
    "v_pk_add_f32 v[210:211], v[210:211], v[74:75]\n\t"
    "v_pk_add_f32 v[212:213], v[212:213], v[76:77]\n\t"
    "v_pk_add_f32 v[214:215], v[214:215], v[78:79]\n\t"
    "v_pk_add_f32 v[216:217], v[216:217], v[80:81]\n\t"
    "v_pk_add_f32 v[218:219], v[218:219], v[82:83]\n\t"
    "s_waitcnt lgkmcnt(0)\n\t"
    "v_pk_add_f32 v[200:201], v[200:201], v[84:85]\n\t"
    "v_pk_add_f32 v[202:203], v[202:203], v[86:87]\n\t"
    "v_pk_add_f32 v[204:205], v[204:205], v[88:89]\n\t"
    "v_pk_add_f32 v[206:207], v[206:207], v[90:91]\n\t"
    "v_pk_add_f32 v[208:209], v[208:209], v[92:93]\n\t"
    "v_pk_add_f32 v[210:211], v[210:211], v[94:95]\n\t"
    "v_pk_add_f32 v[212:213], v[212:213], v[96:97]\n\t"
    "v_pk_add_f32 v[214:215], v[214:215], v[98:99]\n\t"
    "v_pk_add_f32 v[216:217], v[216:217], v[100:101]\n\t"
    "v_pk_add_f32 v[218:219], v[218:219], v[102:103]\n\t"
    "s_mov_b64 s[16:17], s[12:13]\n\t"
    "s_mov_b64 s[20:21], s[12:13]\n\t"
    "s_mov_b64 s[8:9], s[12:13]\n\t"
    "s_mov_b32 s10, 0x215e000\n\t"
    "s_lshr_b32 s50, s33, 1\n\t"
    "s_add_u32 s50, s50, s34\n\t"
    "s_cmp_lt_u32 s50, 6\n\t"
    "s_cselect_b32 s18, 0x215e000, 0\n\t"
    "s_cmp_lt_u32 s33, 2\n\t"
    "s_cselect_b32 s22, 0x215e000, 0\n\t"
    "v_lshrrev_b32 v254, 5, v0\n\t"
    "v_lshlrev_b32 v254, 5, v254\n\t"
    "v_mov_b32 v255, 64\n\t"
    "s_mul_i32 s90, s39, 0x200\n\t"
    "s_mul_i32 s53, s33, 0x80\n\t"
    "s_add_u32 s90, s90, s53\n\t"
    "s_add_u32 s90, s90, 0xe8000\n\t"
    "s_waitcnt vmcnt(11)\n\t"
    "v_mul_f32 v234, 0x3ea52b5c, v233\n\t"
    "v_mov_b32 v233, v232\n\t"
    "v_mov_b32 v236, v232\n\t"
    "v_mov_b32 v240, v232\n\t"
    "v_mov_b32 v238, v234\n\t"
    "v_mov_b32 v242, v234\n\t"
    "v_mov_b32 v235, v234\n\t"
    "v_permlane32_swap_b32 v236, v240\n\t"
    "v_permlane32_swap_b32 v238, v242\n\t"
    "v_mov_b32 v237, v236\n\t"
    "v_mov_b32 v241, v240\n\t"
    "v_mov_b32 v239, v238\n\t"
    "v_mov_b32 v243, v242\n\t"
    "s_waitcnt vmcnt(8)\n\t"
    "s_barrier\n\t"
    "ds_read_b128 v[96:99], v125\n\t"
    "ds_read_b64 v[120:121], v126 offset:2048\n\t"
    "ds_read_b128 v[100:103], v125 offset:1024\n\t"
    "ds_read_b128 v[104:107], v127 offset:2560\n\t"
    "ds_read_b128 v[112:115], v127 offset:3232\n\t"
    "v_pk_fma_f32 v[200:201], v[236:237], v[220:221], v[200:201]\n\t"
    "v_pk_fma_f32 v[202:203], v[236:237], v[222:223], v[202:203]\n\t"
    "v_pk_fma_f32 v[204:205], v[236:237], v[224:225], v[204:205]\n\t"
    "v_pk_fma_f32 v[206:207], v[236:237], v[226:227], v[206:207]\n\t"
    "v_pk_fma_f32 v[208:209], v[240:241], v[220:221], v[208:209]\n\t"
    "v_pk_fma_f32 v[210:211], v[240:241], v[222:223], v[210:211]\n\t"
    "v_pk_fma_f32 v[212:213], v[240:241], v[224:225], v[212:213]\n\t"
    "v_pk_fma_f32 v[214:215], v[240:241], v[226:227], v[214:215]\n\t"
    "v_pk_fma_f32 v[216:217], v[232:233], v[228:229], v[216:217]\n\t"
    "v_pk_fma_f32 v[218:219], v[232:233], v[230:231], v[218:219]\n\t"
    "v_pk_mul_f32 v[200:201], v[200:201], v[238:239]\n\t"
    "v_pk_mul_f32 v[202:203], v[202:203], v[238:239]\n\t"
    "v_pk_mul_f32 v[204:205], v[204:205], v[238:239]\n\t"
    "v_pk_mul_f32 v[206:207], v[206:207], v[238:239]\n\t"
    "v_pk_mul_f32 v[208:209], v[208:209], v[242:243]\n\t"
    "v_pk_mul_f32 v[210:211], v[210:211], v[242:243]\n\t"
    "v_pk_mul_f32 v[212:213], v[212:213], v[242:243]\n\t"
    "v_pk_mul_f32 v[214:215], v[214:215], v[242:243]\n\t"
    "v_pk_mul_f32 v[216:217], v[216:217], v[234:235]\n\t"
    "v_pk_mul_f32 v[218:219], v[218:219], v[234:235]\n\t"
    "v_cvt_pk_bf16_f32 v16, v200, v201\n\t"
    "v_cvt_pk_bf16_f32 v17, v202, v203\n\t"
    "v_cvt_pk_bf16_f32 v18, v204, v205\n\t"
    "v_cvt_pk_bf16_f32 v19, v206, v207\n\t"
    "v_cvt_pk_bf16_f32 v20, v208, v209\n\t"
    "v_cvt_pk_bf16_f32 v21, v210, v211\n\t"
    "v_cvt_pk_bf16_f32 v22, v212, v213\n\t"
    "v_cvt_pk_bf16_f32 v23, v214, v215\n\t"
    "v_cvt_pk_bf16_f32 v24, v216, v217\n\t"
    "v_cvt_pk_bf16_f32 v25, v218, v219\n\t"
    "s_waitcnt lgkmcnt(4)\n\t"
    "v_mfma_f32_32x32x16_bf16 v[32:47], v[96:99], v[16:19], 0\n\t"
    "v_mfma_f32_32x32x16_bf16 v[48:63], v[96:99], v[20:23], 0\n\t"
    "s_mov_b32 s57, m0\n\t"
    "s_mul_i32 m0, s33, 0x1800\n\t"
    "s_add_u32 m0, m0, 0x22000\n\t"
    "s_nop 0\n\t"
    "buffer_load_dwordx4 v244, s[4:7], s77 offen lds\n\t"
    "s_waitcnt lgkmcnt(3)\n\t"
    "s_nop 3\n\t"
    "v_mfma_f32_32x32x4_2b_bf16 v[32:63], v[120:121], v[24:25], v[32:63] cbsz:1 abid:0\n\t"
    "buffer_load_dwordx4 v245, s[4:7], s77 offen offset:1024 lds\n\t"
    "s_nop 15\n\t"
    "s_nop 2\n\t"
    "v_exp_f32 v32, v32\n\t"
    "v_exp_f32 v33, v33\n\t"
    "s_waitcnt lgkmcnt(0)\n\t"
    "ds_read_b128 v[96:99], v125 offset:5248\n\t"
    "v_exp_f32 v34, v34\n\t"
    "v_exp_f32 v35, v35\n\t"
    "v_exp_f32 v36, v36\n\t"
    "v_exp_f32 v37, v37\n\t"
    "v_mfma_f32_32x32x4_2b_bf16 v[64:95], v[120:121], v[24:25], 0 cbsz:1 abid:1\n\t"
    "ds_read_b128 v[108:111], v127 offset:3904\n\t"
    "v_exp_f32 v38, v38\n\t"
    "v_exp_f32 v39, v39\n\t"
    "v_exp_f32 v40, v40\n\t"
    "v_exp_f32 v41, v41\n\t"
    "v_exp_f32 v42, v42\n\t"
    "v_exp_f32 v43, v43\n\t"
    "v_exp_f32 v44, v44\n\t"
    "v_exp_f32 v45, v45\n\t"
    "v_smfmac_f32_32x32x32_bf16 v[128:143], v[104:107], v[32:39], v124\n\t"
    "ds_read_b128 v[116:119], v127 offset:4576\n\t"
    "buffer_load_dwordx4 v246, s[4:7], s77 offen offset:2048 lds\n\t"
    "v_exp_f32 v46, v46\n\t"
    "v_exp_f32 v47, v47\n\t"
    "v_exp_f32 v48, v48\n\t"
    "v_exp_f32 v49, v49\n\t"
    "v_smfmac_f32_32x32x32_bf16 v[128:143], v[112:115], v[40:47], v124\n\t"
    "ds_read_b64 v[122:123], v126 offset:7296\n\t"
    "v_exp_f32 v50, v50\n\t"
    "v_exp_f32 v51, v51\n\t"
    "v_exp_f32 v52, v52\n\t"
    "v_exp_f32 v53, v53\n\t"
    "v_mfma_f32_32x32x16_bf16 v[64:79], v[100:103], v[16:19], v[64:79]\n\t"
    "buffer_load_dwordx4 v247, s[4:7], s77 offen offset:3072 lds\n\t"
    "v_exp_f32 v54, v54\n\t"
    "v_exp_f32 v55, v55\n\t"
    "v_exp_f32 v56, v56\n\t"
    "v_exp_f32 v57, v57\n\t"
    "v_smfmac_f32_32x32x32_bf16 v[144:159], v[104:107], v[48:55], v124\n\t"
    "v_exp_f32 v58, v58\n\t"
    "v_exp_f32 v59, v59\n\t"
    "v_exp_f32 v60, v60\n\t"
    "v_exp_f32 v61, v61\n\t"
    "v_mfma_f32_32x32x16_bf16 v[80:95], v[100:103], v[20:23], v[80:95]\n\t"
    "s_add_u32 m0, m0, 0x1000\n\t"
    "s_add_u32 s77, s77, 0x1000\n\t"
    "buffer_load_dwordx4 v248, s[4:7], s77 offen lds\n\t"
    "v_exp_f32 v62, v62\n\t"
    "v_exp_f32 v63, v63\n\t"
    "v_exp_f32 v64, v64\n\t"
    "v_exp_f32 v65, v65\n\t"
    "v_smfmac_f32_32x32x32_bf16 v[144:159], v[112:115], v[56:63], v124\n\t"
    "s_waitcnt lgkmcnt(0)\n\t"
    "ds_read_b128 v[100:103], v125 offset:6272\n\t"
    "v_exp_f32 v66, v66\n\t"
    "v_exp_f32 v67, v67\n\t"
    "v_exp_f32 v68, v68\n\t"
    "v_exp_f32 v69, v69\n\t"
    "v_mfma_f32_32x32x4_2b_bf16 v[32:63], v[122:123], v[24:25], 0 cbsz:1 abid:0\n\t"
    "ds_read_b128 v[104:107], v127 offset:7808\n\t"
    "v_exp_f32 v70, v70\n\t"
    "v_exp_f32 v71, v71\n\t"
    "v_exp_f32 v72, v72\n\t"
    "v_exp_f32 v73, v73\n\t"
    "v_exp_f32 v74, v74\n\t"
    "v_exp_f32 v75, v75\n\t"
    "v_exp_f32 v76, v76\n\t"
    "v_exp_f32 v77, v77\n\t"
    "v_smfmac_f32_32x32x32_bf16 v[128:143], v[108:111], v[64:71], v124\n\t"
    "ds_read_b128 v[112:115], v127 offset:8480\n\t"
    "s_cmp_eq_u32 s38, 2\n\t"
    "s_cselect_b32 s6, s6, 0\n\t"
    "buffer_load_dwordx4 v249, s[4:7], s77 offen offset:1024 lds\n\t"
    "s_mov_b32 m0, s57\n\t"
    "s_mov_b32 s6, 0\n\t"
    "v_exp_f32 v78, v78\n\t"
    "v_exp_f32 v79, v79\n\t"
    "v_exp_f32 v80, v80\n\t"
    "v_exp_f32 v81, v81\n\t"
    "v_smfmac_f32_32x32x32_bf16 v[128:143], v[116:119], v[72:79], v124\n\t"
    "v_exp_f32 v82, v82\n\t"
    "v_exp_f32 v83, v83\n\t"
    "v_exp_f32 v84, v84\n\t"
    "v_exp_f32 v85, v85\n\t"
    "v_mfma_f32_32x32x16_bf16 v[32:47], v[96:99], v[16:19], v[32:47]\n\t"
    "s_mul_i32 s50, s35, 4\n\t"
    "s_add_u32 s50, s50, s58\n\t"
    "s_add_u32 s50, s50, 0xffffffb5\n\t"
    "s_min_u32 s50, s50, 35\n\t"
    "s_and_b32 s51, s34, 1\n\t"
    "s_mul_i32 s51, s51, 0xa000\n\t"
    "s_mul_i32 s53, s33, 0x2800\n\t"
    "s_add_u32 s51, s51, s53\n\t"
    "s_mul_i32 s53, s50, 0x400\n\t"
    "s_add_u32 s51, s51, s53\n\t"
    "s_add_u32 s51, s51, 0x101c00\n\t"
    "s_mul_i32 s52, s34, 0x19000\n\t"
    "s_mul_i32 s53, s33, 0x6400\n\t"
    "s_add_u32 s52, s52, s53\n\t"
    "s_mul_i32 s53, s35, 0x400\n\t"
    "s_add_u32 s52, s52, s53\n\t"
    "s_add_u32 s52, s52, 0x20000\n\t"
    "s_cmp_lt_u32 s35, 25\n\t"
    "s_cselect_b32 s50, s35, s50\n\t"
    "s_cselect_b32 s51, s52, s51\n\t"
    "s_mul_i32 s50, s50, 12\n\t"
    "s_add_u32 s50, s50, 12\n\t"
    "s_getpc_b64 s[42:43]\n\t"
    "s_add_u32 s42, s42, s50\n\t"
    "s_addc_u32 s43, s43, 0\n\t"
    "s_setpc_b64 s[42:43]\n\t"
    "buffer_store_dwordx4 a[80:83], v1, s[12:15], s51 offen sc1\n\t"
    "s_branch fo20_%=\n\t"
    "buffer_store_dwordx4 a[84:87], v1, s[12:15], s51 offen sc1\n\t"
    "s_branch fo20_%=\n\t"
    "buffer_store_dwordx4 a[88:91], v1, s[12:15], s51 offen sc1\n\t"
    "s_branch fo20_%=\n\t"
    "buffer_store_dwordx4 a[92:95], v1, s[12:15], s51 offen sc1\n\t"
    "s_branch fo20_%=\n\t"
    "buffer_store_dwordx4 a[96:99], v1, s[12:15], s51 offen sc1\n\t"
    "s_branch fo20_%=\n\t"
    "buffer_store_dwordx4 a[100:103], v1, s[12:15], s51 offen sc1\n\t"
    "s_branch fo20_%=\n\t"
    "buffer_store_dwordx4 a[104:107], v1, s[12:15], s51 offen sc1\n\t"
    "s_branch fo20_%=\n\t"
    "buffer_store_dwordx4 a[108:111], v1, s[12:15], s51 offen sc1\n\t"
    "s_branch fo20_%=\n\t"
    "buffer_store_dwordx4 a[112:115], v1, s[12:15], s51 offen sc1\n\t"
    "s_branch fo20_%=\n\t"
    "buffer_store_dwordx4 a[116:119], v1, s[12:15], s51 offen sc1\n\t"
    "s_branch fo20_%=\n\t"
    "buffer_store_dwordx4 a[120:123], v1, s[12:15], s51 offen sc1\n\t"
    "s_branch fo20_%=\n\t"
    "buffer_store_dwordx4 a[124:127], v1, s[12:15], s51 offen sc1\n\t"
    "s_branch fo20_%=\n\t"
    "buffer_store_dwordx4 a[160:163], v1, s[12:15], s51 offen sc1\n\t"
    "s_branch fo20_%=\n\t"
    "buffer_store_dwordx4 a[164:167], v1, s[12:15], s51 offen sc1\n\t"
    "s_branch fo20_%=\n\t"
    "buffer_store_dwordx4 a[168:171], v1, s[12:15], s51 offen sc1\n\t"
    "s_branch fo20_%=\n\t"
    "buffer_store_dwordx4 a[128:131], v1, s[12:15], s51 offen sc1\n\t"
    "s_branch fo20_%=\n\t"
    "buffer_store_dwordx4 a[172:175], v1, s[12:15], s51 offen sc1\n\t"
    "s_branch fo20_%=\n\t"
    "buffer_store_dwordx4 a[176:179], v1, s[12:15], s51 offen sc1\n\t"
    "s_branch fo20_%=\n\t"
    "buffer_store_dwordx4 a[208:211], v1, s[12:15], s51 offen sc1\n\t"
    "s_branch fo20_%=\n\t"
    "buffer_store_dwordx4 a[212:215], v1, s[12:15], s51 offen sc1\n\t"
    "s_branch fo20_%=\n\t"
    "buffer_store_dwordx4 a[132:135], v1, s[12:15], s51 offen sc1\n\t"
    "s_branch fo20_%=\n\t"
    "buffer_store_dwordx4 a[180:183], v1, s[12:15], s51 offen sc1\n\t"
    "s_branch fo20_%=\n\t"
    "buffer_store_dwordx4 a[216:219], v1, s[12:15], s51 offen sc1\n\t"
    "s_branch fo20_%=\n\t"
    "buffer_store_dwordx4 a[220:223], v1, s[12:15], s51 offen sc1\n\t"
    "s_branch fo20_%=\n\t"
    "buffer_store_dwordx4 a[224:227], v1, s[12:15], s51 offen sc1\n\t"
    "s_branch fo20_%=\n\t"
    "buffer_store_dwordx4 a[0:3], v1, s[12:15], s51 offen sc1\n\t"
    "s_branch fo20_%=\n\t"
    "buffer_store_dwordx4 a[4:7], v1, s[12:15], s51 offen sc1\n\t"
    "s_branch fo20_%=\n\t"
    "buffer_store_dwordx4 a[8:11], v1, s[12:15], s51 offen sc1\n\t"
    "s_branch fo20_%=\n\t"
    "buffer_store_dwordx4 a[12:15], v1, s[12:15], s51 offen sc1\n\t"
    "s_branch fo20_%=\n\t"
    "buffer_store_dwordx4 a[16:19], v1, s[12:15], s51 offen sc1\n\t"
    "s_branch fo20_%=\n\t"
    "buffer_store_dwordx4 a[20:23], v1, s[12:15], s51 offen sc1\n\t"
    "s_branch fo20_%=\n\t"
    "buffer_store_dwordx4 a[24:27], v1, s[12:15], s51 offen sc1\n\t"
    "s_branch fo20_%=\n\t"
    "buffer_store_dwordx4 a[28:31], v1, s[12:15], s51 offen sc1\n\t"
    "s_branch fo20_%=\n\t"
    "buffer_store_dwordx4 a[32:35], v1, s[12:15], s51 offen sc1\n\t"
    "s_branch fo20_%=\n\t"
    "buffer_store_dwordx4 a[36:39], v1, s[12:15], s51 offen sc1\n\t"
    "s_branch fo20_%=\n\t"
    "fo20_%=:\n\t"
    "s_nop 1\n\t"
    "v_exp_f32 v86, v86\n\t"
    "v_exp_f32 v87, v87\n\t"
    "v_exp_f32 v88, v88\n\t"
    "v_exp_f32 v89, v89\n\t"
    "v_smfmac_f32_32x32x32_bf16 v[144:159], v[108:111], v[80:87], v124\n\t"
    "v_exp_f32 v90, v90\n\t"
    "v_exp_f32 v91, v91\n\t"
    "v_exp_f32 v92, v92\n\t"
    "v_exp_f32 v93, v93\n\t"
    "v_mfma_f32_32x32x16_bf16 v[48:63], v[96:99], v[20:23], v[48:63]\n\t"
    "v_exp_f32 v94, v94\n\t"
    "v_exp_f32 v95, v95\n\t"
    "s_waitcnt vmcnt(6)\n\t"
    "s_barrier\n\t"
    "v_exp_f32 v32, v32\n\t"
    "v_exp_f32 v33, v33\n\t"
    "v_smfmac_f32_32x32x32_bf16 v[144:159], v[116:119], v[88:95], v124\n\t"
    "s_waitcnt lgkmcnt(0)\n\t"
    "ds_read_b128 v[96:99], v125 offset:10496\n\t"
    "v_exp_f32 v34, v34\n\t"
    "v_exp_f32 v35, v35\n\t"
    "v_exp_f32 v36, v36\n\t"
    "v_exp_f32 v37, v37\n\t"
    "v_mfma_f32_32x32x4_2b_bf16 v[64:95], v[122:123], v[24:25], 0 cbsz:1 abid:1\n\t"
    "ds_read_b128 v[108:111], v127 offset:9152\n\t"
    "v_exp_f32 v38, v38\n\t"
    "v_exp_f32 v39, v39\n\t"
    "v_exp_f32 v40, v40\n\t"
    "v_exp_f32 v41, v41\n\t"
    "v_exp_f32 v42, v42\n\t"
    "v_exp_f32 v43, v43\n\t"
    "v_exp_f32 v44, v44\n\t"
    "v_exp_f32 v45, v45\n\t"
    "v_smfmac_f32_32x32x32_bf16 v[128:143], v[104:107], v[32:39], v124\n\t"
    "ds_read_b128 v[116:119], v127 offset:9824\n\t"
    "v_exp_f32 v46, v46\n\t"
    "v_exp_f32 v47, v47\n\t"
    "v_exp_f32 v48, v48\n\t"
    "v_exp_f32 v49, v49\n\t"
    "v_smfmac_f32_32x32x32_bf16 v[128:143], v[112:115], v[40:47], v124\n\t"
    "ds_read_b64 v[120:121], v126 offset:12544\n\t"
    "v_exp_f32 v50, v50\n\t"
    "v_exp_f32 v51, v51\n\t"
    "v_exp_f32 v52, v52\n\t"
    "v_exp_f32 v53, v53\n\t"
    "v_mfma_f32_32x32x16_bf16 v[64:79], v[100:103], v[16:19], v[64:79]\n\t"
    "s_add_u32 m0, m0, 0x4000\n\t"
    "s_add_u32 s54, s54, 0x4000\n\t"
    "buffer_load_dwordx4 v1, s[12:15], s54 offen sc1 lds\n\t"
    "v_exp_f32 v54, v54\n\t"
    "v_exp_f32 v55, v55\n\t"
    "v_exp_f32 v56, v56\n\t"
    "v_exp_f32 v57, v57\n\t"
    "v_smfmac_f32_32x32x32_bf16 v[144:159], v[104:107], v[48:55], v124\n\t"
    "v_exp_f32 v58, v58\n\t"
    "v_exp_f32 v59, v59\n\t"
    "v_exp_f32 v60, v60\n\t"
    "v_exp_f32 v61, v61\n\t"
    "v_mfma_f32_32x32x16_bf16 v[80:95], v[100:103], v[20:23], v[80:95]\n\t"
    "buffer_load_dwordx4 v1, s[12:15], s54 offen offset:1024 sc1 lds\n\t"
    "v_exp_f32 v62, v62\n\t"
    "v_exp_f32 v63, v63\n\t"
    "v_exp_f32 v64, v64\n\t"
    "v_exp_f32 v65, v65\n\t"
    "v_smfmac_f32_32x32x32_bf16 v[144:159], v[112:115], v[56:63], v124\n\t"
    "s_waitcnt lgkmcnt(0)\n\t"
    "ds_read_b128 v[100:103], v125 offset:11520\n\t"
    "v_exp_f32 v66, v66\n\t"
    "v_exp_f32 v67, v67\n\t"
    "v_exp_f32 v68, v68\n\t"
    "v_exp_f32 v69, v69\n\t"
    "v_mfma_f32_32x32x4_2b_bf16 v[32:63], v[120:121], v[24:25], 0 cbsz:1 abid:0\n\t"
    "ds_read_b128 v[104:107], v127 offset:13056\n\t"
    "v_exp_f32 v70, v70\n\t"
    "v_exp_f32 v71, v71\n\t"
    "v_exp_f32 v72, v72\n\t"
    "v_exp_f32 v73, v73\n\t"
    "v_exp_f32 v74, v74\n\t"
    "v_exp_f32 v75, v75\n\t"
    "v_exp_f32 v76, v76\n\t"
    "v_exp_f32 v77, v77\n\t"
    "v_smfmac_f32_32x32x32_bf16 v[128:143], v[108:111], v[64:71], v124\n\t"
    "ds_read_b128 v[112:115], v127 offset:13728\n\t"
    "buffer_load_dwordx4 v1, s[12:15], s54 offen offset:2048 sc1 lds\n\t"
    "v_exp_f32 v78, v78\n\t"
    "v_exp_f32 v79, v79\n\t"
    "v_exp_f32 v80, v80\n\t"
    "v_exp_f32 v81, v81\n\t"
    "v_smfmac_f32_32x32x32_bf16 v[128:143], v[116:119], v[72:79], v124\n\t"
    "v_exp_f32 v82, v82\n\t"
    "v_exp_f32 v83, v83\n\t"
    "v_exp_f32 v84, v84\n\t"
    "v_exp_f32 v85, v85\n\t"
    "v_mfma_f32_32x32x16_bf16 v[32:47], v[96:99], v[16:19], v[32:47]\n\t"
    "buffer_load_dwordx4 v1, s[12:15], s54 offen offset:3072 sc1 lds\n\t"
    "v_exp_f32 v86, v86\n\t"
    "v_exp_f32 v87, v87\n\t"
    "v_exp_f32 v88, v88\n\t"
    "v_exp_f32 v89, v89\n\t"
    "v_smfmac_f32_32x32x32_bf16 v[144:159], v[108:111], v[80:87], v124\n\t"
    "v_exp_f32 v90, v90\n\t"
    "v_exp_f32 v91, v91\n\t"
    "v_exp_f32 v92, v92\n\t"
    "v_exp_f32 v93, v93\n\t"
    "v_mfma_f32_32x32x16_bf16 v[48:63], v[96:99], v[20:23], v[48:63]\n\t"
    "s_add_u32 m0, m0, 0x4000\n\t"
    "s_add_u32 s54, s54, 0x4000\n\t"
    "buffer_load_dwordx4 v1, s[12:15], s54 offen sc1 lds\n\t"
    "v_exp_f32 v94, v94\n\t"
    "v_exp_f32 v95, v95\n\t"
    "v_exp_f32 v32, v32\n\t"
    "v_exp_f32 v33, v33\n\t"
    "v_smfmac_f32_32x32x32_bf16 v[144:159], v[116:119], v[88:95], v124\n\t"
    "s_waitcnt lgkmcnt(0)\n\t"
    "ds_read_b128 v[96:99], v125 offset:15744\n\t"
    "v_exp_f32 v34, v34\n\t"
    "v_exp_f32 v35, v35\n\t"
    "v_exp_f32 v36, v36\n\t"
    "v_exp_f32 v37, v37\n\t"
    "v_mfma_f32_32x32x4_2b_bf16 v[64:95], v[120:121], v[24:25], 0 cbsz:1 abid:1\n\t"
    "ds_read_b128 v[108:111], v127 offset:14400\n\t"
    "v_exp_f32 v38, v38\n\t"
    "v_exp_f32 v39, v39\n\t"
    "v_exp_f32 v40, v40\n\t"
    "v_exp_f32 v41, v41\n\t"
    "v_exp_f32 v42, v42\n\t"
    "v_exp_f32 v43, v43\n\t"
    "v_exp_f32 v44, v44\n\t"
    "v_exp_f32 v45, v45\n\t"
    "v_smfmac_f32_32x32x32_bf16 v[128:143], v[104:107], v[32:39], v124\n\t"
    "ds_read_b128 v[116:119], v127 offset:15072\n\t"
    "buffer_load_dwordx4 v1, s[12:15], s54 offen offset:1024 sc1 lds\n\t"
    "v_exp_f32 v46, v46\n\t"
    "v_exp_f32 v47, v47\n\t"
    "v_exp_f32 v48, v48\n\t"
    "v_exp_f32 v49, v49\n\t"
    "v_smfmac_f32_32x32x32_bf16 v[128:143], v[112:115], v[40:47], v124\n\t"
    "ds_read_b64 v[122:123], v126 offset:17792\n\t"
    "v_exp_f32 v50, v50\n\t"
    "v_exp_f32 v51, v51\n\t"
    "v_exp_f32 v52, v52\n\t"
    "v_exp_f32 v53, v53\n\t"
    "v_mfma_f32_32x32x16_bf16 v[64:79], v[100:103], v[16:19], v[64:79]\n\t"
    "buffer_load_dwordx4 v1, s[12:15], s54 offen offset:2048 sc1 lds\n\t"
    "v_exp_f32 v54, v54\n\t"
    "v_exp_f32 v55, v55\n\t"
    "v_exp_f32 v56, v56\n\t"
    "v_exp_f32 v57, v57\n\t"
    "v_smfmac_f32_32x32x32_bf16 v[144:159], v[104:107], v[48:55], v124\n\t"
    "v_exp_f32 v58, v58\n\t"
    "v_exp_f32 v59, v59\n\t"
    "v_exp_f32 v60, v60\n\t"
    "v_exp_f32 v61, v61\n\t"
    "v_mfma_f32_32x32x16_bf16 v[80:95], v[100:103], v[20:23], v[80:95]\n\t"
    "buffer_load_dwordx4 v1, s[12:15], s54 offen offset:3072 sc1 lds\n\t"
    "v_exp_f32 v62, v62\n\t"
    "v_exp_f32 v63, v63\n\t"
    "v_exp_f32 v64, v64\n\t"
    "v_exp_f32 v65, v65\n\t"
    "v_smfmac_f32_32x32x32_bf16 v[144:159], v[112:115], v[56:63], v124\n\t"
    "s_waitcnt lgkmcnt(0)\n\t"
    "ds_read_b128 v[100:103], v125 offset:16768\n\t"
    "v_exp_f32 v66, v66\n\t"
    "v_exp_f32 v67, v67\n\t"
    "v_exp_f32 v68, v68\n\t"
    "v_exp_f32 v69, v69\n\t"
    "v_mfma_f32_32x32x4_2b_bf16 v[32:63], v[122:123], v[24:25], 0 cbsz:1 abid:0\n\t"
    "ds_read_b128 v[104:107], v127 offset:18304\n\t"
    "v_exp_f32 v70, v70\n\t"
    "v_exp_f32 v71, v71\n\t"
    "v_exp_f32 v72, v72\n\t"
    "v_exp_f32 v73, v73\n\t"
    "v_exp_f32 v74, v74\n\t"
    "v_exp_f32 v75, v75\n\t"
    "v_exp_f32 v76, v76\n\t"
    "v_exp_f32 v77, v77\n\t"
    "v_smfmac_f32_32x32x32_bf16 v[128:143], v[108:111], v[64:71], v124\n\t"
    "ds_read_b128 v[112:115], v127 offset:18976\n\t"
    "s_add_u32 m0, m0, 0x4000\n\t"
    "s_add_u32 s54, s54, 0x4000\n\t"
    "buffer_load_dwordx4 v1, s[12:15], s54 offen sc1 lds\n\t"
    "v_exp_f32 v78, v78\n\t"
    "v_exp_f32 v79, v79\n\t"
    "v_exp_f32 v80, v80\n\t"
    "v_exp_f32 v81, v81\n\t"
    "v_smfmac_f32_32x32x32_bf16 v[128:143], v[116:119], v[72:79], v124\n\t"
    "v_exp_f32 v82, v82\n\t"
    "v_exp_f32 v83, v83\n\t"
    "v_exp_f32 v84, v84\n\t"
    "v_exp_f32 v85, v85\n\t"
    "v_mfma_f32_32x32x16_bf16 v[32:47], v[96:99], v[16:19], v[32:47]\n\t"
    "buffer_load_dwordx4 v1, s[12:15], s54 offen offset:1024 sc1 lds\n\t"
    "v_exp_f32 v86, v86\n\t"
    "v_exp_f32 v87, v87\n\t"
    "v_exp_f32 v88, v88\n\t"
    "v_exp_f32 v89, v89\n\t"
    "v_smfmac_f32_32x32x32_bf16 v[144:159], v[108:111], v[80:87], v124\n\t"
    "v_exp_f32 v90, v90\n\t"
    "v_exp_f32 v91, v91\n\t"
    "v_exp_f32 v92, v92\n\t"
    "v_exp_f32 v93, v93\n\t"
    "v_mfma_f32_32x32x16_bf16 v[48:63], v[96:99], v[20:23], v[48:63]\n\t"
    "buffer_load_dwordx4 v1, s[12:15], s54 offen offset:2048 sc1 lds\n\t"
    "v_exp_f32 v94, v94\n\t"
    "v_exp_f32 v95, v95\n\t"
    "v_exp_f32 v32, v32\n\t"
    "v_exp_f32 v33, v33\n\t"
    "v_smfmac_f32_32x32x32_bf16 v[144:159], v[116:119], v[88:95], v124\n\t"
    "s_waitcnt lgkmcnt(0)\n\t"
    "ds_read_b128 v[96:99], v125 offset:20992\n\t"
    "v_exp_f32 v34, v34\n\t"
    "v_exp_f32 v35, v35\n\t"
    "v_exp_f32 v36, v36\n\t"
    "v_exp_f32 v37, v37\n\t"
    "v_mfma_f32_32x32x4_2b_bf16 v[64:95], v[122:123], v[24:25], 0 cbsz:1 abid:1\n\t"
    "ds_read_b128 v[108:111], v127 offset:19648\n\t"
    "v_exp_f32 v38, v38\n\t"
    "v_exp_f32 v39, v39\n\t"
    "v_exp_f32 v40, v40\n\t"
    "v_exp_f32 v41, v41\n\t"
    "v_exp_f32 v42, v42\n\t"
    "v_exp_f32 v43, v43\n\t"
    "v_exp_f32 v44, v44\n\t"
    "v_exp_f32 v45, v45\n\t"
    "v_smfmac_f32_32x32x32_bf16 v[128:143], v[104:107], v[32:39], v124\n\t"
    "ds_read_b128 v[116:119], v127 offset:20320\n\t"
    "buffer_load_dwordx4 v1, s[12:15], s54 offen offset:3072 sc1 lds\n\t"
    "v_exp_f32 v46, v46\n\t"
    "v_exp_f32 v47, v47\n\t"
    "v_exp_f32 v48, v48\n\t"
    "v_exp_f32 v49, v49\n\t"
    "v_smfmac_f32_32x32x32_bf16 v[128:143], v[112:115], v[40:47], v124\n\t"
    "ds_read_b64 v[120:121], v126 offset:23040\n\t"
    "v_exp_f32 v50, v50\n\t"
    "v_exp_f32 v51, v51\n\t"
    "v_exp_f32 v52, v52\n\t"
    "v_exp_f32 v53, v53\n\t"
    "v_mfma_f32_32x32x16_bf16 v[64:79], v[100:103], v[16:19], v[64:79]\n\t"
    "s_add_u32 m0, m0, 0x4000\n\t"
    "s_add_u32 s54, s54, 0x4000\n\t"
    "buffer_load_dwordx4 v1, s[12:15], s54 offen sc1 lds\n\t"
    "v_exp_f32 v54, v54\n\t"
    "v_exp_f32 v55, v55\n\t"
    "v_exp_f32 v56, v56\n\t"
    "v_exp_f32 v57, v57\n\t"
    "v_smfmac_f32_32x32x32_bf16 v[144:159], v[104:107], v[48:55], v124\n\t"
    "v_exp_f32 v58, v58\n\t"
    "v_exp_f32 v59, v59\n\t"
    "v_exp_f32 v60, v60\n\t"
    "v_exp_f32 v61, v61\n\t"
    "v_mfma_f32_32x32x16_bf16 v[80:95], v[100:103], v[20:23], v[80:95]\n\t"
    "buffer_load_dwordx4 v1, s[12:15], s54 offen offset:1024 sc1 lds\n\t"
    "v_exp_f32 v62, v62\n\t"
    "v_exp_f32 v63, v63\n\t"
    "v_exp_f32 v64, v64\n\t"
    "v_exp_f32 v65, v65\n\t"
    "v_smfmac_f32_32x32x32_bf16 v[144:159], v[112:115], v[56:63], v124\n\t"
    "s_waitcnt lgkmcnt(0)\n\t"
    "ds_read_b128 v[100:103], v125 offset:22016\n\t"
    "v_exp_f32 v66, v66\n\t"
    "v_exp_f32 v67, v67\n\t"
    "v_exp_f32 v68, v68\n\t"
    "v_exp_f32 v69, v69\n\t"
    "v_mfma_f32_32x32x4_2b_bf16 v[32:63], v[120:121], v[24:25], 0 cbsz:1 abid:0\n\t"
    "ds_read_b128 v[104:107], v127 offset:23552\n\t"
    "v_exp_f32 v70, v70\n\t"
    "v_exp_f32 v71, v71\n\t"
    "v_exp_f32 v72, v72\n\t"
    "v_exp_f32 v73, v73\n\t"
    "v_exp_f32 v74, v74\n\t"
    "v_exp_f32 v75, v75\n\t"
    "v_exp_f32 v76, v76\n\t"
    "v_exp_f32 v77, v77\n\t"
    "v_smfmac_f32_32x32x32_bf16 v[128:143], v[108:111], v[64:71], v124\n\t"
    "ds_read_b128 v[112:115], v127 offset:24224\n\t"
    "s_waitcnt vmcnt(14)\n\t"
    "ds_read_b64_tr_b16 a[40:41], v250\n\t"
    "pad3_%=:\n\t"
    "s_cbranch_execz vb8_%=\n\t"
    "ds_read_b64_tr_b16 a[42:43], v251\n\t"
    "v_exp_f32 v78, v78\n\t"
    "v_exp_f32 v79, v79\n\t"
    "v_exp_f32 v80, v80\n\t"
    "v_exp_f32 v81, v81\n\t"
    "v_smfmac_f32_32x32x32_bf16 v[128:143], v[116:119], v[72:79], v124\n\t"
    "v_exp_f32 v82, v82\n\t"
    "v_exp_f32 v83, v83\n\t"
    "v_exp_f32 v84, v84\n\t"
    "v_exp_f32 v85, v85\n\t"
    "v_mfma_f32_32x32x16_bf16 v[32:47], v[96:99], v[16:19], v[32:47]\n\t"
    "ds_read_b64_tr_b16 a[44:45], v250 offset:8\n\t"
    "ds_read_b64_tr_b16 a[46:47], v251 offset:8\n\t"
    "v_exp_f32 v86, v86\n\t"
    "v_exp_f32 v87, v87\n\t"
    "v_exp_f32 v88, v88\n\t"
    "v_exp_f32 v89, v89\n\t"
    "v_smfmac_f32_32x32x32_bf16 v[144:159], v[108:111], v[80:87], v124\n\t"
    "v_exp_f32 v90, v90\n\t"
    "v_exp_f32 v91, v91\n\t"
    "v_exp_f32 v92, v92\n\t"
    "v_exp_f32 v93, v93\n\t"
    "v_mfma_f32_32x32x16_bf16 v[48:63], v[96:99], v[20:23], v[48:63]\n\t"
    "ds_read_b64_tr_b16 a[48:49], v250 offset:64\n\t"
    "ds_read_b64_tr_b16 a[50:51], v251 offset:64\n\t"
    "v_exp_f32 v94, v94\n\t"
    "v_exp_f32 v95, v95\n\t"
    "v_exp_f32 v32, v32\n\t"
    "v_exp_f32 v33, v33\n\t"
    "v_smfmac_f32_32x32x32_bf16 v[144:159], v[116:119], v[88:95], v124\n\t"
    "s_waitcnt lgkmcnt(0)\n\t"
    "ds_read_b128 v[96:99], v125 offset:26240\n\t"
    "v_exp_f32 v34, v34\n\t"
    "v_exp_f32 v35, v35\n\t"
    "v_exp_f32 v36, v36\n\t"
    "v_exp_f32 v37, v37\n\t"
    "v_mfma_f32_32x32x4_2b_bf16 v[64:95], v[120:121], v[24:25], 0 cbsz:1 abid:1\n\t"
    "ds_read_b128 v[108:111], v127 offset:24896\n\t"
    "v_exp_f32 v38, v38\n\t"
    "v_exp_f32 v39, v39\n\t"
    "v_exp_f32 v40, v40\n\t"
    "v_exp_f32 v41, v41\n\t"
    "v_exp_f32 v42, v42\n\t"
    "v_exp_f32 v43, v43\n\t"
    "v_exp_f32 v44, v44\n\t"
    "v_exp_f32 v45, v45\n\t"
    "v_smfmac_f32_32x32x32_bf16 v[128:143], v[104:107], v[32:39], v124\n\t"
    "ds_read_b128 v[116:119], v127 offset:25568\n\t"
    "buffer_load_dwordx4 v1, s[12:15], s54 offen offset:2048 sc1 lds\n\t"
    "v_exp_f32 v46, v46\n\t"
    "v_exp_f32 v47, v47\n\t"
    "v_exp_f32 v48, v48\n\t"
    "v_exp_f32 v49, v49\n\t"
    "v_smfmac_f32_32x32x32_bf16 v[128:143], v[112:115], v[40:47], v124\n\t"
    "ds_read_b64 v[122:123], v126 offset:28288\n\t"
    "v_exp_f32 v50, v50\n\t"
    "v_exp_f32 v51, v51\n\t"
    "v_exp_f32 v52, v52\n\t"
    "v_exp_f32 v53, v53\n\t"
    "v_mfma_f32_32x32x16_bf16 v[64:79], v[100:103], v[16:19], v[64:79]\n\t"
    "buffer_load_dwordx4 v1, s[12:15], s54 offen offset:3072 sc1 lds\n\t"
    "v_exp_f32 v54, v54\n\t"
    "v_exp_f32 v55, v55\n\t"
    "v_exp_f32 v56, v56\n\t"
    "v_exp_f32 v57, v57\n\t"
    "v_smfmac_f32_32x32x32_bf16 v[144:159], v[104:107], v[48:55], v124\n\t"
    "v_exp_f32 v58, v58\n\t"
    "v_exp_f32 v59, v59\n\t"
    "v_exp_f32 v60, v60\n\t"
    "v_exp_f32 v61, v61\n\t"
    "v_mfma_f32_32x32x16_bf16 v[80:95], v[100:103], v[20:23], v[80:95]\n\t"
    "s_add_u32 m0, m0, 0x4000\n\t"
    "s_add_u32 s54, s54, 0x4000\n\t"
    "buffer_load_dwordx4 v1, s[12:15], s54 offen sc1 lds\n\t"
    "v_exp_f32 v62, v62\n\t"
    "v_exp_f32 v63, v63\n\t"
    "v_exp_f32 v64, v64\n\t"
    "v_exp_f32 v65, v65\n\t"
    "v_smfmac_f32_32x32x32_bf16 v[144:159], v[112:115], v[56:63], v124\n\t"
    "s_waitcnt lgkmcnt(0)\n\t"
    "ds_read_b128 v[100:103], v125 offset:27264\n\t"
    "v_exp_f32 v66, v66\n\t"
    "v_exp_f32 v67, v67\n\t"
    "v_exp_f32 v68, v68\n\t"
    "v_exp_f32 v69, v69\n\t"
    "v_mfma_f32_32x32x4_2b_bf16 v[32:63], v[122:123], v[24:25], 0 cbsz:1 abid:0\n\t"
    "ds_read_b128 v[104:107], v127 offset:28800\n\t"
    "v_exp_f32 v70, v70\n\t"
    "v_exp_f32 v71, v71\n\t"
    "v_exp_f32 v72, v72\n\t"
    "v_exp_f32 v73, v73\n\t"
    "v_exp_f32 v74, v74\n\t"
    "v_exp_f32 v75, v75\n\t"
    "v_exp_f32 v76, v76\n\t"
    "v_exp_f32 v77, v77\n\t"
    "v_smfmac_f32_32x32x32_bf16 v[128:143], v[108:111], v[64:71], v124\n\t"
    "ds_read_b128 v[112:115], v127 offset:29472\n\t"
    "ds_read_b64_tr_b16 a[52:53], v250 offset:72\n\t"
    "ds_read_b64_tr_b16 a[54:55], v251 offset:72\n\t"
    "v_exp_f32 v78, v78\n\t"
    "v_exp_f32 v79, v79\n\t"
    "v_exp_f32 v80, v80\n\t"
    "v_exp_f32 v81, v81\n\t"
    "v_smfmac_f32_32x32x32_bf16 v[128:143], v[116:119], v[72:79], v124\n\t"
    "v_exp_f32 v82, v82\n\t"
    "v_exp_f32 v83, v83\n\t"
    "v_exp_f32 v84, v84\n\t"
    "v_exp_f32 v85, v85\n\t"
    "v_mfma_f32_32x32x16_bf16 v[32:47], v[96:99], v[16:19], v[32:47]\n\t"
    "ds_read_b64_tr_b16 a[56:57], v252\n\t"
    "ds_read_b64_tr_b16 a[58:59], v253\n\t"
    "v_exp_f32 v86, v86\n\t"
    "v_exp_f32 v87, v87\n\t"
    "v_exp_f32 v88, v88\n\t"
    "v_exp_f32 v89, v89\n\t"
    "v_smfmac_f32_32x32x32_bf16 v[144:159], v[108:111], v[80:87], v124\n\t"
    "v_exp_f32 v90, v90\n\t"
    "v_exp_f32 v91, v91\n\t"
    "v_exp_f32 v92, v92\n\t"
    "v_exp_f32 v93, v93\n\t"
    "v_mfma_f32_32x32x16_bf16 v[48:63], v[96:99], v[20:23], v[48:63]\n\t"
    "ds_read_b64_tr_b16 a[60:61], v252 offset:8\n\t"
    "ds_read_b64_tr_b16 a[62:63], v253 offset:8\n\t"
    "v_exp_f32 v94, v94\n\t"
    "v_exp_f32 v95, v95\n\t"
    "v_exp_f32 v32, v32\n\t"
    "v_exp_f32 v33, v33\n\t"
    "v_smfmac_f32_32x32x32_bf16 v[144:159], v[116:119], v[88:95], v124\n\t"
    "s_waitcnt lgkmcnt(0)\n\t"
    "ds_read_b128 v[96:99], v125 offset:31488\n\t"
    "v_exp_f32 v34, v34\n\t"
    "v_exp_f32 v35, v35\n\t"
    "v_exp_f32 v36, v36\n\t"
    "v_exp_f32 v37, v37\n\t"
    "v_mfma_f32_32x32x4_2b_bf16 v[64:95], v[122:123], v[24:25], 0 cbsz:1 abid:1\n\t"
    "ds_read_b128 v[108:111], v127 offset:30144\n\t"
    "v_exp_f32 v38, v38\n\t"
    "v_exp_f32 v39, v39\n\t"
    "v_exp_f32 v40, v40\n\t"
    "v_exp_f32 v41, v41\n\t"
    "v_exp_f32 v42, v42\n\t"
    "v_exp_f32 v43, v43\n\t"
    "v_exp_f32 v44, v44\n\t"
    "v_exp_f32 v45, v45\n\t"
    "v_smfmac_f32_32x32x32_bf16 v[128:143], v[104:107], v[32:39], v124\n\t"
    "ds_read_b128 v[116:119], v127 offset:30816\n\t"
    "buffer_load_dwordx4 v1, s[12:15], s54 offen offset:1024 sc1 lds\n\t"
    "v_exp_f32 v46, v46\n\t"
    "v_exp_f32 v47, v47\n\t"
    "v_exp_f32 v48, v48\n\t"
    "v_exp_f32 v49, v49\n\t"
    "v_smfmac_f32_32x32x32_bf16 v[128:143], v[112:115], v[40:47], v124\n\t"
    "ds_read_b64 v[120:121], v126 offset:33536\n\t"
    "v_exp_f32 v50, v50\n\t"
    "v_exp_f32 v51, v51\n\t"
    "v_exp_f32 v52, v52\n\t"
    "v_exp_f32 v53, v53\n\t"
    "v_mfma_f32_32x32x16_bf16 v[64:79], v[100:103], v[16:19], v[64:79]\n\t"
    "buffer_load_dwordx4 v1, s[12:15], s54 offen offset:2048 sc1 lds\n\t"
    "v_exp_f32 v54, v54\n\t"
    "v_exp_f32 v55, v55\n\t"
    "v_exp_f32 v56, v56\n\t"
    "v_exp_f32 v57, v57\n\t"
    "v_smfmac_f32_32x32x32_bf16 v[144:159], v[104:107], v[48:55], v124\n\t"
    "v_exp_f32 v58, v58\n\t"
    "v_exp_f32 v59, v59\n\t"
    "v_exp_f32 v60, v60\n\t"
    "v_exp_f32 v61, v61\n\t"
    "v_mfma_f32_32x32x16_bf16 v[80:95], v[100:103], v[20:23], v[80:95]\n\t"
    "buffer_load_dwordx4 v1, s[12:15], s54 offen offset:3072 sc1 lds\n\t"
    "v_exp_f32 v62, v62\n\t"
    "v_exp_f32 v63, v63\n\t"
    "v_exp_f32 v64, v64\n\t"
    "v_exp_f32 v65, v65\n\t"
    "v_smfmac_f32_32x32x32_bf16 v[144:159], v[112:115], v[56:63], v124\n\t"
    "s_waitcnt lgkmcnt(0)\n\t"
    "ds_read_b128 v[100:103], v125 offset:32512\n\t"
    "v_exp_f32 v66, v66\n\t"
    "v_exp_f32 v67, v67\n\t"
    "v_exp_f32 v68, v68\n\t"
    "v_exp_f32 v69, v69\n\t"
    "v_mfma_f32_32x32x4_2b_bf16 v[32:63], v[120:121], v[24:25], 0 cbsz:1 abid:0\n\t"
    "ds_read_b128 v[104:107], v127 offset:34048\n\t"
    "v_exp_f32 v70, v70\n\t"
    "v_exp_f32 v71, v71\n\t"
    "v_exp_f32 v72, v72\n\t"
    "v_exp_f32 v73, v73\n\t"
    "v_exp_f32 v74, v74\n\t"
    "v_exp_f32 v75, v75\n\t"
    "v_exp_f32 v76, v76\n\t"
    "v_exp_f32 v77, v77\n\t"
    "v_smfmac_f32_32x32x32_bf16 v[128:143], v[108:111], v[64:71], v124\n\t"
    "ds_read_b128 v[112:115], v127 offset:34720\n\t"
    "buffer_store_dwordx4 a[40:43], v1, s[92:95], s81 offen sc1\n\t"
    "v_exp_f32 v78, v78\n\t"
    "v_exp_f32 v79, v79\n\t"
    "v_exp_f32 v80, v80\n\t"
    "v_exp_f32 v81, v81\n\t"
    "v_smfmac_f32_32x32x32_bf16 v[128:143], v[116:119], v[72:79], v124\n\t"
    "v_exp_f32 v82, v82\n\t"
    "v_exp_f32 v83, v83\n\t"
    "v_exp_f32 v84, v84\n\t"
    "v_exp_f32 v85, v85\n\t"
    "v_mfma_f32_32x32x16_bf16 v[32:47], v[96:99], v[16:19], v[32:47]\n\t"
    "buffer_store_dwordx4 a[44:47], v1, s[92:95], s81 offen offset:1024 sc1\n\t"
    "v_exp_f32 v86, v86\n\t"
    "v_exp_f32 v87, v87\n\t"
    "v_exp_f32 v88, v88\n\t"
    "v_exp_f32 v89, v89\n\t"
    "v_smfmac_f32_32x32x32_bf16 v[144:159], v[108:111], v[80:87], v124\n\t"
    "v_exp_f32 v90, v90\n\t"
    "v_exp_f32 v91, v91\n\t"
    "v_exp_f32 v92, v92\n\t"
    "v_exp_f32 v93, v93\n\t"
    "v_mfma_f32_32x32x16_bf16 v[48:63], v[96:99], v[20:23], v[48:63]\n\t"
    "s_add_u32 s81, s81, s89\n\t"
    "buffer_store_dwordx4 a[48:51], v1, s[92:95], s81 offen sc1\n\t"
    "v_exp_f32 v94, v94\n\t"
    "v_exp_f32 v95, v95\n\t"
    "v_exp_f32 v32, v32\n\t"
    "v_exp_f32 v33, v33\n\t"
    "v_smfmac_f32_32x32x32_bf16 v[144:159], v[116:119], v[88:95], v124\n\t"
    "s_waitcnt lgkmcnt(0)\n\t"
    "ds_read_b128 v[96:99], v125 offset:36736\n\t"
    "v_exp_f32 v34, v34\n\t"
    "v_exp_f32 v35, v35\n\t"
    "v_exp_f32 v36, v36\n\t"
    "v_exp_f32 v37, v37\n\t"
    "v_mfma_f32_32x32x4_2b_bf16 v[64:95], v[120:121], v[24:25], 0 cbsz:1 abid:1\n\t"
    "ds_read_b128 v[108:111], v127 offset:35392\n\t"
    "v_exp_f32 v38, v38\n\t"
    "v_exp_f32 v39, v39\n\t"
    "v_exp_f32 v40, v40\n\t"
    "v_exp_f32 v41, v41\n\t"
    "v_exp_f32 v42, v42\n\t"
    "v_exp_f32 v43, v43\n\t"
    "v_exp_f32 v44, v44\n\t"
    "v_exp_f32 v45, v45\n\t"
    "v_smfmac_f32_32x32x32_bf16 v[128:143], v[104:107], v[32:39], v124\n\t"
    "ds_read_b128 v[116:119], v127 offset:36064\n\t"
    "buffer_store_dwordx4 a[52:55], v1, s[92:95], s81 offen offset:1024 sc1\n\t"
    "v_exp_f32 v46, v46\n\t"
    "v_exp_f32 v47, v47\n\t"
    "v_exp_f32 v48, v48\n\t"
    "v_exp_f32 v49, v49\n\t"
    "v_smfmac_f32_32x32x32_bf16 v[128:143], v[112:115], v[40:47], v124\n\t"
    "ds_read_b64 v[122:123], v126 offset:38784\n\t"
    "v_exp_f32 v50, v50\n\t"
    "v_exp_f32 v51, v51\n\t"
    "v_exp_f32 v52, v52\n\t"
    "v_exp_f32 v53, v53\n\t"
    "v_mfma_f32_32x32x16_bf16 v[64:79], v[100:103], v[16:19], v[64:79]\n\t"
    "s_add_u32 s81, s81, s89\n\t"
    "buffer_store_dwordx4 a[56:59], v1, s[92:95], s81 offen sc1\n\t"
    "v_exp_f32 v54, v54\n\t"
    "v_exp_f32 v55, v55\n\t"
    "v_exp_f32 v56, v56\n\t"
    "v_exp_f32 v57, v57\n\t"
    "v_smfmac_f32_32x32x32_bf16 v[144:159], v[104:107], v[48:55], v124\n\t"
    "v_exp_f32 v58, v58\n\t"
    "v_exp_f32 v59, v59\n\t"
    "v_exp_f32 v60, v60\n\t"
    "v_exp_f32 v61, v61\n\t"
    "v_mfma_f32_32x32x16_bf16 v[80:95], v[100:103], v[20:23], v[80:95]\n\t"
    "s_cmp_eq_u32 s38, 2\n\t"
    "s_cselect_b32 s94, s94, 0\n\t"
    "buffer_store_dwordx4 a[60:63], v1, s[92:95], s81 offen offset:1024 sc1\n\t"
    "s_mov_b32 s94, 0\n\t"
    "v_exp_f32 v62, v62\n\t"
    "v_exp_f32 v63, v63\n\t"
    "v_exp_f32 v64, v64\n\t"
    "v_exp_f32 v65, v65\n\t"
    "v_smfmac_f32_32x32x32_bf16 v[144:159], v[112:115], v[56:63], v124\n\t"
    "s_waitcnt lgkmcnt(0)\n\t"
    "ds_read_b128 v[100:103], v125 offset:37760\n\t"
    "v_exp_f32 v66, v66\n\t"
    "v_exp_f32 v67, v67\n\t"
    "v_exp_f32 v68, v68\n\t"
    "v_exp_f32 v69, v69\n\t"
    "v_mfma_f32_32x32x4_2b_bf16 v[32:63], v[122:123], v[24:25], 0 cbsz:1 abid:0\n\t"
    "ds_read_b128 v[104:107], v127 offset:39296\n\t"
    "v_exp_f32 v70, v70\n\t"
    "v_exp_f32 v71, v71\n\t"
    "v_exp_f32 v72, v72\n\t"
    "v_exp_f32 v73, v73\n\t"
    "v_exp_f32 v74, v74\n\t"
    "v_exp_f32 v75, v75\n\t"
    "v_exp_f32 v76, v76\n\t"
    "v_exp_f32 v77, v77\n\t"
    "v_smfmac_f32_32x32x32_bf16 v[128:143], v[108:111], v[64:71], v124\n\t"
    "ds_read_b128 v[112:115], v127 offset:39968\n\t"
    "v_exp_f32 v78, v78\n\t"
    "v_exp_f32 v79, v79\n\t"
    "v_exp_f32 v80, v80\n\t"
    "v_exp_f32 v81, v81\n\t"
    "v_smfmac_f32_32x32x32_bf16 v[128:143], v[116:119], v[72:79], v124\n\t"
    "v_exp_f32 v82, v82\n\t"
    "v_exp_f32 v83, v83\n\t"
    "v_exp_f32 v84, v84\n\t"
    "v_exp_f32 v85, v85\n\t"
    "v_mfma_f32_32x32x16_bf16 v[32:47], v[96:99], v[16:19], v[32:47]\n\t"
    "v_exp_f32 v86, v86\n\t"
    "v_exp_f32 v87, v87\n\t"
    "v_exp_f32 v88, v88\n\t"
    "v_exp_f32 v89, v89\n\t"
    "v_smfmac_f32_32x32x32_bf16 v[144:159], v[108:111], v[80:87], v124\n\t"
    "v_exp_f32 v90, v90\n\t"
    "v_exp_f32 v91, v91\n\t"
    "v_exp_f32 v92, v92\n\t"
    "v_exp_f32 v93, v93\n\t"
    "v_mfma_f32_32x32x16_bf16 v[48:63], v[96:99], v[20:23], v[48:63]\n\t"
    "v_exp_f32 v94, v94\n\t"
    "v_exp_f32 v95, v95\n\t"
    "s_waitcnt vmcnt(6)\n\t"
    "s_barrier\n\t"
    "v_exp_f32 v32, v32\n\t"
    "v_exp_f32 v33, v33\n\t"
    "v_smfmac_f32_32x32x32_bf16 v[144:159], v[116:119], v[88:95], v124\n\t"
    "s_waitcnt lgkmcnt(0)\n\t"
    "ds_read_b128 v[96:99], v125 offset:41984\n\t"
    "v_exp_f32 v34, v34\n\t"
    "v_exp_f32 v35, v35\n\t"
    "v_exp_f32 v36, v36\n\t"
    "v_exp_f32 v37, v37\n\t"
    "v_mfma_f32_32x32x4_2b_bf16 v[64:95], v[122:123], v[24:25], 0 cbsz:1 abid:1\n\t"
    "ds_read_b128 v[108:111], v127 offset:40640\n\t"
    "v_exp_f32 v38, v38\n\t"
    "v_exp_f32 v39, v39\n\t"
    "v_exp_f32 v40, v40\n\t"
    "v_exp_f32 v41, v41\n\t"
    "v_exp_f32 v42, v42\n\t"
    "v_exp_f32 v43, v43\n\t"
    "v_exp_f32 v44, v44\n\t"
    "v_exp_f32 v45, v45\n\t"
    "v_smfmac_f32_32x32x32_bf16 v[128:143], v[104:107], v[32:39], v124\n\t"
    "ds_read_b128 v[116:119], v127 offset:41312\n\t"
    "s_add_u32 m0, m0, 0x4000\n\t"
    "s_add_u32 s54, s54, 0x4000\n\t"
    "buffer_load_dwordx4 v1, s[12:15], s54 offen sc1 lds\n\t"
    "v_exp_f32 v46, v46\n\t"
    "v_exp_f32 v47, v47\n\t"
    "v_exp_f32 v48, v48\n\t"
    "v_exp_f32 v49, v49\n\t"
    "v_smfmac_f32_32x32x32_bf16 v[128:143], v[112:115], v[40:47], v124\n\t"
    "ds_read_b64 v[120:121], v126 offset:44032\n\t"
    "v_exp_f32 v50, v50\n\t"
    "v_exp_f32 v51, v51\n\t"
    "v_exp_f32 v52, v52\n\t"
    "v_exp_f32 v53, v53\n\t"
    "v_mfma_f32_32x32x16_bf16 v[64:79], v[100:103], v[16:19], v[64:79]\n\t"
    "buffer_load_dwordx4 v1, s[12:15], s54 offen offset:1024 sc1 lds\n\t"
    "v_exp_f32 v54, v54\n\t"
    "v_exp_f32 v55, v55\n\t"
    "v_exp_f32 v56, v56\n\t"
    "v_exp_f32 v57, v57\n\t"
    "v_smfmac_f32_32x32x32_bf16 v[144:159], v[104:107], v[48:55], v124\n\t"
    "v_exp_f32 v58, v58\n\t"
    "v_exp_f32 v59, v59\n\t"
    "v_exp_f32 v60, v60\n\t"
    "v_exp_f32 v61, v61\n\t"
    "v_mfma_f32_32x32x16_bf16 v[80:95], v[100:103], v[20:23], v[80:95]\n\t"
    "buffer_load_dwordx4 v1, s[12:15], s54 offen offset:2048 sc1 lds\n\t"
    "v_exp_f32 v62, v62\n\t"
    "v_exp_f32 v63, v63\n\t"
    "v_exp_f32 v64, v64\n\t"
    "v_exp_f32 v65, v65\n\t"
    "v_smfmac_f32_32x32x32_bf16 v[144:159], v[112:115], v[56:63], v124\n\t"
    "s_waitcnt lgkmcnt(0)\n\t"
    "ds_read_b128 v[100:103], v125 offset:43008\n\t"
    "v_exp_f32 v66, v66\n\t"
    "v_exp_f32 v67, v67\n\t"
    "v_exp_f32 v68, v68\n\t"
    "v_exp_f32 v69, v69\n\t"
    "v_mfma_f32_32x32x4_2b_bf16 v[32:63], v[120:121], v[24:25], 0 cbsz:1 abid:0\n\t"
    "ds_read_b128 v[104:107], v127 offset:44544\n\t"
    "v_exp_f32 v70, v70\n\t"
    "v_exp_f32 v71, v71\n\t"
    "v_exp_f32 v72, v72\n\t"
    "v_exp_f32 v73, v73\n\t"
    "v_exp_f32 v74, v74\n\t"
    "v_exp_f32 v75, v75\n\t"
    "v_exp_f32 v76, v76\n\t"
    "v_exp_f32 v77, v77\n\t"
    "v_smfmac_f32_32x32x32_bf16 v[128:143], v[108:111], v[64:71], v124\n\t"
    "ds_read_b128 v[112:115], v127 offset:45216\n\t"
    "buffer_load_dwordx4 v1, s[12:15], s54 offen offset:3072 sc1 lds\n\t"
    "v_exp_f32 v78, v78\n\t"
    "v_exp_f32 v79, v79\n\t"
    "v_exp_f32 v80, v80\n\t"
    "v_exp_f32 v81, v81\n\t"
    "v_smfmac_f32_32x32x32_bf16 v[128:143], v[116:119], v[72:79], v124\n\t"
    "v_exp_f32 v82, v82\n\t"
    "v_exp_f32 v83, v83\n\t"
    "v_exp_f32 v84, v84\n\t"
    "v_exp_f32 v85, v85\n\t"
    "v_mfma_f32_32x32x16_bf16 v[32:47], v[96:99], v[16:19], v[32:47]\n\t"
    "s_add_u32 m0, m0, 0x4000\n\t"
    "s_add_u32 s54, s54, 0x4000\n\t"
    "buffer_load_dwordx4 v1, s[12:15], s54 offen sc1 lds\n\t"
    "v_exp_f32 v86, v86\n\t"
    "v_exp_f32 v87, v87\n\t"
    "v_exp_f32 v88, v88\n\t"
    "v_exp_f32 v89, v89\n\t"
    "v_smfmac_f32_32x32x32_bf16 v[144:159], v[108:111], v[80:87], v124\n\t"
    "v_exp_f32 v90, v90\n\t"
    "v_exp_f32 v91, v91\n\t"
    "v_exp_f32 v92, v92\n\t"
    "v_exp_f32 v93, v93\n\t"
    "v_mfma_f32_32x32x16_bf16 v[48:63], v[96:99], v[20:23], v[48:63]\n\t"
    "buffer_load_dwordx4 v1, s[12:15], s54 offen offset:1024 sc1 lds\n\t"
    "v_exp_f32 v94, v94\n\t"
    "v_exp_f32 v95, v95\n\t"
    "v_exp_f32 v32, v32\n\t"
    "v_exp_f32 v33, v33\n\t"
    "v_smfmac_f32_32x32x32_bf16 v[144:159], v[116:119], v[88:95], v124\n\t"
    "s_waitcnt lgkmcnt(0)\n\t"
    "ds_read_b128 v[96:99], v125 offset:47232\n\t"
    "v_exp_f32 v34, v34\n\t"
    "v_exp_f32 v35, v35\n\t"
    "v_exp_f32 v36, v36\n\t"
    "v_exp_f32 v37, v37\n\t"
    "v_mfma_f32_32x32x4_2b_bf16 v[64:95], v[120:121], v[24:25], 0 cbsz:1 abid:1\n\t"
    "ds_read_b128 v[108:111], v127 offset:45888\n\t"
    "v_exp_f32 v38, v38\n\t"
    "v_exp_f32 v39, v39\n\t"
    "v_exp_f32 v40, v40\n\t"
    "v_exp_f32 v41, v41\n\t"
    "v_exp_f32 v42, v42\n\t"
    "v_exp_f32 v43, v43\n\t"
    "v_exp_f32 v44, v44\n\t"
    "v_exp_f32 v45, v45\n\t"
    "v_smfmac_f32_32x32x32_bf16 v[128:143], v[104:107], v[32:39], v124\n\t"
    "ds_read_b128 v[116:119], v127 offset:46560\n\t"
    "buffer_load_dwordx4 v1, s[12:15], s54 offen offset:2048 sc1 lds\n\t"
    "v_exp_f32 v46, v46\n\t"
    "v_exp_f32 v47, v47\n\t"
    "v_exp_f32 v48, v48\n\t"
    "v_exp_f32 v49, v49\n\t"
    "v_smfmac_f32_32x32x32_bf16 v[128:143], v[112:115], v[40:47], v124\n\t"
    "ds_read_b64 v[122:123], v126 offset:49280\n\t"
    "v_exp_f32 v50, v50\n\t"
    "v_exp_f32 v51, v51\n\t"
    "v_exp_f32 v52, v52\n\t"
    "v_exp_f32 v53, v53\n\t"
    "v_mfma_f32_32x32x16_bf16 v[64:79], v[100:103], v[16:19], v[64:79]\n\t"
    "buffer_load_dwordx4 v1, s[12:15], s54 offen offset:3072 sc1 lds\n\t"
    "v_exp_f32 v54, v54\n\t"
    "v_exp_f32 v55, v55\n\t"
    "v_exp_f32 v56, v56\n\t"
    "v_exp_f32 v57, v57\n\t"
    "v_smfmac_f32_32x32x32_bf16 v[144:159], v[104:107], v[48:55], v124\n\t"
    "v_exp_f32 v58, v58\n\t"
    "v_exp_f32 v59, v59\n\t"
    "v_exp_f32 v60, v60\n\t"
    "v_exp_f32 v61, v61\n\t"
    "v_mfma_f32_32x32x16_bf16 v[80:95], v[100:103], v[20:23], v[80:95]\n\t"
    "s_mul_i32 m0, s33, 0x400\n\t"
    "s_add_u32 m0, m0, 0x27000\n\t"
    "s_add_u32 s50, m0, s45\n\t"
    "buffer_load_dwordx4 v1, s[12:15], s50 offen sc1 lds\n\t"
    "v_exp_f32 v62, v62\n\t"
    "v_exp_f32 v63, v63\n\t"
    "v_exp_f32 v64, v64\n\t"
    "v_exp_f32 v65, v65\n\t"
    "v_smfmac_f32_32x32x32_bf16 v[144:159], v[112:115], v[56:63], v124\n\t"
    "s_waitcnt lgkmcnt(0)\n\t"
    "ds_read_b128 v[100:103], v125 offset:48256\n\t"
    "v_exp_f32 v66, v66\n\t"
    "v_exp_f32 v67, v67\n\t"
    "v_exp_f32 v68, v68\n\t"
    "v_exp_f32 v69, v69\n\t"
    "v_mfma_f32_32x32x4_2b_bf16 v[32:63], v[122:123], v[24:25], 0 cbsz:1 abid:0\n\t"
    "ds_read_b128 v[104:107], v127 offset:49792\n\t"
    "v_exp_f32 v70, v70\n\t"
    "v_exp_f32 v71, v71\n\t"
    "v_exp_f32 v72, v72\n\t"
    "v_exp_f32 v73, v73\n\t"
    "v_exp_f32 v74, v74\n\t"
    "v_exp_f32 v75, v75\n\t"
    "v_exp_f32 v76, v76\n\t"
    "v_exp_f32 v77, v77\n\t"
    "v_smfmac_f32_32x32x32_bf16 v[128:143], v[108:111], v[64:71], v124\n\t"
    "ds_read_b128 v[112:115], v127 offset:50464\n\t"
    "s_mov_b32 exec_lo, 1\n\t"
    "s_mov_b32 exec_hi, 1\n\t"
    "buffer_store_dwordx4 v[220:223], v254, s[12:15], s90 offen sc1\n\t"
    "buffer_store_dwordx4 v[224:227], v254, s[12:15], s90 offen offset:16 sc1\n\t"
    "buffer_store_dwordx4 v[228:231], v255, s[12:15], s90 offen sc1\n\t"
    "s_mov_b64 exec, -1\n\t"
    "v_exp_f32 v78, v78\n\t"
    "v_exp_f32 v79, v79\n\t"
    "v_exp_f32 v80, v80\n\t"
    "v_exp_f32 v81, v81\n\t"
    "v_smfmac_f32_32x32x32_bf16 v[128:143], v[116:119], v[72:79], v124\n\t"
    "v_exp_f32 v82, v82\n\t"
    "v_exp_f32 v83, v83\n\t"
    "v_exp_f32 v84, v84\n\t"
    "v_exp_f32 v85, v85\n\t"
    "v_mfma_f32_32x32x16_bf16 v[32:47], v[96:99], v[16:19], v[32:47]\n\t"
    "v_exp_f32 v86, v86\n\t"
    "v_exp_f32 v87, v87\n\t"
    "v_exp_f32 v88, v88\n\t"
    "v_exp_f32 v89, v89\n\t"
    "v_smfmac_f32_32x32x32_bf16 v[144:159], v[108:111], v[80:87], v124\n\t"
    "v_exp_f32 v90, v90\n\t"
    "v_exp_f32 v91, v91\n\t"
    "v_exp_f32 v92, v92\n\t"
    "v_exp_f32 v93, v93\n\t"
    "v_mfma_f32_32x32x16_bf16 v[48:63], v[96:99], v[20:23], v[48:63]\n\t"
    "v_exp_f32 v94, v94\n\t"
    "v_exp_f32 v95, v95\n\t"
    "v_exp_f32 v32, v32\n\t"
    "v_exp_f32 v33, v33\n\t"
    "v_smfmac_f32_32x32x32_bf16 v[144:159], v[116:119], v[88:95], v124\n\t"
    "s_waitcnt lgkmcnt(0)\n\t"
    "ds_read_b128 v[96:99], v125 offset:52480\n\t"
    "v_exp_f32 v34, v34\n\t"
    "v_exp_f32 v35, v35\n\t"
    "v_exp_f32 v36, v36\n\t"
    "v_exp_f32 v37, v37\n\t"
    "v_mfma_f32_32x32x4_2b_bf16 v[64:95], v[122:123], v[24:25], 0 cbsz:1 abid:1\n\t"
    "ds_read_b128 v[108:111], v127 offset:51136\n\t"
    "v_exp_f32 v38, v38\n\t"
    "v_exp_f32 v39, v39\n\t"
    "v_exp_f32 v40, v40\n\t"
    "v_exp_f32 v41, v41\n\t"
    "v_exp_f32 v42, v42\n\t"
    "v_exp_f32 v43, v43\n\t"
    "v_exp_f32 v44, v44\n\t"
    "v_exp_f32 v45, v45\n\t"
    "v_smfmac_f32_32x32x32_bf16 v[128:143], v[104:107], v[32:39], v124\n\t"
    "ds_read_b128 v[116:119], v127 offset:51808\n\t"
    "v_exp_f32 v46, v46\n\t"
    "v_exp_f32 v47, v47\n\t"
    "v_exp_f32 v48, v48\n\t"
    "v_exp_f32 v49, v49\n\t"
    "v_smfmac_f32_32x32x32_bf16 v[128:143], v[112:115], v[40:47], v124\n\t"
    "ds_read_b64 v[120:121], v126 offset:54528\n\t"
    "v_exp_f32 v50, v50\n\t"
    "v_exp_f32 v51, v51\n\t"
    "v_exp_f32 v52, v52\n\t"
    "v_exp_f32 v53, v53\n\t"
    "v_mfma_f32_32x32x16_bf16 v[64:79], v[100:103], v[16:19], v[64:79]\n\t"
    "v_exp_f32 v54, v54\n\t"
    "v_exp_f32 v55, v55\n\t"
    "v_exp_f32 v56, v56\n\t"
    "v_exp_f32 v57, v57\n\t"
    "v_smfmac_f32_32x32x32_bf16 v[144:159], v[104:107], v[48:55], v124\n\t"
    "v_exp_f32 v58, v58\n\t"
    "v_exp_f32 v59, v59\n\t"
    "v_exp_f32 v60, v60\n\t"
    "v_exp_f32 v61, v61\n\t"
    "v_mfma_f32_32x32x16_bf16 v[80:95], v[100:103], v[20:23], v[80:95]\n\t"
    "v_exp_f32 v62, v62\n\t"
    "v_exp_f32 v63, v63\n\t"
    "s_cmp_eq_u32 s33, 0\n\t"
    "s_waitcnt vmcnt(12)\n\t"
    "s_barrier\n\t"
    "s_cbranch_scc0 L21_%=\n\t"
    "v_and_b32 v30, 7, v0\n\t"
    "v_lshlrev_b32 v30, 11, v30\n\t"
    "s_mul_i32 s50, s39, 8\n\t"
    "s_add_u32 s50, s50, 0x18000\n\t"
    "s_mov_b64 exec, 0xff\n\t"
    "buffer_store_dwordx2 v[2:3], v30, s[12:15], s50 offen sc1\n\t"
    "s_mov_b64 exec, -1\n\t"
    "s_cmp_eq_u32 s39, 0\n\t"
    "s_cbranch_scc0 L22_%=\n\t"
    "s_mov_b32 s50, 0x1c000\n\t"
    "s_mov_b64 exec, 1\n\t"
    "buffer_store_dwordx2 v[2:3], v1, s[12:15], s50 offen sc1\n\t"
    "s_mov_b64 exec, -1\n\t"
    "L22_%=:\n\t"
    "L21_%=:\n\t"
    "v_exp_f32 v64, v64\n\t"
    "v_exp_f32 v65, v65\n\t"
    "v_smfmac_f32_32x32x32_bf16 v[144:159], v[112:115], v[56:63], v124\n\t"
    "s_waitcnt lgkmcnt(0)\n\t"
    "ds_read_b128 v[100:103], v125 offset:53504\n\t"
    "v_exp_f32 v66, v66\n\t"
    "v_exp_f32 v67, v67\n\t"
    "v_exp_f32 v68, v68\n\t"
    "v_exp_f32 v69, v69\n\t"
    "v_mfma_f32_32x32x4_2b_bf16 v[32:63], v[120:121], v[24:25], 0 cbsz:1 abid:0\n\t"
    "ds_read_b128 v[104:107], v127 offset:55040\n\t"
    "v_exp_f32 v70, v70\n\t"
    "v_exp_f32 v71, v71\n\t"
    "v_exp_f32 v72, v72\n\t"
    "v_exp_f32 v73, v73\n\t"
    "v_exp_f32 v74, v74\n\t"
    "v_exp_f32 v75, v75\n\t"
    "v_exp_f32 v76, v76\n\t"
    "v_exp_f32 v77, v77\n\t"
    "v_smfmac_f32_32x32x32_bf16 v[128:143], v[108:111], v[64:71], v124\n\t"
    "ds_read_b128 v[112:115], v127 offset:55712\n\t"
    "v_exp_f32 v78, v78\n\t"
    "v_exp_f32 v79, v79\n\t"
    "v_exp_f32 v80, v80\n\t"
    "v_exp_f32 v81, v81\n\t"
    "v_smfmac_f32_32x32x32_bf16 v[128:143], v[116:119], v[72:79], v124\n\t"
    "v_exp_f32 v82, v82\n\t"
    "v_exp_f32 v83, v83\n\t"
    "v_exp_f32 v84, v84\n\t"
    "v_exp_f32 v85, v85\n\t"
    "v_mfma_f32_32x32x16_bf16 v[32:47], v[96:99], v[16:19], v[32:47]\n\t"
    "v_exp_f32 v86, v86\n\t"
    "v_exp_f32 v87, v87\n\t"
    "v_exp_f32 v88, v88\n\t"
    "v_exp_f32 v89, v89\n\t"
    "v_smfmac_f32_32x32x32_bf16 v[144:159], v[108:111], v[80:87], v124\n\t"
    "v_exp_f32 v90, v90\n\t"
    "v_exp_f32 v91, v91\n\t"
    "v_exp_f32 v92, v92\n\t"
    "v_exp_f32 v93, v93\n\t"
    "v_mfma_f32_32x32x16_bf16 v[48:63], v[96:99], v[20:23], v[48:63]\n\t"
    "v_exp_f32 v94, v94\n\t"
    "v_exp_f32 v95, v95\n\t"
    "v_exp_f32 v32, v32\n\t"
    "v_exp_f32 v33, v33\n\t"
    "v_smfmac_f32_32x32x32_bf16 v[144:159], v[116:119], v[88:95], v124\n\t"
    "s_waitcnt lgkmcnt(0)\n\t"
    "ds_read_b128 v[96:99], v125 offset:57728\n\t"
    "v_exp_f32 v34, v34\n\t"
    "v_exp_f32 v35, v35\n\t"
    "v_exp_f32 v36, v36\n\t"
    "v_exp_f32 v37, v37\n\t"
    "v_mfma_f32_32x32x4_2b_bf16 v[64:95], v[120:121], v[24:25], 0 cbsz:1 abid:1\n\t"
    "ds_read_b128 v[108:111], v127 offset:56384\n\t"
    "v_exp_f32 v38, v38\n\t"
    "v_exp_f32 v39, v39\n\t"
    "v_exp_f32 v40, v40\n\t"
    "v_exp_f32 v41, v41\n\t"
    "v_exp_f32 v42, v42\n\t"
    "v_exp_f32 v43, v43\n\t"
    "v_exp_f32 v44, v44\n\t"
    "v_exp_f32 v45, v45\n\t"
    "v_smfmac_f32_32x32x32_bf16 v[128:143], v[104:107], v[32:39], v124\n\t"
    "ds_read_b128 v[116:119], v127 offset:57056\n\t"
    "v_exp_f32 v46, v46\n\t"
    "v_exp_f32 v47, v47\n\t"
    "v_exp_f32 v48, v48\n\t"
    "v_exp_f32 v49, v49\n\t"
    "v_smfmac_f32_32x32x32_bf16 v[128:143], v[112:115], v[40:47], v124\n\t"
    "ds_read_b64 v[122:123], v126 offset:59776\n\t"
    "v_exp_f32 v50, v50\n\t"
    "v_exp_f32 v51, v51\n\t"
    "v_exp_f32 v52, v52\n\t"
    "v_exp_f32 v53, v53\n\t"
    "v_mfma_f32_32x32x16_bf16 v[64:79], v[100:103], v[16:19], v[64:79]\n\t"
    "v_exp_f32 v54, v54\n\t"
    "v_exp_f32 v55, v55\n\t"
    "v_exp_f32 v56, v56\n\t"
    "v_exp_f32 v57, v57\n\t"
    "v_smfmac_f32_32x32x32_bf16 v[144:159], v[104:107], v[48:55], v124\n\t"
    "v_exp_f32 v58, v58\n\t"
    "v_exp_f32 v59, v59\n\t"
    "v_exp_f32 v60, v60\n\t"
    "v_exp_f32 v61, v61\n\t"
    "v_mfma_f32_32x32x16_bf16 v[80:95], v[100:103], v[20:23], v[80:95]\n\t"
    "v_exp_f32 v62, v62\n\t"
    "v_exp_f32 v63, v63\n\t"
    "v_exp_f32 v64, v64\n\t"
    "v_exp_f32 v65, v65\n\t"
    "v_smfmac_f32_32x32x32_bf16 v[144:159], v[112:115], v[56:63], v124\n\t"
    "s_waitcnt lgkmcnt(0)\n\t"
    "ds_read_b128 v[100:103], v125 offset:58752\n\t"
    "v_exp_f32 v66, v66\n\t"
    "v_exp_f32 v67, v67\n\t"
    "v_exp_f32 v68, v68\n\t"
    "v_exp_f32 v69, v69\n\t"
    "v_mfma_f32_32x32x4_2b_bf16 v[32:63], v[122:123], v[24:25], 0 cbsz:1 abid:0\n\t"
    "ds_read_b128 v[104:107], v127 offset:60288\n\t"
    "v_exp_f32 v70, v70\n\t"
    "v_exp_f32 v71, v71\n\t"
    "v_exp_f32 v72, v72\n\t"
    "v_exp_f32 v73, v73\n\t"
    "v_exp_f32 v74, v74\n\t"
    "v_exp_f32 v75, v75\n\t"
    "v_exp_f32 v76, v76\n\t"
    "v_exp_f32 v77, v77\n\t"
    "v_smfmac_f32_32x32x32_bf16 v[128:143], v[108:111], v[64:71], v124\n\t"
    "ds_read_b128 v[112:115], v127 offset:60960\n\t"
    "v_exp_f32 v78, v78\n\t"
    "v_exp_f32 v79, v79\n\t"
    "v_exp_f32 v80, v80\n\t"
    "v_exp_f32 v81, v81\n\t"
    "v_smfmac_f32_32x32x32_bf16 v[128:143], v[116:119], v[72:79], v124\n\t"
    "v_exp_f32 v82, v82\n\t"
    "v_exp_f32 v83, v83\n\t"
    "v_exp_f32 v84, v84\n\t"
    "v_exp_f32 v85, v85\n\t"
    "v_mfma_f32_32x32x16_bf16 v[32:47], v[96:99], v[16:19], v[32:47]\n\t"
    "v_exp_f32 v86, v86\n\t"
    "v_exp_f32 v87, v87\n\t"
    "v_exp_f32 v88, v88\n\t"
    "v_exp_f32 v89, v89\n\t"
    "v_smfmac_f32_32x32x32_bf16 v[144:159], v[108:111], v[80:87], v124\n\t"
    "v_exp_f32 v90, v90\n\t"
    "v_exp_f32 v91, v91\n\t"
    "v_exp_f32 v92, v92\n\t"
    "v_exp_f32 v93, v93\n\t"
    "v_mfma_f32_32x32x16_bf16 v[48:63], v[96:99], v[20:23], v[48:63]\n\t"
    "v_exp_f32 v94, v94\n\t"
    "v_exp_f32 v95, v95\n\t"
    "v_exp_f32 v32, v32\n\t"
    "v_exp_f32 v33, v33\n\t"
    "v_smfmac_f32_32x32x32_bf16 v[144:159], v[116:119], v[88:95], v124\n\t"
    "s_waitcnt lgkmcnt(0)\n\t"
    "v_add_u32 v125, 0xf600, v125\n\t"
    "ds_read_b128 v[96:99], v125\n\t"
    "v_exp_f32 v34, v34\n\t"
    "v_exp_f32 v35, v35\n\t"
    "v_exp_f32 v36, v36\n\t"
    "v_exp_f32 v37, v37\n\t"
    "v_mfma_f32_32x32x4_2b_bf16 v[64:95], v[122:123], v[24:25], 0 cbsz:1 abid:1\n\t"
    "ds_read_b128 v[108:111], v127 offset:61632\n\t"
    "v_exp_f32 v38, v38\n\t"
    "v_exp_f32 v39, v39\n\t"
    "v_exp_f32 v40, v40\n\t"
    "v_exp_f32 v41, v41\n\t"
    "v_exp_f32 v42, v42\n\t"
    "v_exp_f32 v43, v43\n\t"
    "v_exp_f32 v44, v44\n\t"
    "v_exp_f32 v45, v45\n\t"
    "v_smfmac_f32_32x32x32_bf16 v[128:143], v[104:107], v[32:39], v124\n\t"
    "ds_read_b128 v[116:119], v127 offset:62304\n\t"
    "v_exp_f32 v46, v46\n\t"
    "v_exp_f32 v47, v47\n\t"
    "v_exp_f32 v48, v48\n\t"
    "v_exp_f32 v49, v49\n\t"
    "v_smfmac_f32_32x32x32_bf16 v[128:143], v[112:115], v[40:47], v124\n\t"
    "v_add_u32 v126, 0xf600, v126\n\t"
    "ds_read_b64 v[120:121], v126 offset:2048\n\t"
    "v_exp_f32 v50, v50\n\t"
    "v_exp_f32 v51, v51\n\t"
    "v_exp_f32 v52, v52\n\t"
    "v_exp_f32 v53, v53\n\t"
    "v_mfma_f32_32x32x16_bf16 v[64:79], v[100:103], v[16:19], v[64:79]\n\t"
    "v_exp_f32 v54, v54\n\t"
    "v_exp_f32 v55, v55\n\t"
    "v_exp_f32 v56, v56\n\t"
    "v_exp_f32 v57, v57\n\t"
    "v_smfmac_f32_32x32x32_bf16 v[144:159], v[104:107], v[48:55], v124\n\t"
    "v_exp_f32 v58, v58\n\t"
    "v_exp_f32 v59, v59\n\t"
    "v_exp_f32 v60, v60\n\t"
    "v_exp_f32 v61, v61\n\t"
    "v_mfma_f32_32x32x16_bf16 v[80:95], v[100:103], v[20:23], v[80:95]\n\t"
    "v_exp_f32 v62, v62\n\t"
    "v_exp_f32 v63, v63\n\t"
    "v_exp_f32 v64, v64\n\t"
    "v_exp_f32 v65, v65\n\t"
    "v_smfmac_f32_32x32x32_bf16 v[144:159], v[112:115], v[56:63], v124\n\t"
    "s_waitcnt lgkmcnt(0)\n\t"
    "ds_read_b128 v[100:103], v125 offset:1024\n\t"
    "v_exp_f32 v66, v66\n\t"
    "v_exp_f32 v67, v67\n\t"
    "v_exp_f32 v68, v68\n\t"
    "v_exp_f32 v69, v69\n\t"
    "v_mfma_f32_32x32x4_2b_bf16 v[32:63], v[120:121], v[24:25], 0 cbsz:1 abid:0\n\t"
    "v_add_u32 v127, 0xf600, v127\n\t"
    "ds_read_b128 v[104:107], v127 offset:2560\n\t"
    "v_exp_f32 v70, v70\n\t"
    "v_exp_f32 v71, v71\n\t"
    "v_exp_f32 v72, v72\n\t"
    "v_exp_f32 v73, v73\n\t"
    "v_exp_f32 v74, v74\n\t"
    "v_exp_f32 v75, v75\n\t"
    "v_exp_f32 v76, v76\n\t"
    "v_exp_f32 v77, v77\n\t"
    "v_smfmac_f32_32x32x32_bf16 v[128:143], v[108:111], v[64:71], v124\n\t"
    "ds_read_b128 v[112:115], v127 offset:3232\n\t"
    "v_exp_f32 v78, v78\n\t"
    "v_exp_f32 v79, v79\n\t"
    "v_exp_f32 v80, v80\n\t"
    "v_exp_f32 v81, v81\n\t"
    "v_smfmac_f32_32x32x32_bf16 v[128:143], v[116:119], v[72:79], v124\n\t"
    "v_exp_f32 v82, v82\n\t"
    "v_exp_f32 v83, v83\n\t"
    "v_exp_f32 v84, v84\n\t"
    "v_exp_f32 v85, v85\n\t"
    "v_mfma_f32_32x32x16_bf16 v[32:47], v[96:99], v[16:19], v[32:47]\n\t"
    "v_exp_f32 v86, v86\n\t"
    "v_exp_f32 v87, v87\n\t"
    "v_exp_f32 v88, v88\n\t"
    "v_exp_f32 v89, v89\n\t"
    "v_smfmac_f32_32x32x32_bf16 v[144:159], v[108:111], v[80:87], v124\n\t"
    "v_exp_f32 v90, v90\n\t"
    "v_exp_f32 v91, v91\n\t"
    "v_exp_f32 v92, v92\n\t"
    "v_exp_f32 v93, v93\n\t"
    "v_mfma_f32_32x32x16_bf16 v[48:63], v[96:99], v[20:23], v[48:63]\n\t"
    "v_exp_f32 v94, v94\n\t"
    "v_exp_f32 v95, v95\n\t"
    "s_and_b32 s50, s39, 7\n\t"
    "s_mul_i32 s56, s50, 0x800\n\t"
    "s_add_u32 s56, s56, 0x18000\n\t"
    "s_cmp_eq_u32 s33, 0\n\t"
    "s_cbranch_scc0 L23_%=\n\t"
    "buffer_load_dwordx4 v[168:171], v1, s[12:15], s56 offen sc1\n\t"
    "pad4_%=:\n\t"
    "s_cbranch_execz vb8_%=\n\t"
    "buffer_load_dwordx4 v[172:175], v1, s[12:15], s56 offen offset:1024 sc1\n\t"
    "L23_%=:\n\t"
    "v_exp_f32 v32, v32\n\t"
    "v_exp_f32 v33, v33\n\t"
    "v_smfmac_f32_32x32x32_bf16 v[144:159], v[116:119], v[88:95], v124\n\t"
    "s_waitcnt lgkmcnt(0)\n\t"
    "ds_read_b128 v[96:99], v125 offset:5248\n\t"
    "v_exp_f32 v34, v34\n\t"
    "v_exp_f32 v35, v35\n\t"
    "v_exp_f32 v36, v36\n\t"
    "v_exp_f32 v37, v37\n\t"
    "v_mfma_f32_32x32x4_2b_bf16 v[64:95], v[120:121], v[24:25], 0 cbsz:1 abid:1\n\t"
    "ds_read_b128 v[108:111], v127 offset:3904\n\t"
    "v_exp_f32 v38, v38\n\t"
    "v_exp_f32 v39, v39\n\t"
    "v_exp_f32 v40, v40\n\t"
    "v_exp_f32 v41, v41\n\t"
    "v_exp_f32 v42, v42\n\t"
    "v_exp_f32 v43, v43\n\t"
    "v_exp_f32 v44, v44\n\t"
    "v_exp_f32 v45, v45\n\t"
    "v_smfmac_f32_32x32x32_bf16 v[128:143], v[104:107], v[32:39], v124\n\t"
    "ds_read_b128 v[116:119], v127 offset:4576\n\t"
    "v_exp_f32 v46, v46\n\t"
    "v_exp_f32 v47, v47\n\t"
    "v_exp_f32 v48, v48\n\t"
    "v_exp_f32 v49, v49\n\t"
    "v_smfmac_f32_32x32x32_bf16 v[128:143], v[112:115], v[40:47], v124\n\t"
    "ds_read_b64 v[122:123], v126 offset:7296\n\t"
    "v_exp_f32 v50, v50\n\t"
    "v_exp_f32 v51, v51\n\t"
    "v_exp_f32 v52, v52\n\t"
    "v_exp_f32 v53, v53\n\t"
    "v_mfma_f32_32x32x16_bf16 v[64:79], v[100:103], v[16:19], v[64:79]\n\t"
    "v_exp_f32 v54, v54\n\t"
    "v_exp_f32 v55, v55\n\t"
    "v_exp_f32 v56, v56\n\t"
    "v_exp_f32 v57, v57\n\t"
    "v_smfmac_f32_32x32x32_bf16 v[144:159], v[104:107], v[48:55], v124\n\t"
    "v_exp_f32 v58, v58\n\t"
    "v_exp_f32 v59, v59\n\t"
    "v_exp_f32 v60, v60\n\t"
    "v_exp_f32 v61, v61\n\t"
    "v_mfma_f32_32x32x16_bf16 v[80:95], v[100:103], v[20:23], v[80:95]\n\t"
    "v_exp_f32 v62, v62\n\t"
    "v_exp_f32 v63, v63\n\t"
    "v_exp_f32 v64, v64\n\t"
    "v_exp_f32 v65, v65\n\t"
    "v_smfmac_f32_32x32x32_bf16 v[144:159], v[112:115], v[56:63], v124\n\t"
    "s_waitcnt lgkmcnt(0)\n\t"
    "ds_read_b128 v[100:103], v125 offset:6272\n\t"
    "v_exp_f32 v66, v66\n\t"
    "v_exp_f32 v67, v67\n\t"
    "v_exp_f32 v68, v68\n\t"
    "v_exp_f32 v69, v69\n\t"
    "v_mfma_f32_32x32x4_2b_bf16 v[32:63], v[122:123], v[24:25], 0 cbsz:1 abid:0\n\t"
    "ds_read_b128 v[104:107], v127 offset:7808\n\t"
    "v_exp_f32 v70, v70\n\t"
    "v_exp_f32 v71, v71\n\t"
    "v_exp_f32 v72, v72\n\t"
    "v_exp_f32 v73, v73\n\t"
    "v_exp_f32 v74, v74\n\t"
    "v_exp_f32 v75, v75\n\t"
    "v_exp_f32 v76, v76\n\t"
    "v_exp_f32 v77, v77\n\t"
    "v_smfmac_f32_32x32x32_bf16 v[128:143], v[108:111], v[64:71], v124\n\t"
    "ds_read_b128 v[112:115], v127 offset:8480\n\t"
    "v_exp_f32 v78, v78\n\t"
    "v_exp_f32 v79, v79\n\t"
    "v_exp_f32 v80, v80\n\t"
    "v_exp_f32 v81, v81\n\t"
    "v_smfmac_f32_32x32x32_bf16 v[128:143], v[116:119], v[72:79], v124\n\t"
    "v_exp_f32 v82, v82\n\t"
    "v_exp_f32 v83, v83\n\t"
    "v_exp_f32 v84, v84\n\t"
    "v_exp_f32 v85, v85\n\t"
    "v_mfma_f32_32x32x16_bf16 v[32:47], v[96:99], v[16:19], v[32:47]\n\t"
    "v_exp_f32 v86, v86\n\t"
    "v_exp_f32 v87, v87\n\t"
    "v_exp_f32 v88, v88\n\t"
    "v_exp_f32 v89, v89\n\t"
    "v_smfmac_f32_32x32x32_bf16 v[144:159], v[108:111], v[80:87], v124\n\t"
    "v_exp_f32 v90, v90\n\t"
    "v_exp_f32 v91, v91\n\t"
    "v_exp_f32 v92, v92\n\t"
    "v_exp_f32 v93, v93\n\t"
    "v_mfma_f32_32x32x16_bf16 v[48:63], v[96:99], v[20:23], v[48:63]\n\t"
    "v_exp_f32 v94, v94\n\t"
    "v_exp_f32 v95, v95\n\t"
    "s_cmp_eq_u32 s33, 0\n\t"
    "s_cbranch_scc0 L24_%=\n\t"
    "s_waitcnt vmcnt(0)\n\t"
    "v_cmp_ne_u64 vcc, v[2:3], v[168:169]\n\t"
    "v_cmp_ne_u64 s[42:43], v[2:3], v[170:171]\n\t"
    "s_nop 0\n\t"
    "s_or_b64 vcc, vcc, s[42:43]\n\t"
    "v_cmp_ne_u64 s[42:43], v[2:3], v[172:173]\n\t"
    "s_nop 0\n\t"
    "s_or_b64 vcc, vcc, s[42:43]\n\t"
    "v_cmp_ne_u64 s[42:43], v[2:3], v[174:175]\n\t"
    "s_nop 0\n\t"
    "s_or_b64 vcc, vcc, s[42:43]\n\t"
    "s_cbranch_vccz L25_%=\n\t"
    "poll26_%=:\n\t"
    "s_sleep 4\n\t"
    "buffer_load_dwordx4 v[168:171], v1, s[12:15], s56 offen sc1\n\t"
    "buffer_load_dwordx4 v[172:175], v1, s[12:15], s56 offen offset:1024 sc1\n\t"
    "s_waitcnt vmcnt(0)\n\t"
    "v_cmp_ne_u64 vcc, v[2:3], v[168:169]\n\t"
    "v_cmp_ne_u64 s[42:43], v[2:3], v[170:171]\n\t"
    "s_nop 0\n\t"
    "s_or_b64 vcc, vcc, s[42:43]\n\t"
    "v_cmp_ne_u64 s[42:43], v[2:3], v[172:173]\n\t"
    "s_nop 0\n\t"
    "s_or_b64 vcc, vcc, s[42:43]\n\t"
    "v_cmp_ne_u64 s[42:43], v[2:3], v[174:175]\n\t"
    "s_nop 0\n\t"
    "s_or_b64 vcc, vcc, s[42:43]\n\t"
    "s_cbranch_vccz L25_%=\n\t"
    "s_sub_u32 s37, s37, 1\n\t"
    "s_cbranch_scc0 poll26_%=\n\t"
    "s_mov_b32 s37, 0\n\t"
    "L25_%=:\n\t"
    "L24_%=:\n\t"
    "s_barrier\n\t"
    "layer27_%=:\n\t"
    "v_exp_f32 v32, v32\n\t"
    "v_exp_f32 v33, v33\n\t"
    "v_smfmac_f32_32x32x32_bf16 v[144:159], v[116:119], v[88:95], v124\n\t"
    "s_waitcnt lgkmcnt(0)\n\t"
    "ds_read_b128 v[96:99], v125 offset:10496\n\t"
    "v_exp_f32 v34, v34\n\t"
    "v_exp_f32 v35, v35\n\t"
    "v_exp_f32 v36, v36\n\t"
    "v_exp_f32 v37, v37\n\t"
    "v_mfma_f32_32x32x4_2b_bf16 v[64:95], v[122:123], v[24:25], 0 cbsz:1 abid:1\n\t"
    "ds_read_b128 v[108:111], v127 offset:9152\n\t"
    "v_exp_f32 v38, v38\n\t"
    "v_exp_f32 v39, v39\n\t"
    "v_exp_f32 v40, v40\n\t"
    "v_exp_f32 v41, v41\n\t"
    "v_exp_f32 v42, v42\n\t"
    "v_exp_f32 v43, v43\n\t"
    "v_exp_f32 v44, v44\n\t"
    "v_exp_f32 v45, v45\n\t"
    "v_smfmac_f32_32x32x32_bf16 v[128:143], v[104:107], v[32:39], v124\n\t"
    "ds_read_b128 v[116:119], v127 offset:9824\n\t"
    "s_mul_i32 s66, s34, 0x19000\n\t"
    "s_mul_i32 s32, s33, 0x6400\n\t"
    "s_add_u32 s66, s66, s32\n\t"
    "s_add_u32 s66, s66, 0x124000\n\t"
    "buffer_load_dwordx4 a[0:3], v1, s[12:15], s66 offen sc1\n\t"
    "v_exp_f32 v46, v46\n\t"
    "v_exp_f32 v47, v47\n\t"
    "v_exp_f32 v48, v48\n\t"
    "v_exp_f32 v49, v49\n\t"
    "v_smfmac_f32_32x32x32_bf16 v[128:143], v[112:115], v[40:47], v124\n\t"
    "ds_read_b64 v[120:121], v126 offset:12544\n\t"
    "v_exp_f32 v50, v50\n\t"
    "v_exp_f32 v51, v51\n\t"
    "v_exp_f32 v52, v52\n\t"
    "v_exp_f32 v53, v53\n\t"
    "v_mfma_f32_32x32x16_bf16 v[64:79], v[100:103], v[16:19], v[64:79]\n\t"
    "buffer_load_dwordx4 a[4:7], v1, s[12:15], s66 offen offset:1024 sc1\n\t"
    "v_exp_f32 v54, v54\n\t"
    "v_exp_f32 v55, v55\n\t"
    "v_exp_f32 v56, v56\n\t"
    "v_exp_f32 v57, v57\n\t"
    "v_smfmac_f32_32x32x32_bf16 v[144:159], v[104:107], v[48:55], v124\n\t"
    "v_exp_f32 v58, v58\n\t"
    "v_exp_f32 v59, v59\n\t"
    "v_exp_f32 v60, v60\n\t"
    "v_exp_f32 v61, v61\n\t"
    "v_mfma_f32_32x32x16_bf16 v[80:95], v[100:103], v[20:23], v[80:95]\n\t"
    "buffer_load_dwordx4 a[8:11], v1, s[12:15], s66 offen offset:2048 sc1\n\t"
    "v_exp_f32 v62, v62\n\t"
    "v_exp_f32 v63, v63\n\t"
    "v_exp_f32 v64, v64\n\t"
    "v_exp_f32 v65, v65\n\t"
    "v_smfmac_f32_32x32x32_bf16 v[144:159], v[112:115], v[56:63], v124\n\t"
    "s_waitcnt lgkmcnt(0)\n\t"
    "ds_read_b128 v[100:103], v125 offset:11520\n\t"
    "v_exp_f32 v66, v66\n\t"
    "v_exp_f32 v67, v67\n\t"
    "v_exp_f32 v68, v68\n\t"
    "v_exp_f32 v69, v69\n\t"
    "v_mfma_f32_32x32x4_2b_bf16 v[32:63], v[120:121], v[24:25], 0 cbsz:1 abid:0\n\t"
    "ds_read_b128 v[104:107], v127 offset:13056\n\t"
    "v_exp_f32 v70, v70\n\t"
    "v_exp_f32 v71, v71\n\t"
    "v_exp_f32 v72, v72\n\t"
    "v_exp_f32 v73, v73\n\t"
    "v_exp_f32 v74, v74\n\t"
    "v_exp_f32 v75, v75\n\t"
    "v_exp_f32 v76, v76\n\t"
    "v_exp_f32 v77, v77\n\t"
    "v_smfmac_f32_32x32x32_bf16 v[128:143], v[108:111], v[64:71], v124\n\t"
    "ds_read_b128 v[112:115], v127 offset:13728\n\t"
    "v_exp_f32 v78, v78\n\t"
    "v_exp_f32 v79, v79\n\t"
    "v_exp_f32 v80, v80\n\t"
    "v_exp_f32 v81, v81\n\t"
    "v_smfmac_f32_32x32x32_bf16 v[128:143], v[116:119], v[72:79], v124\n\t"
    "v_exp_f32 v82, v82\n\t"
    "v_exp_f32 v83, v83\n\t"
    "v_exp_f32 v84, v84\n\t"
    "v_exp_f32 v85, v85\n\t"
    "v_mfma_f32_32x32x16_bf16 v[32:47], v[96:99], v[16:19], v[32:47]\n\t"
    "buffer_load_dwordx4 a[12:15], v1, s[12:15], s66 offen offset:3072 sc1\n\t"
    "v_exp_f32 v86, v86\n\t"
    "v_exp_f32 v87, v87\n\t"
    "v_exp_f32 v88, v88\n\t"
    "v_exp_f32 v89, v89\n\t"
    "v_smfmac_f32_32x32x32_bf16 v[144:159], v[108:111], v[80:87], v124\n\t"
    "v_exp_f32 v90, v90\n\t"
    "v_exp_f32 v91, v91\n\t"
    "v_exp_f32 v92, v92\n\t"
    "v_exp_f32 v93, v93\n\t"
    "v_mfma_f32_32x32x16_bf16 v[48:63], v[96:99], v[20:23], v[48:63]\n\t"
    "s_add_u32 s66, s66, 0x1000\n\t"
    "buffer_load_dwordx4 a[16:19], v1, s[12:15], s66 offen sc1\n\t"
    "v_exp_f32 v94, v94\n\t"
    "v_exp_f32 v95, v95\n\t"
    "v_exp_f32 v32, v32\n\t"
    "v_exp_f32 v33, v33\n\t"
    "v_smfmac_f32_32x32x32_bf16 v[144:159], v[116:119], v[88:95], v124\n\t"
    "s_waitcnt lgkmcnt(0)\n\t"
    "ds_read_b128 v[96:99], v125 offset:15744\n\t"
    "v_exp_f32 v34, v34\n\t"
    "v_exp_f32 v35, v35\n\t"
    "v_exp_f32 v36, v36\n\t"
    "v_exp_f32 v37, v37\n\t"
    "v_mfma_f32_32x32x4_2b_bf16 v[64:95], v[120:121], v[24:25], 0 cbsz:1 abid:1\n\t"
    "ds_read_b128 v[108:111], v127 offset:14400\n\t"
    "v_exp_f32 v38, v38\n\t"
    "v_exp_f32 v39, v39\n\t"
    "v_exp_f32 v40, v40\n\t"
    "v_exp_f32 v41, v41\n\t"
    "v_exp_f32 v42, v42\n\t"
    "v_exp_f32 v43, v43\n\t"
    "v_exp_f32 v44, v44\n\t"
    "v_exp_f32 v45, v45\n\t"
    "v_smfmac_f32_32x32x32_bf16 v[128:143], v[104:107], v[32:39], v124\n\t"
    "ds_read_b128 v[116:119], v127 offset:15072\n\t"
    "buffer_load_dwordx4 a[20:23], v1, s[12:15], s66 offen offset:1024 sc1\n\t"
    "v_exp_f32 v46, v46\n\t"
    "v_exp_f32 v47, v47\n\t"
    "v_exp_f32 v48, v48\n\t"
    "v_exp_f32 v49, v49\n\t"
    "v_smfmac_f32_32x32x32_bf16 v[128:143], v[112:115], v[40:47], v124\n\t"
    "ds_read_b64 v[122:123], v126 offset:17792\n\t"
    "v_exp_f32 v50, v50\n\t"
    "v_exp_f32 v51, v51\n\t"
    "v_exp_f32 v52, v52\n\t"
    "v_exp_f32 v53, v53\n\t"
    "v_mfma_f32_32x32x16_bf16 v[64:79], v[100:103], v[16:19], v[64:79]\n\t"
    "buffer_load_dwordx4 a[24:27], v1, s[12:15], s66 offen offset:2048 sc1\n\t"
    "v_exp_f32 v54, v54\n\t"
    "v_exp_f32 v55, v55\n\t"
    "v_exp_f32 v56, v56\n\t"
    "v_exp_f32 v57, v57\n\t"
    "v_smfmac_f32_32x32x32_bf16 v[144:159], v[104:107], v[48:55], v124\n\t"
    "v_exp_f32 v58, v58\n\t"
    "v_exp_f32 v59, v59\n\t"
    "v_exp_f32 v60, v60\n\t"
    "v_exp_f32 v61, v61\n\t"
    "v_mfma_f32_32x32x16_bf16 v[80:95], v[100:103], v[20:23], v[80:95]\n\t"
    "buffer_load_dwordx4 a[28:31], v1, s[12:15], s66 offen offset:3072 sc1\n\t"
    "v_exp_f32 v62, v62\n\t"
    "v_exp_f32 v63, v63\n\t"
    "v_exp_f32 v64, v64\n\t"
    "v_exp_f32 v65, v65\n\t"
    "v_smfmac_f32_32x32x32_bf16 v[144:159], v[112:115], v[56:63], v124\n\t"
    "s_waitcnt lgkmcnt(0)\n\t"
    "ds_read_b128 v[100:103], v125 offset:16768\n\t"
    "v_exp_f32 v66, v66\n\t"
    "v_exp_f32 v67, v67\n\t"
    "v_exp_f32 v68, v68\n\t"
    "v_exp_f32 v69, v69\n\t"
    "v_mfma_f32_32x32x4_2b_bf16 v[32:63], v[122:123], v[24:25], 0 cbsz:1 abid:0\n\t"
    "ds_read_b128 v[104:107], v127 offset:18304\n\t"
    "v_exp_f32 v70, v70\n\t"
    "v_exp_f32 v71, v71\n\t"
    "v_exp_f32 v72, v72\n\t"
    "v_exp_f32 v73, v73\n\t"
    "v_exp_f32 v74, v74\n\t"
    "v_exp_f32 v75, v75\n\t"
    "v_exp_f32 v76, v76\n\t"
    "v_exp_f32 v77, v77\n\t"
    "v_smfmac_f32_32x32x32_bf16 v[128:143], v[108:111], v[64:71], v124\n\t"
    "ds_read_b128 v[112:115], v127 offset:18976\n\t"
    "s_add_u32 s66, s66, 0x1000\n\t"
    "buffer_load_dwordx4 a[32:35], v1, s[12:15], s66 offen sc1\n\t"
    "v_exp_f32 v78, v78\n\t"
    "v_exp_f32 v79, v79\n\t"
    "v_exp_f32 v80, v80\n\t"
    "v_exp_f32 v81, v81\n\t"
    "v_smfmac_f32_32x32x32_bf16 v[128:143], v[116:119], v[72:79], v124\n\t"
    "v_exp_f32 v82, v82\n\t"
    "v_exp_f32 v83, v83\n\t"
    "v_exp_f32 v84, v84\n\t"
    "v_exp_f32 v85, v85\n\t"
    "v_mfma_f32_32x32x16_bf16 v[32:47], v[96:99], v[16:19], v[32:47]\n\t"
    "buffer_load_dwordx4 a[36:39], v1, s[12:15], s66 offen offset:1024 sc1\n\t"
    "v_exp_f32 v86, v86\n\t"
    "v_exp_f32 v87, v87\n\t"
    "v_exp_f32 v88, v88\n\t"
    "v_exp_f32 v89, v89\n\t"
    "v_smfmac_f32_32x32x32_bf16 v[144:159], v[108:111], v[80:87], v124\n\t"
    "v_exp_f32 v90, v90\n\t"
    "v_exp_f32 v91, v91\n\t"
    "v_exp_f32 v92, v92\n\t"
    "v_exp_f32 v93, v93\n\t"
    "v_mfma_f32_32x32x16_bf16 v[48:63], v[96:99], v[20:23], v[48:63]\n\t"
    "buffer_load_dwordx4 a[40:43], v1, s[12:15], s66 offen offset:2048 sc1\n\t"
    "v_exp_f32 v94, v94\n\t"
    "v_exp_f32 v95, v95\n\t"
    "v_exp_f32 v32, v32\n\t"
    "v_exp_f32 v33, v33\n\t"
    "v_smfmac_f32_32x32x32_bf16 v[144:159], v[116:119], v[88:95], v124\n\t"
    "s_waitcnt lgkmcnt(0)\n\t"
    "ds_read_b128 v[96:99], v125 offset:20992\n\t"
    "v_exp_f32 v34, v34\n\t"
    "v_exp_f32 v35, v35\n\t"
    "v_exp_f32 v36, v36\n\t"
    "v_exp_f32 v37, v37\n\t"
    "v_mfma_f32_32x32x4_2b_bf16 v[64:95], v[122:123], v[24:25], 0 cbsz:1 abid:1\n\t"
    "ds_read_b128 v[108:111], v127 offset:19648\n\t"
    "v_exp_f32 v38, v38\n\t"
    "v_exp_f32 v39, v39\n\t"
    "v_exp_f32 v40, v40\n\t"
    "v_exp_f32 v41, v41\n\t"
    "v_exp_f32 v42, v42\n\t"
    "v_exp_f32 v43, v43\n\t"
    "v_exp_f32 v44, v44\n\t"
    "v_exp_f32 v45, v45\n\t"
    "v_smfmac_f32_32x32x32_bf16 v[128:143], v[104:107], v[32:39], v124\n\t"
    "ds_read_b128 v[116:119], v127 offset:20320\n\t"
    "buffer_load_dwordx4 a[44:47], v1, s[12:15], s66 offen offset:3072 sc1\n\t"
    "v_exp_f32 v46, v46\n\t"
    "v_exp_f32 v47, v47\n\t"
    "v_exp_f32 v48, v48\n\t"
    "v_exp_f32 v49, v49\n\t"
    "v_smfmac_f32_32x32x32_bf16 v[128:143], v[112:115], v[40:47], v124\n\t"
    "ds_read_b64 v[120:121], v126 offset:23040\n\t"
    "v_exp_f32 v50, v50\n\t"
    "v_exp_f32 v51, v51\n\t"
    "v_exp_f32 v52, v52\n\t"
    "v_exp_f32 v53, v53\n\t"
    "v_mfma_f32_32x32x16_bf16 v[64:79], v[100:103], v[16:19], v[64:79]\n\t"
    "s_add_u32 s66, s66, 0x1000\n\t"
    "buffer_load_dwordx4 a[48:51], v1, s[12:15], s66 offen sc1\n\t"
    "v_exp_f32 v54, v54\n\t"
    "v_exp_f32 v55, v55\n\t"
    "v_exp_f32 v56, v56\n\t"
    "v_exp_f32 v57, v57\n\t"
    "v_smfmac_f32_32x32x32_bf16 v[144:159], v[104:107], v[48:55], v124\n\t"
    "v_exp_f32 v58, v58\n\t"
    "v_exp_f32 v59, v59\n\t"
    "v_exp_f32 v60, v60\n\t"
    "v_exp_f32 v61, v61\n\t"
    "v_mfma_f32_32x32x16_bf16 v[80:95], v[100:103], v[20:23], v[80:95]\n\t"
    "buffer_load_dwordx4 a[52:55], v1, s[12:15], s66 offen offset:1024 sc1\n\t"
    "v_exp_f32 v62, v62\n\t"
    "v_exp_f32 v63, v63\n\t"
    "v_exp_f32 v64, v64\n\t"
    "v_exp_f32 v65, v65\n\t"
    "v_smfmac_f32_32x32x32_bf16 v[144:159], v[112:115], v[56:63], v124\n\t"
    "s_waitcnt lgkmcnt(0)\n\t"
    "ds_read_b128 v[100:103], v125 offset:22016\n\t"
    "v_exp_f32 v66, v66\n\t"
    "v_exp_f32 v67, v67\n\t"
    "v_exp_f32 v68, v68\n\t"
    "v_exp_f32 v69, v69\n\t"
    "v_mfma_f32_32x32x4_2b_bf16 v[32:63], v[120:121], v[24:25], 0 cbsz:1 abid:0\n\t"
    "ds_read_b128 v[104:107], v127 offset:23552\n\t"
    "v_exp_f32 v70, v70\n\t"
    "v_exp_f32 v71, v71\n\t"
    "v_exp_f32 v72, v72\n\t"
    "v_exp_f32 v73, v73\n\t"
    "v_exp_f32 v74, v74\n\t"
    "v_exp_f32 v75, v75\n\t"
    "v_exp_f32 v76, v76\n\t"
    "v_exp_f32 v77, v77\n\t"
    "v_smfmac_f32_32x32x32_bf16 v[128:143], v[108:111], v[64:71], v124\n\t"
    "ds_read_b128 v[112:115], v127 offset:24224\n\t"
    "buffer_load_dwordx4 a[56:59], v1, s[12:15], s66 offen offset:2048 sc1\n\t"
    "v_exp_f32 v78, v78\n\t"
    "v_exp_f32 v79, v79\n\t"
    "v_exp_f32 v80, v80\n\t"
    "v_exp_f32 v81, v81\n\t"
    "v_smfmac_f32_32x32x32_bf16 v[128:143], v[116:119], v[72:79], v124\n\t"
    "v_exp_f32 v82, v82\n\t"
    "v_exp_f32 v83, v83\n\t"
    "v_exp_f32 v84, v84\n\t"
    "v_exp_f32 v85, v85\n\t"
    "v_mfma_f32_32x32x16_bf16 v[32:47], v[96:99], v[16:19], v[32:47]\n\t"
    "buffer_load_dwordx4 a[60:63], v1, s[12:15], s66 offen offset:3072 sc1\n\t"
    "v_exp_f32 v86, v86\n\t"
    "v_exp_f32 v87, v87\n\t"
    "v_exp_f32 v88, v88\n\t"
    "v_exp_f32 v89, v89\n\t"
    "v_smfmac_f32_32x32x32_bf16 v[144:159], v[108:111], v[80:87], v124\n\t"
    "v_exp_f32 v90, v90\n\t"
    "v_exp_f32 v91, v91\n\t"
    "v_exp_f32 v92, v92\n\t"
    "v_exp_f32 v93, v93\n\t"
    "v_mfma_f32_32x32x16_bf16 v[48:63], v[96:99], v[20:23], v[48:63]\n\t"
    "s_add_u32 s66, s66, 0x1000\n\t"
    "buffer_load_dwordx4 a[64:67], v1, s[12:15], s66 offen sc1\n\t"
    "v_exp_f32 v94, v94\n\t"
    "v_exp_f32 v95, v95\n\t"
    "v_exp_f32 v32, v32\n\t"
    "v_exp_f32 v33, v33\n\t"
    "v_smfmac_f32_32x32x32_bf16 v[144:159], v[116:119], v[88:95], v124\n\t"
    "s_waitcnt lgkmcnt(0)\n\t"
    "ds_read_b128 v[96:99], v125 offset:26240\n\t"
    "v_exp_f32 v34, v34\n\t"
    "v_exp_f32 v35, v35\n\t"
    "v_exp_f32 v36, v36\n\t"
    "v_exp_f32 v37, v37\n\t"
    "v_mfma_f32_32x32x4_2b_bf16 v[64:95], v[120:121], v[24:25], 0 cbsz:1 abid:1\n\t"
    "ds_read_b128 v[108:111], v127 offset:24896\n\t"
    "v_exp_f32 v38, v38\n\t"
    "v_exp_f32 v39, v39\n\t"
    "v_exp_f32 v40, v40\n\t"
    "v_exp_f32 v41, v41\n\t"
    "v_exp_f32 v42, v42\n\t"
    "v_exp_f32 v43, v43\n\t"
    "v_exp_f32 v44, v44\n\t"
    "v_exp_f32 v45, v45\n\t"
    "v_smfmac_f32_32x32x32_bf16 v[128:143], v[104:107], v[32:39], v124\n\t"
    "ds_read_b128 v[116:119], v127 offset:25568\n\t"
    "buffer_load_dwordx4 a[68:71], v1, s[12:15], s66 offen offset:1024 sc1\n\t"
    "v_exp_f32 v46, v46\n\t"
    "v_exp_f32 v47, v47\n\t"
    "v_exp_f32 v48, v48\n\t"
    "v_exp_f32 v49, v49\n\t"
    "v_smfmac_f32_32x32x32_bf16 v[128:143], v[112:115], v[40:47], v124\n\t"
    "ds_read_b64 v[122:123], v126 offset:28288\n\t"
    "v_exp_f32 v50, v50\n\t"
    "v_exp_f32 v51, v51\n\t"
    "v_exp_f32 v52, v52\n\t"
    "v_exp_f32 v53, v53\n\t"
    "v_mfma_f32_32x32x16_bf16 v[64:79], v[100:103], v[16:19], v[64:79]\n\t"
    "buffer_load_dwordx4 a[72:75], v1, s[12:15], s66 offen offset:2048 sc1\n\t"
    "v_exp_f32 v54, v54\n\t"
    "v_exp_f32 v55, v55\n\t"
    "v_exp_f32 v56, v56\n\t"
    "v_exp_f32 v57, v57\n\t"
    "v_smfmac_f32_32x32x32_bf16 v[144:159], v[104:107], v[48:55], v124\n\t"
    "v_exp_f32 v58, v58\n\t"
    "v_exp_f32 v59, v59\n\t"
    "v_exp_f32 v60, v60\n\t"
    "v_exp_f32 v61, v61\n\t"
    "v_mfma_f32_32x32x16_bf16 v[80:95], v[100:103], v[20:23], v[80:95]\n\t"
    "buffer_load_dwordx4 a[76:79], v1, s[12:15], s66 offen offset:3072 sc1\n\t"
    "v_exp_f32 v62, v62\n\t"
    "v_exp_f32 v63, v63\n\t"
    "v_exp_f32 v64, v64\n\t"
    "v_exp_f32 v65, v65\n\t"
    "v_smfmac_f32_32x32x32_bf16 v[144:159], v[112:115], v[56:63], v124\n\t"
    "s_waitcnt lgkmcnt(0)\n\t"
    "ds_read_b128 v[100:103], v125 offset:27264\n\t"
    "v_exp_f32 v66, v66\n\t"
    "v_exp_f32 v67, v67\n\t"
    "v_exp_f32 v68, v68\n\t"
    "v_exp_f32 v69, v69\n\t"
    "v_mfma_f32_32x32x4_2b_bf16 v[32:63], v[122:123], v[24:25], 0 cbsz:1 abid:0\n\t"
    "ds_read_b128 v[104:107], v127 offset:28800\n\t"
    "v_exp_f32 v70, v70\n\t"
    "v_exp_f32 v71, v71\n\t"
    "v_exp_f32 v72, v72\n\t"
    "v_exp_f32 v73, v73\n\t"
    "v_exp_f32 v74, v74\n\t"
    "v_exp_f32 v75, v75\n\t"
    "v_exp_f32 v76, v76\n\t"
    "v_exp_f32 v77, v77\n\t"
    "v_smfmac_f32_32x32x32_bf16 v[128:143], v[108:111], v[64:71], v124\n\t"
    "ds_read_b128 v[112:115], v127 offset:29472\n\t"
    "s_add_u32 s66, s66, 0x1000\n\t"
    "buffer_load_dwordx4 a[80:83], v1, s[12:15], s66 offen sc1\n\t"
    "v_exp_f32 v78, v78\n\t"
    "v_exp_f32 v79, v79\n\t"
    "v_exp_f32 v80, v80\n\t"
    "v_exp_f32 v81, v81\n\t"
    "v_smfmac_f32_32x32x32_bf16 v[128:143], v[116:119], v[72:79], v124\n\t"
    "v_exp_f32 v82, v82\n\t"
    "v_exp_f32 v83, v83\n\t"
    "v_exp_f32 v84, v84\n\t"
    "v_exp_f32 v85, v85\n\t"
    "v_mfma_f32_32x32x16_bf16 v[32:47], v[96:99], v[16:19], v[32:47]\n\t"
    "buffer_load_dwordx4 a[84:87], v1, s[12:15], s66 offen offset:1024 sc1\n\t"
    "v_exp_f32 v86, v86\n\t"
    "v_exp_f32 v87, v87\n\t"
    "v_exp_f32 v88, v88\n\t"
    "v_exp_f32 v89, v89\n\t"
    "v_smfmac_f32_32x32x32_bf16 v[144:159], v[108:111], v[80:87], v124\n\t"
    "v_exp_f32 v90, v90\n\t"
    "v_exp_f32 v91, v91\n\t"
    "v_exp_f32 v92, v92\n\t"
    "v_exp_f32 v93, v93\n\t"
    "v_mfma_f32_32x32x16_bf16 v[48:63], v[96:99], v[20:23], v[48:63]\n\t"
    "buffer_load_dwordx4 a[88:91], v1, s[12:15], s66 offen offset:2048 sc1\n\t"
    "v_exp_f32 v94, v94\n\t"
    "v_exp_f32 v95, v95\n\t"
    "v_exp_f32 v32, v32\n\t"
    "v_exp_f32 v33, v33\n\t"
    "v_smfmac_f32_32x32x32_bf16 v[144:159], v[116:119], v[88:95], v124\n\t"
    "s_waitcnt lgkmcnt(0)\n\t"
    "ds_read_b128 v[96:99], v125 offset:31488\n\t"
    "v_exp_f32 v34, v34\n\t"
    "v_exp_f32 v35, v35\n\t"
    "v_exp_f32 v36, v36\n\t"
    "v_exp_f32 v37, v37\n\t"
    "v_mfma_f32_32x32x4_2b_bf16 v[64:95], v[122:123], v[24:25], 0 cbsz:1 abid:1\n\t"
    "ds_read_b128 v[108:111], v127 offset:30144\n\t"
    "v_exp_f32 v38, v38\n\t"
    "v_exp_f32 v39, v39\n\t"
    "v_exp_f32 v40, v40\n\t"
    "v_exp_f32 v41, v41\n\t"
    "v_exp_f32 v42, v42\n\t"
    "v_exp_f32 v43, v43\n\t"
    "v_exp_f32 v44, v44\n\t"
    "v_exp_f32 v45, v45\n\t"
    "v_smfmac_f32_32x32x32_bf16 v[128:143], v[104:107], v[32:39], v124\n\t"
    "ds_read_b128 v[116:119], v127 offset:30816\n\t"
    "buffer_load_dwordx4 a[92:95], v1, s[12:15], s66 offen offset:3072 sc1\n\t"
    "v_exp_f32 v46, v46\n\t"
    "v_exp_f32 v47, v47\n\t"
    "v_exp_f32 v48, v48\n\t"
    "v_exp_f32 v49, v49\n\t"
    "v_smfmac_f32_32x32x32_bf16 v[128:143], v[112:115], v[40:47], v124\n\t"
    "ds_read_b64 v[120:121], v126 offset:33536\n\t"
    "v_exp_f32 v50, v50\n\t"
    "v_exp_f32 v51, v51\n\t"
    "v_exp_f32 v52, v52\n\t"
    "v_exp_f32 v53, v53\n\t"
    "v_mfma_f32_32x32x16_bf16 v[64:79], v[100:103], v[16:19], v[64:79]\n\t"
    "s_add_u32 s66, s66, 0x1000\n\t"
    "buffer_load_dwordx4 a[96:99], v1, s[12:15], s66 offen sc1\n\t"
    "v_exp_f32 v54, v54\n\t"
    "v_exp_f32 v55, v55\n\t"
    "v_exp_f32 v56, v56\n\t"
    "v_exp_f32 v57, v57\n\t"
    "v_smfmac_f32_32x32x32_bf16 v[144:159], v[104:107], v[48:55], v124\n\t"
    "v_exp_f32 v58, v58\n\t"
    "v_exp_f32 v59, v59\n\t"
    "v_exp_f32 v60, v60\n\t"
    "v_exp_f32 v61, v61\n\t"
    "v_mfma_f32_32x32x16_bf16 v[80:95], v[100:103], v[20:23], v[80:95]\n\t"
    "s_mul_i32 s66, s34, 0xa000\n\t"
    "s_mul_i32 s32, s82, 0xa000\n\t"
    "s_add_u32 s66, s66, s32\n\t"
    "s_add_u32 s66, s66, 0x1ec000\n\t"
    "buffer_load_dwordx4 a[100:103], v1, s[12:15], s66 offen sc1\n\t"
    "v_exp_f32 v62, v62\n\t"
    "v_exp_f32 v63, v63\n\t"
    "s_waitcnt vmcnt(26)\n\t"
    "s_barrier\n\t"
    "s_mul_i32 m0, s33, 0x800\n\t"
    "s_add_u32 m0, m0, 0x13380\n\t"
    "s_add_u32 s50, m0, s45\n\t"
    "s_add_u32 s50, s50, 0x14800\n\t"
    "buffer_load_dwordx4 v1, s[12:15], s50 offen sc1 lds\n\t"
    "buffer_load_dwordx4 v1, s[12:15], s50 offen offset:1024 sc1 lds\n\t"
    "v_exp_f32 v64, v64\n\t"
    "v_exp_f32 v65, v65\n\t"
    "v_smfmac_f32_32x32x32_bf16 v[144:159], v[112:115], v[56:63], v124\n\t"
    "s_waitcnt lgkmcnt(0)\n\t"
    "ds_read_b128 v[100:103], v125 offset:32512\n\t"
    "v_exp_f32 v66, v66\n\t"
    "v_exp_f32 v67, v67\n\t"
    "v_exp_f32 v68, v68\n\t"
    "v_exp_f32 v69, v69\n\t"
    "v_mfma_f32_32x32x4_2b_bf16 v[32:63], v[120:121], v[24:25], 0 cbsz:1 abid:0\n\t"
    "ds_read_b128 v[104:107], v127 offset:34048\n\t"
    "v_exp_f32 v70, v70\n\t"
    "v_exp_f32 v71, v71\n\t"
    "v_exp_f32 v72, v72\n\t"
    "v_exp_f32 v73, v73\n\t"
    "v_exp_f32 v74, v74\n\t"
    "v_exp_f32 v75, v75\n\t"
    "v_exp_f32 v76, v76\n\t"
    "v_exp_f32 v77, v77\n\t"
    "v_smfmac_f32_32x32x32_bf16 v[128:143], v[108:111], v[64:71], v124\n\t"
    "ds_read_b128 v[112:115], v127 offset:34720\n\t"
    "buffer_load_dwordx4 a[104:107], v1, s[12:15], s66 offen offset:1024 sc1\n\t"
    "v_exp_f32 v78, v78\n\t"
    "v_exp_f32 v79, v79\n\t"
    "v_exp_f32 v80, v80\n\t"
    "v_exp_f32 v81, v81\n\t"
    "v_smfmac_f32_32x32x32_bf16 v[128:143], v[116:119], v[72:79], v124\n\t"
    "v_exp_f32 v82, v82\n\t"
    "v_exp_f32 v83, v83\n\t"
    "v_exp_f32 v84, v84\n\t"
    "v_exp_f32 v85, v85\n\t"
    "v_mfma_f32_32x32x16_bf16 v[32:47], v[96:99], v[16:19], v[32:47]\n\t"
    "buffer_load_dwordx4 a[108:111], v1, s[12:15], s66 offen offset:2048 sc1\n\t"
    "v_exp_f32 v86, v86\n\t"
    "v_exp_f32 v87, v87\n\t"
    "v_exp_f32 v88, v88\n\t"
    "v_exp_f32 v89, v89\n\t"
    "v_smfmac_f32_32x32x32_bf16 v[144:159], v[108:111], v[80:87], v124\n\t"
    "v_exp_f32 v90, v90\n\t"
    "v_exp_f32 v91, v91\n\t"
    "v_exp_f32 v92, v92\n\t"
    "v_exp_f32 v93, v93\n\t"
    "v_mfma_f32_32x32x16_bf16 v[48:63], v[96:99], v[20:23], v[48:63]\n\t"
    "buffer_load_dwordx4 a[112:115], v1, s[12:15], s66 offen offset:3072 sc1\n\t"
    "v_exp_f32 v94, v94\n\t"
    "v_exp_f32 v95, v95\n\t"
    "v_exp_f32 v32, v32\n\t"
    "v_exp_f32 v33, v33\n\t"
    "v_smfmac_f32_32x32x32_bf16 v[144:159], v[116:119], v[88:95], v124\n\t"
    "s_waitcnt lgkmcnt(0)\n\t"
    "ds_read_b128 v[96:99], v125 offset:36736\n\t"
    "v_exp_f32 v34, v34\n\t"
    "v_exp_f32 v35, v35\n\t"
    "v_exp_f32 v36, v36\n\t"
    "v_exp_f32 v37, v37\n\t"
    "v_mfma_f32_32x32x4_2b_bf16 v[64:95], v[120:121], v[24:25], 0 cbsz:1 abid:1\n\t"
    "ds_read_b128 v[108:111], v127 offset:35392\n\t"
    "v_exp_f32 v38, v38\n\t"
    "v_exp_f32 v39, v39\n\t"
    "v_exp_f32 v40, v40\n\t"
    "v_exp_f32 v41, v41\n\t"
    "v_exp_f32 v42, v42\n\t"
    "v_exp_f32 v43, v43\n\t"
    "v_exp_f32 v44, v44\n\t"
    "v_exp_f32 v45, v45\n\t"
    "v_smfmac_f32_32x32x32_bf16 v[128:143], v[104:107], v[32:39], v124\n\t"
    "ds_read_b128 v[116:119], v127 offset:36064\n\t"
    "v_exp_f32 v46, v46\n\t"
    "v_exp_f32 v47, v47\n\t"
    "v_exp_f32 v48, v48\n\t"
    "v_exp_f32 v49, v49\n\t"
    "v_smfmac_f32_32x32x32_bf16 v[128:143], v[112:115], v[40:47], v124\n\t"
    "ds_read_b64 v[122:123], v126 offset:38784\n\t"
    "v_exp_f32 v50, v50\n\t"
    "v_exp_f32 v51, v51\n\t"
    "v_exp_f32 v52, v52\n\t"
    "v_exp_f32 v53, v53\n\t"
    "v_mfma_f32_32x32x16_bf16 v[64:79], v[100:103], v[16:19], v[64:79]\n\t"
    "s_add_u32 s66, s66, 0x1000\n\t"
    "buffer_load_dwordx4 a[116:119], v1, s[12:15], s66 offen sc1\n\t"
    "v_exp_f32 v54, v54\n\t"
    "v_exp_f32 v55, v55\n\t"
    "v_exp_f32 v56, v56\n\t"
    "v_exp_f32 v57, v57\n\t"
    "v_smfmac_f32_32x32x32_bf16 v[144:159], v[104:107], v[48:55], v124\n\t"
    "v_exp_f32 v58, v58\n\t"
    "v_exp_f32 v59, v59\n\t"
    "v_exp_f32 v60, v60\n\t"
    "v_exp_f32 v61, v61\n\t"
    "v_mfma_f32_32x32x16_bf16 v[80:95], v[100:103], v[20:23], v[80:95]\n\t"
    "buffer_load_dwordx4 a[120:123], v1, s[12:15], s66 offen offset:1024 sc1\n\t"
    "v_exp_f32 v62, v62\n\t"
    "v_exp_f32 v63, v63\n\t"
    "v_exp_f32 v64, v64\n\t"
    "v_exp_f32 v65, v65\n\t"
    "v_smfmac_f32_32x32x32_bf16 v[144:159], v[112:115], v[56:63], v124\n\t"
    "s_waitcnt lgkmcnt(0)\n\t"
    "ds_read_b128 v[100:103], v125 offset:37760\n\t"
    "v_exp_f32 v66, v66\n\t"
    "v_exp_f32 v67, v67\n\t"
    "v_exp_f32 v68, v68\n\t"
    "v_exp_f32 v69, v69\n\t"
    "v_mfma_f32_32x32x4_2b_bf16 v[32:63], v[122:123], v[24:25], 0 cbsz:1 abid:0\n\t"
    "ds_read_b128 v[104:107], v127 offset:39296\n\t"
    "v_exp_f32 v70, v70\n\t"
    "v_exp_f32 v71, v71\n\t"
    "v_exp_f32 v72, v72\n\t"
    "v_exp_f32 v73, v73\n\t"
    "v_exp_f32 v74, v74\n\t"
    "v_exp_f32 v75, v75\n\t"
    "v_exp_f32 v76, v76\n\t"
    "v_exp_f32 v77, v77\n\t"
    "v_smfmac_f32_32x32x32_bf16 v[128:143], v[108:111], v[64:71], v124\n\t"
    "ds_read_b128 v[112:115], v127 offset:39968\n\t"
    "buffer_load_dwordx4 a[124:127], v1, s[12:15], s66 offen offset:2048 sc1\n\t"
    "v_exp_f32 v78, v78\n\t"
    "v_exp_f32 v79, v79\n\t"
    "v_exp_f32 v80, v80\n\t"
    "v_exp_f32 v81, v81\n\t"
    "v_smfmac_f32_32x32x32_bf16 v[128:143], v[116:119], v[72:79], v124\n\t"
    "v_exp_f32 v82, v82\n\t"
    "v_exp_f32 v83, v83\n\t"
    "v_exp_f32 v84, v84\n\t"
    "v_exp_f32 v85, v85\n\t"
    "v_mfma_f32_32x32x16_bf16 v[32:47], v[96:99], v[16:19], v[32:47]\n\t"
    "buffer_load_dwordx4 a[128:131], v1, s[12:15], s66 offen offset:3072 sc1\n\t"
    "v_exp_f32 v86, v86\n\t"
    "v_exp_f32 v87, v87\n\t"
    "v_exp_f32 v88, v88\n\t"
    "v_exp_f32 v89, v89\n\t"
    "v_smfmac_f32_32x32x32_bf16 v[144:159], v[108:111], v[80:87], v124\n\t"
    "v_exp_f32 v90, v90\n\t"
    "v_exp_f32 v91, v91\n\t"
    "v_exp_f32 v92, v92\n\t"
    "v_exp_f32 v93, v93\n\t"
    "v_mfma_f32_32x32x16_bf16 v[48:63], v[96:99], v[20:23], v[48:63]\n\t"
    "s_add_u32 s66, s66, 0x1000\n\t"
    "buffer_load_dwordx4 a[132:135], v1, s[12:15], s66 offen sc1\n\t"
    "v_exp_f32 v94, v94\n\t"
    "v_exp_f32 v95, v95\n\t"
    "v_exp_f32 v32, v32\n\t"
    "v_exp_f32 v33, v33\n\t"
    "v_smfmac_f32_32x32x32_bf16 v[144:159], v[116:119], v[88:95], v124\n\t"
    "s_waitcnt lgkmcnt(0)\n\t"
    "ds_read_b128 v[96:99], v125 offset:41984\n\t"
    "v_exp_f32 v34, v34\n\t"
    "v_exp_f32 v35, v35\n\t"
    "v_exp_f32 v36, v36\n\t"
    "v_exp_f32 v37, v37\n\t"
    "v_mfma_f32_32x32x4_2b_bf16 v[64:95], v[122:123], v[24:25], 0 cbsz:1 abid:1\n\t"
    "ds_read_b128 v[108:111], v127 offset:40640\n\t"
    "v_exp_f32 v38, v38\n\t"
    "v_exp_f32 v39, v39\n\t"
    "v_exp_f32 v40, v40\n\t"
    "v_exp_f32 v41, v41\n\t"
    "v_exp_f32 v42, v42\n\t"
    "v_exp_f32 v43, v43\n\t"
    "v_exp_f32 v44, v44\n\t"
    "v_exp_f32 v45, v45\n\t"
    "v_smfmac_f32_32x32x32_bf16 v[128:143], v[104:107], v[32:39], v124\n\t"
    "ds_read_b128 v[116:119], v127 offset:41312\n\t"
    "buffer_load_dwordx4 a[136:139], v1, s[12:15], s66 offen offset:1024 sc1\n\t"
    "v_exp_f32 v46, v46\n\t"
    "v_exp_f32 v47, v47\n\t"
    "v_exp_f32 v48, v48\n\t"
    "v_exp_f32 v49, v49\n\t"
    "v_smfmac_f32_32x32x32_bf16 v[128:143], v[112:115], v[40:47], v124\n\t"
    "ds_read_b64 v[120:121], v126 offset:44032\n\t"
    "v_exp_f32 v50, v50\n\t"
    "v_exp_f32 v51, v51\n\t"
    "v_exp_f32 v52, v52\n\t"
    "v_exp_f32 v53, v53\n\t"
    "v_mfma_f32_32x32x16_bf16 v[64:79], v[100:103], v[16:19], v[64:79]\n\t"
    "buffer_load_dwordx4 a[140:143], v1, s[12:15], s66 offen offset:2048 sc1\n\t"
    "v_exp_f32 v54, v54\n\t"
    "v_exp_f32 v55, v55\n\t"
    "v_exp_f32 v56, v56\n\t"
    "v_exp_f32 v57, v57\n\t"
    "v_smfmac_f32_32x32x32_bf16 v[144:159], v[104:107], v[48:55], v124\n\t"
    "v_exp_f32 v58, v58\n\t"
    "v_exp_f32 v59, v59\n\t"
    "v_exp_f32 v60, v60\n\t"
    "v_exp_f32 v61, v61\n\t"
    "v_mfma_f32_32x32x16_bf16 v[80:95], v[100:103], v[20:23], v[80:95]\n\t"
    "buffer_load_dwordx4 a[144:147], v1, s[12:15], s66 offen offset:3072 sc1\n\t"
    "v_exp_f32 v62, v62\n\t"
    "v_exp_f32 v63, v63\n\t"
    "v_exp_f32 v64, v64\n\t"
    "v_exp_f32 v65, v65\n\t"
    "v_smfmac_f32_32x32x32_bf16 v[144:159], v[112:115], v[56:63], v124\n\t"
    "s_waitcnt lgkmcnt(0)\n\t"
    "ds_read_b128 v[100:103], v125 offset:43008\n\t"
    "v_exp_f32 v66, v66\n\t"
    "v_exp_f32 v67, v67\n\t"
    "v_exp_f32 v68, v68\n\t"
    "v_exp_f32 v69, v69\n\t"
    "v_mfma_f32_32x32x4_2b_bf16 v[32:63], v[120:121], v[24:25], 0 cbsz:1 abid:0\n\t"
    "ds_read_b128 v[104:107], v127 offset:44544\n\t"
    "v_exp_f32 v70, v70\n\t"
    "v_exp_f32 v71, v71\n\t"
    "v_exp_f32 v72, v72\n\t"
    "v_exp_f32 v73, v73\n\t"
    "v_exp_f32 v74, v74\n\t"
    "v_exp_f32 v75, v75\n\t"
    "v_exp_f32 v76, v76\n\t"
    "v_exp_f32 v77, v77\n\t"
    "v_smfmac_f32_32x32x32_bf16 v[128:143], v[108:111], v[64:71], v124\n\t"
    "ds_read_b128 v[112:115], v127 offset:45216\n\t"
    "s_add_u32 s66, s66, 0x1000\n\t"
    "buffer_load_dwordx4 a[148:151], v1, s[12:15], s66 offen sc1\n\t"
    "v_exp_f32 v78, v78\n\t"
    "v_exp_f32 v79, v79\n\t"
    "v_exp_f32 v80, v80\n\t"
    "v_exp_f32 v81, v81\n\t"
    "v_smfmac_f32_32x32x32_bf16 v[128:143], v[116:119], v[72:79], v124\n\t"
    "v_exp_f32 v82, v82\n\t"
    "v_exp_f32 v83, v83\n\t"
    "v_exp_f32 v84, v84\n\t"
    "v_exp_f32 v85, v85\n\t"
    "v_mfma_f32_32x32x16_bf16 v[32:47], v[96:99], v[16:19], v[32:47]\n\t"
    "buffer_load_dwordx4 a[152:155], v1, s[12:15], s66 offen offset:1024 sc1\n\t"
    "v_exp_f32 v86, v86\n\t"
    "v_exp_f32 v87, v87\n\t"
    "v_exp_f32 v88, v88\n\t"
    "v_exp_f32 v89, v89\n\t"
    "v_smfmac_f32_32x32x32_bf16 v[144:159], v[108:111], v[80:87], v124\n\t"
    "pad5_%=:\n\t"
    "s_cbranch_execz vb8_%=\n\t"
    "v_exp_f32 v90, v90\n\t"
    "v_exp_f32 v91, v91\n\t"
    "v_exp_f32 v92, v92\n\t"
    "v_exp_f32 v93, v93\n\t"
    "v_mfma_f32_32x32x16_bf16 v[48:63], v[96:99], v[20:23], v[48:63]\n\t"
    "buffer_load_dwordx4 a[156:159], v1, s[12:15], s66 offen offset:2048 sc1\n\t"
    "v_exp_f32 v94, v94\n\t"
    "v_exp_f32 v95, v95\n\t"
    "v_exp_f32 v32, v32\n\t"
    "v_exp_f32 v33, v33\n\t"
    "v_smfmac_f32_32x32x32_bf16 v[144:159], v[116:119], v[88:95], v124\n\t"
    "s_waitcnt lgkmcnt(0)\n\t"
    "ds_read_b128 v[96:99], v125 offset:47232\n\t"
    "v_exp_f32 v34, v34\n\t"
    "v_exp_f32 v35, v35\n\t"
    "v_exp_f32 v36, v36\n\t"
    "v_exp_f32 v37, v37\n\t"
    "v_mfma_f32_32x32x4_2b_bf16 v[64:95], v[120:121], v[24:25], 0 cbsz:1 abid:1\n\t"
    "ds_read_b128 v[108:111], v127 offset:45888\n\t"
    "v_exp_f32 v38, v38\n\t"
    "v_exp_f32 v39, v39\n\t"
    "v_exp_f32 v40, v40\n\t"
    "v_exp_f32 v41, v41\n\t"
    "v_exp_f32 v42, v42\n\t"
    "v_exp_f32 v43, v43\n\t"
    "v_exp_f32 v44, v44\n\t"
    "v_exp_f32 v45, v45\n\t"
    "v_smfmac_f32_32x32x32_bf16 v[128:143], v[104:107], v[32:39], v124\n\t"
    "ds_read_b128 v[116:119], v127 offset:46560\n\t"
    "buffer_load_dwordx4 a[160:163], v1, s[12:15], s66 offen offset:3072 sc1\n\t"
    "v_exp_f32 v46, v46\n\t"
    "v_exp_f32 v47, v47\n\t"
    "v_exp_f32 v48, v48\n\t"
    "v_exp_f32 v49, v49\n\t"
    "v_smfmac_f32_32x32x32_bf16 v[128:143], v[112:115], v[40:47], v124\n\t"
    "ds_read_b64 v[122:123], v126 offset:49280\n\t"
    "v_exp_f32 v50, v50\n\t"
    "v_exp_f32 v51, v51\n\t"
    "v_exp_f32 v52, v52\n\t"
    "v_exp_f32 v53, v53\n\t"
    "v_mfma_f32_32x32x16_bf16 v[64:79], v[100:103], v[16:19], v[64:79]\n\t"
    "s_add_u32 s66, s66, 0x1000\n\t"
    "buffer_load_dwordx4 a[164:167], v1, s[12:15], s66 offen sc1\n\t"
    "v_exp_f32 v54, v54\n\t"
    "v_exp_f32 v55, v55\n\t"
    "v_exp_f32 v56, v56\n\t"
    "v_exp_f32 v57, v57\n\t"
    "v_smfmac_f32_32x32x32_bf16 v[144:159], v[104:107], v[48:55], v124\n\t"
    "v_exp_f32 v58, v58\n\t"
    "v_exp_f32 v59, v59\n\t"
    "v_exp_f32 v60, v60\n\t"
    "v_exp_f32 v61, v61\n\t"
    "v_mfma_f32_32x32x16_bf16 v[80:95], v[100:103], v[20:23], v[80:95]\n\t"
    "buffer_load_dwordx4 a[168:171], v1, s[12:15], s66 offen offset:1024 sc1\n\t"
    "v_exp_f32 v62, v62\n\t"
    "v_exp_f32 v63, v63\n\t"
    "v_exp_f32 v64, v64\n\t"
    "v_exp_f32 v65, v65\n\t"
    "v_smfmac_f32_32x32x32_bf16 v[144:159], v[112:115], v[56:63], v124\n\t"
    "s_waitcnt lgkmcnt(0)\n\t"
    "ds_read_b128 v[100:103], v125 offset:48256\n\t"
    "v_exp_f32 v66, v66\n\t"
    "v_exp_f32 v67, v67\n\t"
    "v_exp_f32 v68, v68\n\t"
    "v_exp_f32 v69, v69\n\t"
    "v_mfma_f32_32x32x4_2b_bf16 v[32:63], v[122:123], v[24:25], 0 cbsz:1 abid:0\n\t"
    "ds_read_b128 v[104:107], v127 offset:49792\n\t"
    "v_exp_f32 v70, v70\n\t"
    "v_exp_f32 v71, v71\n\t"
    "v_exp_f32 v72, v72\n\t"
    "v_exp_f32 v73, v73\n\t"
    "v_exp_f32 v74, v74\n\t"
    "v_exp_f32 v75, v75\n\t"
    "v_exp_f32 v76, v76\n\t"
    "v_exp_f32 v77, v77\n\t"
    "v_smfmac_f32_32x32x32_bf16 v[128:143], v[108:111], v[64:71], v124\n\t"
    "ds_read_b128 v[112:115], v127 offset:50464\n\t"
    "buffer_load_dwordx4 a[172:175], v1, s[12:15], s66 offen offset:2048 sc1\n\t"
    "v_exp_f32 v78, v78\n\t"
    "v_exp_f32 v79, v79\n\t"
    "v_exp_f32 v80, v80\n\t"
    "v_exp_f32 v81, v81\n\t"
    "v_smfmac_f32_32x32x32_bf16 v[128:143], v[116:119], v[72:79], v124\n\t"
    "v_exp_f32 v82, v82\n\t"
    "v_exp_f32 v83, v83\n\t"
    "v_exp_f32 v84, v84\n\t"
    "v_exp_f32 v85, v85\n\t"
    "v_mfma_f32_32x32x16_bf16 v[32:47], v[96:99], v[16:19], v[32:47]\n\t"
    "buffer_load_dwordx4 a[176:179], v1, s[12:15], s66 offen offset:3072 sc1\n\t"
    "v_exp_f32 v86, v86\n\t"
    "v_exp_f32 v87, v87\n\t"
    "v_exp_f32 v88, v88\n\t"
    "v_exp_f32 v89, v89\n\t"
    "v_smfmac_f32_32x32x32_bf16 v[144:159], v[108:111], v[80:87], v124\n\t"
    "v_exp_f32 v90, v90\n\t"
    "v_exp_f32 v91, v91\n\t"
    "v_exp_f32 v92, v92\n\t"
    "v_exp_f32 v93, v93\n\t"
    "v_mfma_f32_32x32x16_bf16 v[48:63], v[96:99], v[20:23], v[48:63]\n\t"
    "s_add_u32 s66, s66, 0x1000\n\t"
    "buffer_load_dwordx4 a[180:183], v1, s[12:15], s66 offen sc1\n\t"
    "v_exp_f32 v94, v94\n\t"
    "v_exp_f32 v95, v95\n\t"
    "v_exp_f32 v32, v32\n\t"
    "v_exp_f32 v33, v33\n\t"
    "v_smfmac_f32_32x32x32_bf16 v[144:159], v[116:119], v[88:95], v124\n\t"
    "s_waitcnt lgkmcnt(0)\n\t"
    "ds_read_b128 v[96:99], v125 offset:52480\n\t"
    "v_exp_f32 v34, v34\n\t"
    "v_exp_f32 v35, v35\n\t"
    "v_exp_f32 v36, v36\n\t"
    "v_exp_f32 v37, v37\n\t"
    "v_mfma_f32_32x32x4_2b_bf16 v[64:95], v[122:123], v[24:25], 0 cbsz:1 abid:1\n\t"
    "ds_read_b128 v[108:111], v127 offset:51136\n\t"
    "v_exp_f32 v38, v38\n\t"
    "v_exp_f32 v39, v39\n\t"
    "v_exp_f32 v40, v40\n\t"
    "v_exp_f32 v41, v41\n\t"
    "v_exp_f32 v42, v42\n\t"
    "v_exp_f32 v43, v43\n\t"
    "v_exp_f32 v44, v44\n\t"
    "v_exp_f32 v45, v45\n\t"
    "v_smfmac_f32_32x32x32_bf16 v[128:143], v[104:107], v[32:39], v124\n\t"
    "ds_read_b128 v[116:119], v127 offset:51808\n\t"
    "buffer_load_dwordx4 a[184:187], v1, s[12:15], s66 offen offset:1024 sc1\n\t"
    "v_exp_f32 v46, v46\n\t"
    "v_exp_f32 v47, v47\n\t"
    "v_exp_f32 v48, v48\n\t"
    "v_exp_f32 v49, v49\n\t"
    "v_smfmac_f32_32x32x32_bf16 v[128:143], v[112:115], v[40:47], v124\n\t"
    "ds_read_b64 v[120:121], v126 offset:54528\n\t"
    "v_exp_f32 v50, v50\n\t"
    "v_exp_f32 v51, v51\n\t"
    "v_exp_f32 v52, v52\n\t"
    "v_exp_f32 v53, v53\n\t"
    "v_mfma_f32_32x32x16_bf16 v[64:79], v[100:103], v[16:19], v[64:79]\n\t"
    "buffer_load_dwordx4 a[188:191], v1, s[12:15], s66 offen offset:2048 sc1\n\t"
    "v_exp_f32 v54, v54\n\t"
    "v_exp_f32 v55, v55\n\t"
    "v_exp_f32 v56, v56\n\t"
    "v_exp_f32 v57, v57\n\t"
    "v_smfmac_f32_32x32x32_bf16 v[144:159], v[104:107], v[48:55], v124\n\t"
    "v_exp_f32 v58, v58\n\t"
    "v_exp_f32 v59, v59\n\t"
    "v_exp_f32 v60, v60\n\t"
    "v_exp_f32 v61, v61\n\t"
    "v_mfma_f32_32x32x16_bf16 v[80:95], v[100:103], v[20:23], v[80:95]\n\t"
    "buffer_load_dwordx4 a[192:195], v1, s[12:15], s66 offen offset:3072 sc1\n\t"
    "v_exp_f32 v62, v62\n\t"
    "v_exp_f32 v63, v63\n\t"
    "v_exp_f32 v64, v64\n\t"
    "v_exp_f32 v65, v65\n\t"
    "v_smfmac_f32_32x32x32_bf16 v[144:159], v[112:115], v[56:63], v124\n\t"
    "s_waitcnt lgkmcnt(0)\n\t"
    "ds_read_b128 v[100:103], v125 offset:53504\n\t"
    "v_exp_f32 v66, v66\n\t"
    "v_exp_f32 v67, v67\n\t"
    "v_exp_f32 v68, v68\n\t"
    "v_exp_f32 v69, v69\n\t"
    "v_mfma_f32_32x32x4_2b_bf16 v[32:63], v[120:121], v[24:25], 0 cbsz:1 abid:0\n\t"
    "ds_read_b128 v[104:107], v127 offset:55040\n\t"
    "v_exp_f32 v70, v70\n\t"
    "v_exp_f32 v71, v71\n\t"
    "v_exp_f32 v72, v72\n\t"
    "v_exp_f32 v73, v73\n\t"
    "v_exp_f32 v74, v74\n\t"
    "v_exp_f32 v75, v75\n\t"
    "v_exp_f32 v76, v76\n\t"
    "v_exp_f32 v77, v77\n\t"
    "v_smfmac_f32_32x32x32_bf16 v[128:143], v[108:111], v[64:71], v124\n\t"
    "ds_read_b128 v[112:115], v127 offset:55712\n\t"
    "s_add_u32 s66, s66, 0x1000\n\t"
    "buffer_load_dwordx4 a[196:199], v1, s[12:15], s66 offen sc1\n\t"
    "v_exp_f32 v78, v78\n\t"
    "v_exp_f32 v79, v79\n\t"
    "v_exp_f32 v80, v80\n\t"
    "v_exp_f32 v81, v81\n\t"
    "v_smfmac_f32_32x32x32_bf16 v[128:143], v[116:119], v[72:79], v124\n\t"
    "v_exp_f32 v82, v82\n\t"
    "v_exp_f32 v83, v83\n\t"
    "v_exp_f32 v84, v84\n\t"
    "v_exp_f32 v85, v85\n\t"
    "v_mfma_f32_32x32x16_bf16 v[32:47], v[96:99], v[16:19], v[32:47]\n\t"
    "buffer_load_dwordx4 a[200:203], v1, s[12:15], s66 offen offset:1024 sc1\n\t"
    "v_exp_f32 v86, v86\n\t"
    "v_exp_f32 v87, v87\n\t"
    "v_exp_f32 v88, v88\n\t"
    "v_exp_f32 v89, v89\n\t"
    "v_smfmac_f32_32x32x32_bf16 v[144:159], v[108:111], v[80:87], v124\n\t"
    "v_exp_f32 v90, v90\n\t"
    "v_exp_f32 v91, v91\n\t"
    "v_exp_f32 v92, v92\n\t"
    "v_exp_f32 v93, v93\n\t"
    "v_mfma_f32_32x32x16_bf16 v[48:63], v[96:99], v[20:23], v[48:63]\n\t"
    "buffer_load_dwordx4 a[204:207], v1, s[12:15], s66 offen offset:2048 sc1\n\t"
    "v_exp_f32 v94, v94\n\t"
    "v_exp_f32 v95, v95\n\t"
    "v_exp_f32 v32, v32\n\t"
    "v_exp_f32 v33, v33\n\t"
    "v_smfmac_f32_32x32x32_bf16 v[144:159], v[116:119], v[88:95], v124\n\t"
    "s_waitcnt lgkmcnt(0)\n\t"
    "ds_read_b128 v[96:99], v125 offset:57728\n\t"
    "v_exp_f32 v34, v34\n\t"
    "v_exp_f32 v35, v35\n\t"
    "v_exp_f32 v36, v36\n\t"
    "v_exp_f32 v37, v37\n\t"
    "v_mfma_f32_32x32x4_2b_bf16 v[64:95], v[120:121], v[24:25], 0 cbsz:1 abid:1\n\t"
    "ds_read_b128 v[108:111], v127 offset:56384\n\t"
    "v_exp_f32 v38, v38\n\t"
    "v_exp_f32 v39, v39\n\t"
    "v_exp_f32 v40, v40\n\t"
    "v_exp_f32 v41, v41\n\t"
    "v_exp_f32 v42, v42\n\t"
    "v_exp_f32 v43, v43\n\t"
    "v_exp_f32 v44, v44\n\t"
    "v_exp_f32 v45, v45\n\t"
    "v_smfmac_f32_32x32x32_bf16 v[128:143], v[104:107], v[32:39], v124\n\t"
    "ds_read_b128 v[116:119], v127 offset:57056\n\t"
    "buffer_load_dwordx4 a[208:211], v1, s[12:15], s66 offen offset:3072 sc1\n\t"
    "v_exp_f32 v46, v46\n\t"
    "v_exp_f32 v47, v47\n\t"
    "v_exp_f32 v48, v48\n\t"
    "v_exp_f32 v49, v49\n\t"
    "v_smfmac_f32_32x32x32_bf16 v[128:143], v[112:115], v[40:47], v124\n\t"
    "ds_read_b64 v[122:123], v126 offset:59776\n\t"
    "v_exp_f32 v50, v50\n\t"
    "v_exp_f32 v51, v51\n\t"
    "v_exp_f32 v52, v52\n\t"
    "v_exp_f32 v53, v53\n\t"
    "v_mfma_f32_32x32x16_bf16 v[64:79], v[100:103], v[16:19], v[64:79]\n\t"
    "s_add_u32 s66, s66, 0x1000\n\t"
    "buffer_load_dwordx4 a[212:215], v1, s[12:15], s66 offen sc1\n\t"
    "v_exp_f32 v54, v54\n\t"
    "v_exp_f32 v55, v55\n\t"
    "v_exp_f32 v56, v56\n\t"
    "v_exp_f32 v57, v57\n\t"
    "v_smfmac_f32_32x32x32_bf16 v[144:159], v[104:107], v[48:55], v124\n\t"
    "v_exp_f32 v58, v58\n\t"
    "v_exp_f32 v59, v59\n\t"
    "v_exp_f32 v60, v60\n\t"
    "v_exp_f32 v61, v61\n\t"
    "v_mfma_f32_32x32x16_bf16 v[80:95], v[100:103], v[20:23], v[80:95]\n\t"
    "buffer_load_dwordx4 a[216:219], v1, s[12:15], s66 offen offset:1024 sc1\n\t"
    "v_exp_f32 v62, v62\n\t"
    "v_exp_f32 v63, v63\n\t"
    "v_exp_f32 v64, v64\n\t"
    "v_exp_f32 v65, v65\n\t"
    "v_smfmac_f32_32x32x32_bf16 v[144:159], v[112:115], v[56:63], v124\n\t"
    "s_waitcnt lgkmcnt(0)\n\t"
    "ds_read_b128 v[100:103], v125 offset:58752\n\t"
    "v_exp_f32 v66, v66\n\t"
    "v_exp_f32 v67, v67\n\t"
    "v_exp_f32 v68, v68\n\t"
    "v_exp_f32 v69, v69\n\t"
    "v_mfma_f32_32x32x4_2b_bf16 v[32:63], v[122:123], v[24:25], 0 cbsz:1 abid:0\n\t"
    "ds_read_b128 v[104:107], v127 offset:60288\n\t"
    "v_exp_f32 v70, v70\n\t"
    "v_exp_f32 v71, v71\n\t"
    "v_exp_f32 v72, v72\n\t"
    "v_exp_f32 v73, v73\n\t"
    "v_exp_f32 v74, v74\n\t"
    "v_exp_f32 v75, v75\n\t"
    "v_exp_f32 v76, v76\n\t"
    "v_exp_f32 v77, v77\n\t"
    "v_smfmac_f32_32x32x32_bf16 v[128:143], v[108:111], v[64:71], v124\n\t"
    "ds_read_b128 v[112:115], v127 offset:60960\n\t"
    "v_exp_f32 v78, v78\n\t"
    "v_exp_f32 v79, v79\n\t"
    "v_exp_f32 v80, v80\n\t"
    "v_exp_f32 v81, v81\n\t"
    "v_smfmac_f32_32x32x32_bf16 v[128:143], v[116:119], v[72:79], v124\n\t"
    "v_exp_f32 v82, v82\n\t"
    "v_exp_f32 v83, v83\n\t"
    "v_exp_f32 v84, v84\n\t"
    "v_exp_f32 v85, v85\n\t"
    "v_mfma_f32_32x32x16_bf16 v[32:47], v[96:99], v[16:19], v[32:47]\n\t"
    "buffer_load_dwordx4 a[220:223], v1, s[12:15], s66 offen offset:2048 sc1\n\t"
    "v_exp_f32 v86, v86\n\t"
    "v_exp_f32 v87, v87\n\t"
    "v_exp_f32 v88, v88\n\t"
    "v_exp_f32 v89, v89\n\t"
    "v_smfmac_f32_32x32x32_bf16 v[144:159], v[108:111], v[80:87], v124\n\t"
    "v_exp_f32 v90, v90\n\t"
    "v_exp_f32 v91, v91\n\t"
    "v_exp_f32 v92, v92\n\t"
    "v_exp_f32 v93, v93\n\t"
    "v_mfma_f32_32x32x16_bf16 v[48:63], v[96:99], v[20:23], v[48:63]\n\t"
    "buffer_load_dwordx4 a[224:227], v1, s[12:15], s66 offen offset:3072 sc1\n\t"
    "v_exp_f32 v94, v94\n\t"
    "v_exp_f32 v95, v95\n\t"
    "v_exp_f32 v32, v32\n\t"
    "v_exp_f32 v33, v33\n\t"
    "v_smfmac_f32_32x32x32_bf16 v[144:159], v[116:119], v[88:95], v124\n\t"
    "s_waitcnt lgkmcnt(0)\n\t"
    "v_add_u32 v125, 0xf600, v125\n\t"
    "ds_read_b128 v[96:99], v125\n\t"
    "v_exp_f32 v34, v34\n\t"
    "v_exp_f32 v35, v35\n\t"
    "v_exp_f32 v36, v36\n\t"
    "v_exp_f32 v37, v37\n\t"
    "v_mfma_f32_32x32x4_2b_bf16 v[64:95], v[122:123], v[24:25], 0 cbsz:1 abid:1\n\t"
    "ds_read_b128 v[108:111], v127 offset:61632\n\t"
    "v_exp_f32 v38, v38\n\t"
    "v_exp_f32 v39, v39\n\t"
    "v_exp_f32 v40, v40\n\t"
    "v_exp_f32 v41, v41\n\t"
    "v_exp_f32 v42, v42\n\t"
    "v_exp_f32 v43, v43\n\t"
    "v_exp_f32 v44, v44\n\t"
    "v_exp_f32 v45, v45\n\t"
    "v_smfmac_f32_32x32x32_bf16 v[128:143], v[104:107], v[32:39], v124\n\t"
    "ds_read_b128 v[116:119], v127 offset:62304\n\t"
    "s_add_u32 s66, s66, 0x1000\n\t"
    "buffer_load_dwordx4 a[228:231], v1, s[12:15], s66 offen sc1\n\t"
    "v_exp_f32 v46, v46\n\t"
    "v_exp_f32 v47, v47\n\t"
    "v_exp_f32 v48, v48\n\t"
    "v_exp_f32 v49, v49\n\t"
    "v_smfmac_f32_32x32x32_bf16 v[128:143], v[112:115], v[40:47], v124\n\t"
    "v_add_u32 v126, 0xf600, v126\n\t"
    "ds_read_b64 v[120:121], v126 offset:2048\n\t"
    "v_exp_f32 v50, v50\n\t"
    "v_exp_f32 v51, v51\n\t"
    "v_exp_f32 v52, v52\n\t"
    "v_exp_f32 v53, v53\n\t"
    "v_mfma_f32_32x32x16_bf16 v[64:79], v[100:103], v[16:19], v[64:79]\n\t"
    "buffer_load_dwordx4 a[232:235], v1, s[12:15], s66 offen offset:1024 sc1\n\t"
    "v_exp_f32 v54, v54\n\t"
    "v_exp_f32 v55, v55\n\t"
    "v_exp_f32 v56, v56\n\t"
    "v_exp_f32 v57, v57\n\t"
    "v_smfmac_f32_32x32x32_bf16 v[144:159], v[104:107], v[48:55], v124\n\t"
    "v_exp_f32 v58, v58\n\t"
    "v_exp_f32 v59, v59\n\t"
    "v_exp_f32 v60, v60\n\t"
    "v_exp_f32 v61, v61\n\t"
    "v_mfma_f32_32x32x16_bf16 v[80:95], v[100:103], v[20:23], v[80:95]\n\t"
    "buffer_load_dwordx4 a[236:239], v1, s[12:15], s66 offen offset:2048 sc1\n\t"
    "v_exp_f32 v62, v62\n\t"
    "v_exp_f32 v63, v63\n\t"
    "v_exp_f32 v64, v64\n\t"
    "v_exp_f32 v65, v65\n\t"
    "v_smfmac_f32_32x32x32_bf16 v[144:159], v[112:115], v[56:63], v124\n\t"
    "s_waitcnt lgkmcnt(0)\n\t"
    "ds_read_b128 v[100:103], v125 offset:1024\n\t"
    "v_exp_f32 v66, v66\n\t"
    "v_exp_f32 v67, v67\n\t"
    "v_exp_f32 v68, v68\n\t"
    "v_exp_f32 v69, v69\n\t"
    "v_mfma_f32_32x32x4_2b_bf16 v[32:63], v[120:121], v[24:25], 0 cbsz:1 abid:0\n\t"
    "v_add_u32 v127, 0xf600, v127\n\t"
    "ds_read_b128 v[104:107], v127 offset:2560\n\t"
    "v_exp_f32 v70, v70\n\t"
    "v_exp_f32 v71, v71\n\t"
    "v_exp_f32 v72, v72\n\t"
    "v_exp_f32 v73, v73\n\t"
    "v_exp_f32 v74, v74\n\t"
    "v_exp_f32 v75, v75\n\t"
    "v_exp_f32 v76, v76\n\t"
    "v_exp_f32 v77, v77\n\t"
    "v_smfmac_f32_32x32x32_bf16 v[128:143], v[108:111], v[64:71], v124\n\t"
    "ds_read_b128 v[112:115], v127 offset:3232\n\t"
    "buffer_load_dwordx4 a[240:243], v1, s[12:15], s66 offen offset:3072 sc1\n\t"
    "v_exp_f32 v78, v78\n\t"
    "v_exp_f32 v79, v79\n\t"
    "v_exp_f32 v80, v80\n\t"
    "v_exp_f32 v81, v81\n\t"
    "v_smfmac_f32_32x32x32_bf16 v[128:143], v[116:119], v[72:79], v124\n\t"
    "v_exp_f32 v82, v82\n\t"
    "v_exp_f32 v83, v83\n\t"
    "v_exp_f32 v84, v84\n\t"
    "v_exp_f32 v85, v85\n\t"
    "v_mfma_f32_32x32x16_bf16 v[32:47], v[96:99], v[16:19], v[32:47]\n\t"
    "s_add_u32 s66, s66, 0x1000\n\t"
    "buffer_load_dwordx4 a[244:247], v1, s[12:15], s66 offen sc1\n\t"
    "v_exp_f32 v86, v86\n\t"
    "v_exp_f32 v87, v87\n\t"
    "v_exp_f32 v88, v88\n\t"
    "v_exp_f32 v89, v89\n\t"
    "v_smfmac_f32_32x32x32_bf16 v[144:159], v[108:111], v[80:87], v124\n\t"
    "v_exp_f32 v90, v90\n\t"
    "v_exp_f32 v91, v91\n\t"
    "v_exp_f32 v92, v92\n\t"
    "v_exp_f32 v93, v93\n\t"
    "v_mfma_f32_32x32x16_bf16 v[48:63], v[96:99], v[20:23], v[48:63]\n\t"
    "buffer_load_dwordx4 a[248:251], v1, s[12:15], s66 offen offset:1024 sc1\n\t"
    "v_exp_f32 v94, v94\n\t"
    "v_exp_f32 v95, v95\n\t"
    "v_exp_f32 v32, v32\n\t"
    "v_exp_f32 v33, v33\n\t"
    "v_smfmac_f32_32x32x32_bf16 v[144:159], v[116:119], v[88:95], v124\n\t"
    "s_waitcnt lgkmcnt(0)\n\t"
    "ds_read_b128 v[96:99], v125 offset:5248\n\t"
    "v_exp_f32 v34, v34\n\t"
    "v_exp_f32 v35, v35\n\t"
    "v_exp_f32 v36, v36\n\t"
    "v_exp_f32 v37, v37\n\t"
    "v_mfma_f32_32x32x4_2b_bf16 v[64:95], v[120:121], v[24:25], 0 cbsz:1 abid:1\n\t"
    "ds_read_b128 v[108:111], v127 offset:3904\n\t"
    "v_exp_f32 v38, v38\n\t"
    "v_exp_f32 v39, v39\n\t"
    "v_exp_f32 v40, v40\n\t"
    "v_exp_f32 v41, v41\n\t"
    "v_exp_f32 v42, v42\n\t"
    "v_exp_f32 v43, v43\n\t"
    "v_exp_f32 v44, v44\n\t"
    "v_exp_f32 v45, v45\n\t"
    "v_smfmac_f32_32x32x32_bf16 v[128:143], v[104:107], v[32:39], v124\n\t"
    "ds_read_b128 v[116:119], v127 offset:4576\n\t"
    "buffer_load_dwordx4 a[252:255], v1, s[12:15], s66 offen offset:2048 sc1\n\t"
    "v_exp_f32 v46, v46\n\t"
    "v_exp_f32 v47, v47\n\t"
    "v_exp_f32 v48, v48\n\t"
    "v_exp_f32 v49, v49\n\t"
    "v_smfmac_f32_32x32x32_bf16 v[128:143], v[112:115], v[40:47], v124\n\t"
    "ds_read_b64 v[122:123], v126 offset:7296\n\t"
    "v_exp_f32 v50, v50\n\t"
    "v_exp_f32 v51, v51\n\t"
    "v_exp_f32 v52, v52\n\t"
    "v_exp_f32 v53, v53\n\t"
    "v_mfma_f32_32x32x16_bf16 v[64:79], v[100:103], v[16:19], v[64:79]\n\t"
    "buffer_load_dwordx4 v[252:255], v1, s[12:15], s66 offen offset:3072 sc1\n\t"
    "v_exp_f32 v54, v54\n\t"
    "v_exp_f32 v55, v55\n\t"
    "v_exp_f32 v56, v56\n\t"
    "v_exp_f32 v57, v57\n\t"
    "v_smfmac_f32_32x32x32_bf16 v[144:159], v[104:107], v[48:55], v124\n\t"
    "v_exp_f32 v58, v58\n\t"
    "v_exp_f32 v59, v59\n\t"
    "v_exp_f32 v60, v60\n\t"
    "v_exp_f32 v61, v61\n\t"
    "v_mfma_f32_32x32x16_bf16 v[80:95], v[100:103], v[20:23], v[80:95]\n\t"
    "s_mul_i32 s66, s34, 0xa000\n\t"
    "s_mul_i32 s32, s82, 0xa000\n\t"
    "s_add_u32 s66, s66, s32\n\t"
    "s_add_u32 s66, s66, 0x23c000\n\t"
    "buffer_load_dwordx4 v[248:251], v1, s[16:19], s66 offen sc1\n\t"
    "v_exp_f32 v62, v62\n\t"
    "v_exp_f32 v63, v63\n\t"
    "v_exp_f32 v64, v64\n\t"
    "v_exp_f32 v65, v65\n\t"
    "v_smfmac_f32_32x32x32_bf16 v[144:159], v[112:115], v[56:63], v124\n\t"
    "s_waitcnt lgkmcnt(0)\n\t"
    "ds_read_b128 v[100:103], v125 offset:6272\n\t"
    "v_exp_f32 v66, v66\n\t"
    "v_exp_f32 v67, v67\n\t"
    "v_exp_f32 v68, v68\n\t"
    "v_exp_f32 v69, v69\n\t"
    "v_mfma_f32_32x32x4_2b_bf16 v[32:63], v[122:123], v[24:25], 0 cbsz:1 abid:0\n\t"
    "ds_read_b128 v[104:107], v127 offset:7808\n\t"
    "v_exp_f32 v70, v70\n\t"
    "v_exp_f32 v71, v71\n\t"
    "v_exp_f32 v72, v72\n\t"
    "v_exp_f32 v73, v73\n\t"
    "v_exp_f32 v74, v74\n\t"
    "v_exp_f32 v75, v75\n\t"
    "v_exp_f32 v76, v76\n\t"
    "v_exp_f32 v77, v77\n\t"
    "v_smfmac_f32_32x32x32_bf16 v[128:143], v[108:111], v[64:71], v124\n\t"
    "ds_read_b128 v[112:115], v127 offset:8480\n\t"
    "buffer_load_dwordx4 v[244:247], v1, s[16:19], s66 offen offset:1024 sc1\n\t"
    "v_exp_f32 v78, v78\n\t"
    "v_exp_f32 v79, v79\n\t"
    "v_exp_f32 v80, v80\n\t"
    "v_exp_f32 v81, v81\n\t"
    "v_smfmac_f32_32x32x32_bf16 v[128:143], v[116:119], v[72:79], v124\n\t"
    "v_exp_f32 v82, v82\n\t"
    "v_exp_f32 v83, v83\n\t"
    "v_exp_f32 v84, v84\n\t"
    "v_exp_f32 v85, v85\n\t"
    "v_mfma_f32_32x32x16_bf16 v[32:47], v[96:99], v[16:19], v[32:47]\n\t"
    "buffer_load_dwordx4 v[240:243], v1, s[16:19], s66 offen offset:2048 sc1\n\t"
    "v_exp_f32 v86, v86\n\t"
    "v_exp_f32 v87, v87\n\t"
    "v_exp_f32 v88, v88\n\t"
    "v_exp_f32 v89, v89\n\t"
    "v_smfmac_f32_32x32x32_bf16 v[144:159], v[108:111], v[80:87], v124\n\t"
    "v_exp_f32 v90, v90\n\t"
    "v_exp_f32 v91, v91\n\t"
    "v_exp_f32 v92, v92\n\t"
    "v_exp_f32 v93, v93\n\t"
    "v_mfma_f32_32x32x16_bf16 v[48:63], v[96:99], v[20:23], v[48:63]\n\t"
    "buffer_load_dwordx4 v[236:239], v1, s[16:19], s66 offen offset:3072 sc1\n\t"
    "v_exp_f32 v94, v94\n\t"
    "v_exp_f32 v95, v95\n\t"
    "v_exp_f32 v32, v32\n\t"
    "v_exp_f32 v33, v33\n\t"
    "v_smfmac_f32_32x32x32_bf16 v[144:159], v[116:119], v[88:95], v124\n\t"
    "s_waitcnt lgkmcnt(0)\n\t"
    "ds_read_b128 v[96:99], v125 offset:10496\n\t"
    "v_exp_f32 v34, v34\n\t"
    "v_exp_f32 v35, v35\n\t"
    "v_exp_f32 v36, v36\n\t"
    "v_exp_f32 v37, v37\n\t"
    "v_mfma_f32_32x32x4_2b_bf16 v[64:95], v[122:123], v[24:25], 0 cbsz:1 abid:1\n\t"
    "ds_read_b128 v[108:111], v127 offset:9152\n\t"
    "v_exp_f32 v38, v38\n\t"
    "v_exp_f32 v39, v39\n\t"
    "v_exp_f32 v40, v40\n\t"
    "v_exp_f32 v41, v41\n\t"
    "v_exp_f32 v42, v42\n\t"
    "v_exp_f32 v43, v43\n\t"
    "v_exp_f32 v44, v44\n\t"
    "v_exp_f32 v45, v45\n\t"
    "v_smfmac_f32_32x32x32_bf16 v[128:143], v[104:107], v[32:39], v124\n\t"
    "ds_read_b128 v[116:119], v127 offset:9824\n\t"
    "s_add_u32 s66, s66, 0x1000\n\t"
    "buffer_load_dwordx4 v[232:235], v1, s[16:19], s66 offen sc1\n\t"
    "v_exp_f32 v46, v46\n\t"
    "v_exp_f32 v47, v47\n\t"
    "v_exp_f32 v48, v48\n\t"
    "v_exp_f32 v49, v49\n\t"
    "v_smfmac_f32_32x32x32_bf16 v[128:143], v[112:115], v[40:47], v124\n\t"
    "ds_read_b64 v[120:121], v126 offset:12544\n\t"
    "v_exp_f32 v50, v50\n\t"
    "v_exp_f32 v51, v51\n\t"
    "v_exp_f32 v52, v52\n\t"
    "v_exp_f32 v53, v53\n\t"
    "v_mfma_f32_32x32x16_bf16 v[64:79], v[100:103], v[16:19], v[64:79]\n\t"
    "buffer_load_dwordx4 v[228:231], v1, s[16:19], s66 offen offset:1024 sc1\n\t"
    "v_exp_f32 v54, v54\n\t"
    "v_exp_f32 v55, v55\n\t"
    "v_exp_f32 v56, v56\n\t"
    "v_exp_f32 v57, v57\n\t"
    "v_smfmac_f32_32x32x32_bf16 v[144:159], v[104:107], v[48:55], v124\n\t"
    "v_exp_f32 v58, v58\n\t"
    "v_exp_f32 v59, v59\n\t"
    "v_exp_f32 v60, v60\n\t"
    "v_exp_f32 v61, v61\n\t"
    "v_mfma_f32_32x32x16_bf16 v[80:95], v[100:103], v[20:23], v[80:95]\n\t"
    "buffer_load_dwordx4 v[224:227], v1, s[16:19], s66 offen offset:2048 sc1\n\t"
    "v_exp_f32 v62, v62\n\t"
    "v_exp_f32 v63, v63\n\t"
    "v_exp_f32 v64, v64\n\t"
    "v_exp_f32 v65, v65\n\t"
    "v_smfmac_f32_32x32x32_bf16 v[144:159], v[112:115], v[56:63], v124\n\t"
    "s_waitcnt lgkmcnt(0)\n\t"
    "ds_read_b128 v[100:103], v125 offset:11520\n\t"
    "v_exp_f32 v66, v66\n\t"
    "v_exp_f32 v67, v67\n\t"
    "v_exp_f32 v68, v68\n\t"
    "v_exp_f32 v69, v69\n\t"
    "v_mfma_f32_32x32x4_2b_bf16 v[32:63], v[120:121], v[24:25], 0 cbsz:1 abid:0\n\t"
    "ds_read_b128 v[104:107], v127 offset:13056\n\t"
    "v_exp_f32 v70, v70\n\t"
    "v_exp_f32 v71, v71\n\t"
    "v_exp_f32 v72, v72\n\t"
    "v_exp_f32 v73, v73\n\t"
    "v_exp_f32 v74, v74\n\t"
    "v_exp_f32 v75, v75\n\t"
    "v_exp_f32 v76, v76\n\t"
    "v_exp_f32 v77, v77\n\t"
    "v_smfmac_f32_32x32x32_bf16 v[128:143], v[108:111], v[64:71], v124\n\t"
    "ds_read_b128 v[112:115], v127 offset:13728\n\t"
    "buffer_load_dwordx4 v[220:223], v1, s[16:19], s66 offen offset:3072 sc1\n\t"
    "v_exp_f32 v78, v78\n\t"
    "v_exp_f32 v79, v79\n\t"
    "v_exp_f32 v80, v80\n\t"
    "v_exp_f32 v81, v81\n\t"
    "v_smfmac_f32_32x32x32_bf16 v[128:143], v[116:119], v[72:79], v124\n\t"
    "v_exp_f32 v82, v82\n\t"
    "v_exp_f32 v83, v83\n\t"
    "v_exp_f32 v84, v84\n\t"
    "v_exp_f32 v85, v85\n\t"
    "v_mfma_f32_32x32x16_bf16 v[32:47], v[96:99], v[16:19], v[32:47]\n\t"
    "s_add_u32 s66, s66, 0x1000\n\t"
    "buffer_load_dwordx4 v[216:219], v1, s[16:19], s66 offen sc1\n\t"
    "v_exp_f32 v86, v86\n\t"
    "v_exp_f32 v87, v87\n\t"
    "v_exp_f32 v88, v88\n\t"
    "v_exp_f32 v89, v89\n\t"
    "v_smfmac_f32_32x32x32_bf16 v[144:159], v[108:111], v[80:87], v124\n\t"
    "v_exp_f32 v90, v90\n\t"
    "v_exp_f32 v91, v91\n\t"
    "v_exp_f32 v92, v92\n\t"
    "v_exp_f32 v93, v93\n\t"
    "v_mfma_f32_32x32x16_bf16 v[48:63], v[96:99], v[20:23], v[48:63]\n\t"
    "buffer_load_dwordx4 v[212:215], v1, s[16:19], s66 offen offset:1024 sc1\n\t"
    "v_exp_f32 v94, v94\n\t"
    "v_exp_f32 v95, v95\n\t"
    "v_exp_f32 v32, v32\n\t"
    "v_exp_f32 v33, v33\n\t"
    "v_smfmac_f32_32x32x32_bf16 v[144:159], v[116:119], v[88:95], v124\n\t"
    "s_waitcnt lgkmcnt(0)\n\t"
    "ds_read_b128 v[96:99], v125 offset:15744\n\t"
    "v_exp_f32 v34, v34\n\t"
    "v_exp_f32 v35, v35\n\t"
    "v_exp_f32 v36, v36\n\t"
    "v_exp_f32 v37, v37\n\t"
    "v_mfma_f32_32x32x4_2b_bf16 v[64:95], v[120:121], v[24:25], 0 cbsz:1 abid:1\n\t"
    "ds_read_b128 v[108:111], v127 offset:14400\n\t"
    "v_exp_f32 v38, v38\n\t"
    "v_exp_f32 v39, v39\n\t"
    "v_exp_f32 v40, v40\n\t"
    "v_exp_f32 v41, v41\n\t"
    "v_exp_f32 v42, v42\n\t"
    "v_exp_f32 v43, v43\n\t"
    "v_exp_f32 v44, v44\n\t"
    "v_exp_f32 v45, v45\n\t"
    "v_smfmac_f32_32x32x32_bf16 v[128:143], v[104:107], v[32:39], v124\n\t"
    "ds_read_b128 v[116:119], v127 offset:15072\n\t"
    "buffer_load_dwordx4 v[208:211], v1, s[16:19], s66 offen offset:2048 sc1\n\t"
    "v_exp_f32 v46, v46\n\t"
    "v_exp_f32 v47, v47\n\t"
    "v_exp_f32 v48, v48\n\t"
    "v_exp_f32 v49, v49\n\t"
    "v_smfmac_f32_32x32x32_bf16 v[128:143], v[112:115], v[40:47], v124\n\t"
    "ds_read_b64 v[122:123], v126 offset:17792\n\t"
    "v_exp_f32 v50, v50\n\t"
    "v_exp_f32 v51, v51\n\t"
    "v_exp_f32 v52, v52\n\t"
    "v_exp_f32 v53, v53\n\t"
    "v_mfma_f32_32x32x16_bf16 v[64:79], v[100:103], v[16:19], v[64:79]\n\t"
    "buffer_load_dwordx4 v[204:207], v1, s[16:19], s66 offen offset:3072 sc1\n\t"
    "v_exp_f32 v54, v54\n\t"
    "v_exp_f32 v55, v55\n\t"
    "v_exp_f32 v56, v56\n\t"
    "v_exp_f32 v57, v57\n\t"
    "v_smfmac_f32_32x32x32_bf16 v[144:159], v[104:107], v[48:55], v124\n\t"
    "v_exp_f32 v58, v58\n\t"
    "v_exp_f32 v59, v59\n\t"
    "v_exp_f32 v60, v60\n\t"
    "v_exp_f32 v61, v61\n\t"
    "v_mfma_f32_32x32x16_bf16 v[80:95], v[100:103], v[20:23], v[80:95]\n\t"
    "s_add_u32 s66, s66, 0x1000\n\t"
    "buffer_load_dwordx4 v[200:203], v1, s[16:19], s66 offen sc1\n\t"
    "v_exp_f32 v62, v62\n\t"
    "v_exp_f32 v63, v63\n\t"
    "v_exp_f32 v64, v64\n\t"
    "v_exp_f32 v65, v65\n\t"
    "v_smfmac_f32_32x32x32_bf16 v[144:159], v[112:115], v[56:63], v124\n\t"
    "s_waitcnt lgkmcnt(0)\n\t"
    "ds_read_b128 v[100:103], v125 offset:16768\n\t"
    "v_exp_f32 v66, v66\n\t"
    "v_exp_f32 v67, v67\n\t"
    "v_exp_f32 v68, v68\n\t"
    "v_exp_f32 v69, v69\n\t"
    "v_mfma_f32_32x32x4_2b_bf16 v[32:63], v[122:123], v[24:25], 0 cbsz:1 abid:0\n\t"
    "ds_read_b128 v[104:107], v127 offset:18304\n\t"
    "v_exp_f32 v70, v70\n\t"
    "v_exp_f32 v71, v71\n\t"
    "v_exp_f32 v72, v72\n\t"
    "v_exp_f32 v73, v73\n\t"
    "v_exp_f32 v74, v74\n\t"
    "v_exp_f32 v75, v75\n\t"
    "v_exp_f32 v76, v76\n\t"
    "v_exp_f32 v77, v77\n\t"
    "v_smfmac_f32_32x32x32_bf16 v[128:143], v[108:111], v[64:71], v124\n\t"
    "ds_read_b128 v[112:115], v127 offset:18976\n\t"
    "buffer_load_dwordx4 v[196:199], v1, s[16:19], s66 offen offset:1024 sc1\n\t"
    "v_exp_f32 v78, v78\n\t"
    "v_exp_f32 v79, v79\n\t"
    "v_exp_f32 v80, v80\n\t"
    "v_exp_f32 v81, v81\n\t"
    "v_smfmac_f32_32x32x32_bf16 v[128:143], v[116:119], v[72:79], v124\n\t"
    "v_exp_f32 v82, v82\n\t"
    "v_exp_f32 v83, v83\n\t"
    "v_exp_f32 v84, v84\n\t"
    "v_exp_f32 v85, v85\n\t"
    "v_mfma_f32_32x32x16_bf16 v[32:47], v[96:99], v[16:19], v[32:47]\n\t"
    "buffer_load_dwordx4 v[192:195], v1, s[16:19], s66 offen offset:2048 sc1\n\t"
    "v_exp_f32 v86, v86\n\t"
    "v_exp_f32 v87, v87\n\t"
    "v_exp_f32 v88, v88\n\t"
    "v_exp_f32 v89, v89\n\t"
    "v_smfmac_f32_32x32x32_bf16 v[144:159], v[108:111], v[80:87], v124\n\t"
    "v_exp_f32 v90, v90\n\t"
    "v_exp_f32 v91, v91\n\t"
    "v_exp_f32 v92, v92\n\t"
    "v_exp_f32 v93, v93\n\t"
    "v_mfma_f32_32x32x16_bf16 v[48:63], v[96:99], v[20:23], v[48:63]\n\t"
    "buffer_load_dwordx4 v[188:191], v1, s[16:19], s66 offen offset:3072 sc1\n\t"
    "v_exp_f32 v94, v94\n\t"
    "v_exp_f32 v95, v95\n\t"
    "v_exp_f32 v32, v32\n\t"
    "v_exp_f32 v33, v33\n\t"
    "v_smfmac_f32_32x32x32_bf16 v[144:159], v[116:119], v[88:95], v124\n\t"
    "s_waitcnt lgkmcnt(0)\n\t"
    "ds_read_b128 v[96:99], v125 offset:20992\n\t"
    "v_exp_f32 v34, v34\n\t"
    "v_exp_f32 v35, v35\n\t"
    "v_exp_f32 v36, v36\n\t"
    "v_exp_f32 v37, v37\n\t"
    "v_mfma_f32_32x32x4_2b_bf16 v[64:95], v[122:123], v[24:25], 0 cbsz:1 abid:1\n\t"
    "ds_read_b128 v[108:111], v127 offset:19648\n\t"
    "v_exp_f32 v38, v38\n\t"
    "v_exp_f32 v39, v39\n\t"
    "v_exp_f32 v40, v40\n\t"
    "v_exp_f32 v41, v41\n\t"
    "v_exp_f32 v42, v42\n\t"
    "v_exp_f32 v43, v43\n\t"
    "v_exp_f32 v44, v44\n\t"
    "v_exp_f32 v45, v45\n\t"
    "v_smfmac_f32_32x32x32_bf16 v[128:143], v[104:107], v[32:39], v124\n\t"
    "ds_read_b128 v[116:119], v127 offset:20320\n\t"
    "v_exp_f32 v46, v46\n\t"
    "v_exp_f32 v47, v47\n\t"
    "v_exp_f32 v48, v48\n\t"
    "v_exp_f32 v49, v49\n\t"
    "v_smfmac_f32_32x32x32_bf16 v[128:143], v[112:115], v[40:47], v124\n\t"
    "ds_read_b64 v[120:121], v126 offset:23040\n\t"
    "v_exp_f32 v50, v50\n\t"
    "v_exp_f32 v51, v51\n\t"
    "v_exp_f32 v52, v52\n\t"
    "v_exp_f32 v53, v53\n\t"
    "v_mfma_f32_32x32x16_bf16 v[64:79], v[100:103], v[16:19], v[64:79]\n\t"
    "s_add_u32 s66, s66, 0x1000\n\t"
    "buffer_load_dwordx4 v[184:187], v1, s[16:19], s66 offen sc1\n\t"
    "v_exp_f32 v54, v54\n\t"
    "v_exp_f32 v55, v55\n\t"
    "v_exp_f32 v56, v56\n\t"
    "v_exp_f32 v57, v57\n\t"
    "v_smfmac_f32_32x32x32_bf16 v[144:159], v[104:107], v[48:55], v124\n\t"
    "v_exp_f32 v58, v58\n\t"
    "v_exp_f32 v59, v59\n\t"
    "v_exp_f32 v60, v60\n\t"
    "v_exp_f32 v61, v61\n\t"
    "v_mfma_f32_32x32x16_bf16 v[80:95], v[100:103], v[20:23], v[80:95]\n\t"
    "buffer_load_dwordx4 v[180:183], v1, s[16:19], s66 offen offset:1024 sc1\n\t"
    "v_exp_f32 v62, v62\n\t"
    "v_exp_f32 v63, v63\n\t"
    "v_exp_f32 v64, v64\n\t"
    "v_exp_f32 v65, v65\n\t"
    "v_smfmac_f32_32x32x32_bf16 v[144:159], v[112:115], v[56:63], v124\n\t"
    "s_waitcnt lgkmcnt(0)\n\t"
    "ds_read_b128 v[100:103], v125 offset:22016\n\t"
    "v_exp_f32 v66, v66\n\t"
    "v_exp_f32 v67, v67\n\t"
    "v_exp_f32 v68, v68\n\t"
    "v_exp_f32 v69, v69\n\t"
    "v_mfma_f32_32x32x4_2b_bf16 v[32:63], v[120:121], v[24:25], 0 cbsz:1 abid:0\n\t"
    "ds_read_b128 v[104:107], v127 offset:23552\n\t"
    "v_exp_f32 v70, v70\n\t"
    "v_exp_f32 v71, v71\n\t"
    "v_exp_f32 v72, v72\n\t"
    "v_exp_f32 v73, v73\n\t"
    "v_exp_f32 v74, v74\n\t"
    "v_exp_f32 v75, v75\n\t"
    "v_exp_f32 v76, v76\n\t"
    "v_exp_f32 v77, v77\n\t"
    "v_smfmac_f32_32x32x32_bf16 v[128:143], v[108:111], v[64:71], v124\n\t"
    "ds_read_b128 v[112:115], v127 offset:24224\n\t"
    "buffer_load_dwordx4 v[176:179], v1, s[16:19], s66 offen offset:2048 sc1\n\t"
    "v_exp_f32 v78, v78\n\t"
    "v_exp_f32 v79, v79\n\t"
    "v_exp_f32 v80, v80\n\t"
    "v_exp_f32 v81, v81\n\t"
    "v_smfmac_f32_32x32x32_bf16 v[128:143], v[116:119], v[72:79], v124\n\t"
    "v_exp_f32 v82, v82\n\t"
    "v_exp_f32 v83, v83\n\t"
    "v_exp_f32 v84, v84\n\t"
    "v_exp_f32 v85, v85\n\t"
    "v_mfma_f32_32x32x16_bf16 v[32:47], v[96:99], v[16:19], v[32:47]\n\t"
    "buffer_load_dwordx4 v[172:175], v1, s[16:19], s66 offen offset:3072 sc1\n\t"
    "v_exp_f32 v86, v86\n\t"
    "v_exp_f32 v87, v87\n\t"
    "v_exp_f32 v88, v88\n\t"
    "v_exp_f32 v89, v89\n\t"
    "v_smfmac_f32_32x32x32_bf16 v[144:159], v[108:111], v[80:87], v124\n\t"
    "v_exp_f32 v90, v90\n\t"
    "v_exp_f32 v91, v91\n\t"
    "v_exp_f32 v92, v92\n\t"
    "v_exp_f32 v93, v93\n\t"
    "v_mfma_f32_32x32x16_bf16 v[48:63], v[96:99], v[20:23], v[48:63]\n\t"
    "s_add_u32 s66, s66, 0x1000\n\t"
    "buffer_load_dwordx4 v[168:171], v1, s[16:19], s66 offen sc1\n\t"
    "v_exp_f32 v94, v94\n\t"
    "v_exp_f32 v95, v95\n\t"
    "v_exp_f32 v32, v32\n\t"
    "v_exp_f32 v33, v33\n\t"
    "v_smfmac_f32_32x32x32_bf16 v[144:159], v[116:119], v[88:95], v124\n\t"
    "s_waitcnt lgkmcnt(0)\n\t"
    "ds_read_b128 v[96:99], v125 offset:26240\n\t"
    "v_exp_f32 v34, v34\n\t"
    "v_exp_f32 v35, v35\n\t"
    "v_exp_f32 v36, v36\n\t"
    "v_exp_f32 v37, v37\n\t"
    "v_mfma_f32_32x32x4_2b_bf16 v[64:95], v[120:121], v[24:25], 0 cbsz:1 abid:1\n\t"
    "ds_read_b128 v[108:111], v127 offset:24896\n\t"
    "v_exp_f32 v38, v38\n\t"
    "v_exp_f32 v39, v39\n\t"
    "v_exp_f32 v40, v40\n\t"
    "v_exp_f32 v41, v41\n\t"
    "v_exp_f32 v42, v42\n\t"
    "v_exp_f32 v43, v43\n\t"
    "v_exp_f32 v44, v44\n\t"
    "pad6_%=:\n\t"
    "s_cbranch_execz vb9_%=\n\t"
    "v_exp_f32 v45, v45\n\t"
    "v_smfmac_f32_32x32x32_bf16 v[128:143], v[104:107], v[32:39], v124\n\t"
    "ds_read_b128 v[116:119], v127 offset:25568\n\t"
    "s_mul_i32 s67, s34, 0xa000\n\t"
    "s_mul_i32 s32, s82, 0xa000\n\t"
    "s_add_u32 s67, s67, s32\n\t"
    "s_add_u32 s67, s67, 0x241400\n\t"
    "s_mul_i32 m0, s33, 0x4c00\n\t"
    "s_nop 0\n\t"
    "buffer_load_dwordx4 v1, s[16:19], s67 offen sc1 lds\n\t"
    "v_exp_f32 v46, v46\n\t"
    "v_exp_f32 v47, v47\n\t"
    "v_exp_f32 v48, v48\n\t"
    "v_exp_f32 v49, v49\n\t"
    "v_smfmac_f32_32x32x32_bf16 v[128:143], v[112:115], v[40:47], v124\n\t"
    "ds_read_b64 v[122:123], v126 offset:28288\n\t"
    "v_exp_f32 v50, v50\n\t"
    "v_exp_f32 v51, v51\n\t"
    "v_exp_f32 v52, v52\n\t"
    "v_exp_f32 v53, v53\n\t"
    "v_mfma_f32_32x32x16_bf16 v[64:79], v[100:103], v[16:19], v[64:79]\n\t"
    "buffer_load_dwordx4 v1, s[16:19], s67 offen offset:1024 sc1 lds\n\t"
    "v_exp_f32 v54, v54\n\t"
    "v_exp_f32 v55, v55\n\t"
    "v_exp_f32 v56, v56\n\t"
    "v_exp_f32 v57, v57\n\t"
    "v_smfmac_f32_32x32x32_bf16 v[144:159], v[104:107], v[48:55], v124\n\t"
    "v_exp_f32 v58, v58\n\t"
    "v_exp_f32 v59, v59\n\t"
    "v_exp_f32 v60, v60\n\t"
    "v_exp_f32 v61, v61\n\t"
    "v_mfma_f32_32x32x16_bf16 v[80:95], v[100:103], v[20:23], v[80:95]\n\t"
    "buffer_load_dwordx4 v1, s[16:19], s67 offen offset:2048 sc1 lds\n\t"
    "v_exp_f32 v62, v62\n\t"
    "v_exp_f32 v63, v63\n\t"
    "v_exp_f32 v64, v64\n\t"
    "v_exp_f32 v65, v65\n\t"
    "v_smfmac_f32_32x32x32_bf16 v[144:159], v[112:115], v[56:63], v124\n\t"
    "s_waitcnt lgkmcnt(0)\n\t"
    "ds_read_b128 v[100:103], v125 offset:27264\n\t"
    "v_exp_f32 v66, v66\n\t"
    "v_exp_f32 v67, v67\n\t"
    "v_exp_f32 v68, v68\n\t"
    "v_exp_f32 v69, v69\n\t"
    "v_mfma_f32_32x32x4_2b_bf16 v[32:63], v[122:123], v[24:25], 0 cbsz:1 abid:0\n\t"
    "ds_read_b128 v[104:107], v127 offset:28800\n\t"
    "v_exp_f32 v70, v70\n\t"
    "v_exp_f32 v71, v71\n\t"
    "v_exp_f32 v72, v72\n\t"
    "v_exp_f32 v73, v73\n\t"
    "v_exp_f32 v74, v74\n\t"
    "v_exp_f32 v75, v75\n\t"
    "v_exp_f32 v76, v76\n\t"
    "v_exp_f32 v77, v77\n\t"
    "v_smfmac_f32_32x32x32_bf16 v[128:143], v[108:111], v[64:71], v124\n\t"
    "ds_read_b128 v[112:115], v127 offset:29472\n\t"
    "buffer_load_dwordx4 v1, s[16:19], s67 offen offset:3072 sc1 lds\n\t"
    "v_exp_f32 v78, v78\n\t"
    "v_exp_f32 v79, v79\n\t"
    "v_exp_f32 v80, v80\n\t"
    "v_exp_f32 v81, v81\n\t"
    "v_smfmac_f32_32x32x32_bf16 v[128:143], v[116:119], v[72:79], v124\n\t"
    "v_exp_f32 v82, v82\n\t"
    "v_exp_f32 v83, v83\n\t"
    "v_exp_f32 v84, v84\n\t"
    "v_exp_f32 v85, v85\n\t"
    "v_mfma_f32_32x32x16_bf16 v[32:47], v[96:99], v[16:19], v[32:47]\n\t"
    "s_add_u32 m0, m0, 0x1000\n\t"
    "s_add_u32 s67, s67, 0x1000\n\t"
    "buffer_load_dwordx4 v1, s[16:19], s67 offen sc1 lds\n\t"
    "v_exp_f32 v86, v86\n\t"
    "v_exp_f32 v87, v87\n\t"
    "v_exp_f32 v88, v88\n\t"
    "v_exp_f32 v89, v89\n\t"
    "v_smfmac_f32_32x32x32_bf16 v[144:159], v[108:111], v[80:87], v124\n\t"
    "v_exp_f32 v90, v90\n\t"
    "v_exp_f32 v91, v91\n\t"
    "v_exp_f32 v92, v92\n\t"
    "v_exp_f32 v93, v93\n\t"
    "v_mfma_f32_32x32x16_bf16 v[48:63], v[96:99], v[20:23], v[48:63]\n\t"
    "buffer_load_dwordx4 v1, s[16:19], s67 offen offset:1024 sc1 lds\n\t"
    "v_exp_f32 v94, v94\n\t"
    "v_exp_f32 v95, v95\n\t"
    "s_waitcnt vmcnt(63)\n\t"
    "s_barrier\n\t"
    "v_exp_f32 v32, v32\n\t"
    "v_exp_f32 v33, v33\n\t"
    "v_smfmac_f32_32x32x32_bf16 v[144:159], v[116:119], v[88:95], v124\n\t"
    "s_waitcnt lgkmcnt(0)\n\t"
    "ds_read_b128 v[96:99], v125 offset:31488\n\t"
    "v_exp_f32 v34, v34\n\t"
    "v_exp_f32 v35, v35\n\t"
    "v_exp_f32 v36, v36\n\t"
    "v_exp_f32 v37, v37\n\t"
    "v_mfma_f32_32x32x4_2b_bf16 v[64:95], v[122:123], v[24:25], 0 cbsz:1 abid:1\n\t"
    "ds_read_b128 v[108:111], v127 offset:30144\n\t"
    "v_exp_f32 v38, v38\n\t"
    "v_exp_f32 v39, v39\n\t"
    "v_exp_f32 v40, v40\n\t"
    "v_exp_f32 v41, v41\n\t"
    "v_exp_f32 v42, v42\n\t"
    "v_exp_f32 v43, v43\n\t"
    "v_exp_f32 v44, v44\n\t"
    "v_exp_f32 v45, v45\n\t"
    "v_smfmac_f32_32x32x32_bf16 v[128:143], v[104:107], v[32:39], v124\n\t"
    "ds_read_b128 v[116:119], v127 offset:30816\n\t"
    "buffer_load_dwordx4 v1, s[16:19], s67 offen offset:2048 sc1 lds\n\t"
    "v_exp_f32 v46, v46\n\t"
    "v_exp_f32 v47, v47\n\t"
    "v_exp_f32 v48, v48\n\t"
    "v_exp_f32 v49, v49\n\t"
    "v_smfmac_f32_32x32x32_bf16 v[128:143], v[112:115], v[40:47], v124\n\t"
    "ds_read_b64 v[120:121], v126 offset:33536\n\t"
    "v_exp_f32 v50, v50\n\t"
    "v_exp_f32 v51, v51\n\t"
    "v_exp_f32 v52, v52\n\t"
    "v_exp_f32 v53, v53\n\t"
    "v_mfma_f32_32x32x16_bf16 v[64:79], v[100:103], v[16:19], v[64:79]\n\t"
    "buffer_load_dwordx4 v1, s[16:19], s67 offen offset:3072 sc1 lds\n\t"
    "v_exp_f32 v54, v54\n\t"
    "v_exp_f32 v55, v55\n\t"
    "v_exp_f32 v56, v56\n\t"
    "v_exp_f32 v57, v57\n\t"
    "v_smfmac_f32_32x32x32_bf16 v[144:159], v[104:107], v[48:55], v124\n\t"
    "v_exp_f32 v58, v58\n\t"
    "v_exp_f32 v59, v59\n\t"
    "v_exp_f32 v60, v60\n\t"
    "v_exp_f32 v61, v61\n\t"
    "v_mfma_f32_32x32x16_bf16 v[80:95], v[100:103], v[20:23], v[80:95]\n\t"
    "s_add_u32 m0, m0, 0x1000\n\t"
    "s_add_u32 s67, s67, 0x1000\n\t"
    "buffer_load_dwordx4 v1, s[16:19], s67 offen sc1 lds\n\t"
    "v_exp_f32 v62, v62\n\t"
    "v_exp_f32 v63, v63\n\t"
    "v_exp_f32 v64, v64\n\t"
    "v_exp_f32 v65, v65\n\t"
    "v_smfmac_f32_32x32x32_bf16 v[144:159], v[112:115], v[56:63], v124\n\t"
    "s_waitcnt lgkmcnt(0)\n\t"
    "ds_read_b128 v[100:103], v125 offset:32512\n\t"
    "v_exp_f32 v66, v66\n\t"
    "v_exp_f32 v67, v67\n\t"
    "v_exp_f32 v68, v68\n\t"
    "v_exp_f32 v69, v69\n\t"
    "v_mfma_f32_32x32x4_2b_bf16 v[32:63], v[120:121], v[24:25], 0 cbsz:1 abid:0\n\t"
    "ds_read_b128 v[104:107], v127 offset:34048\n\t"
    "v_exp_f32 v70, v70\n\t"
    "v_exp_f32 v71, v71\n\t"
    "v_exp_f32 v72, v72\n\t"
    "v_exp_f32 v73, v73\n\t"
    "v_exp_f32 v74, v74\n\t"
    "v_exp_f32 v75, v75\n\t"
    "v_exp_f32 v76, v76\n\t"
    "v_exp_f32 v77, v77\n\t"
    "v_smfmac_f32_32x32x32_bf16 v[128:143], v[108:111], v[64:71], v124\n\t"
    "ds_read_b128 v[112:115], v127 offset:34720\n\t"
    "buffer_load_dwordx4 v1, s[16:19], s67 offen offset:1024 sc1 lds\n\t"
    "v_exp_f32 v78, v78\n\t"
    "v_exp_f32 v79, v79\n\t"
    "v_exp_f32 v80, v80\n\t"
    "v_exp_f32 v81, v81\n\t"
    "v_smfmac_f32_32x32x32_bf16 v[128:143], v[116:119], v[72:79], v124\n\t"
    "v_exp_f32 v82, v82\n\t"
    "v_exp_f32 v83, v83\n\t"
    "v_exp_f32 v84, v84\n\t"
    "v_exp_f32 v85, v85\n\t"
    "v_mfma_f32_32x32x16_bf16 v[32:47], v[96:99], v[16:19], v[32:47]\n\t"
    "buffer_load_dwordx4 v1, s[16:19], s67 offen offset:2048 sc1 lds\n\t"
    "v_exp_f32 v86, v86\n\t"
    "v_exp_f32 v87, v87\n\t"
    "v_exp_f32 v88, v88\n\t"
    "v_exp_f32 v89, v89\n\t"
    "v_smfmac_f32_32x32x32_bf16 v[144:159], v[108:111], v[80:87], v124\n\t"
    "v_exp_f32 v90, v90\n\t"
    "v_exp_f32 v91, v91\n\t"
    "v_exp_f32 v92, v92\n\t"
    "v_exp_f32 v93, v93\n\t"
    "v_mfma_f32_32x32x16_bf16 v[48:63], v[96:99], v[20:23], v[48:63]\n\t"
    "buffer_load_dwordx4 v1, s[16:19], s67 offen offset:3072 sc1 lds\n\t"
    "v_exp_f32 v94, v94\n\t"
    "v_exp_f32 v95, v95\n\t"
    "v_exp_f32 v32, v32\n\t"
    "v_exp_f32 v33, v33\n\t"
    "v_smfmac_f32_32x32x32_bf16 v[144:159], v[116:119], v[88:95], v124\n\t"
    "s_waitcnt lgkmcnt(0)\n\t"
    "v_add_u32 v125, 0xffff0a00, v125\n\t"
    "ds_read_b128 v[96:99], v125 offset:15744\n\t"
    "v_exp_f32 v34, v34\n\t"
    "v_exp_f32 v35, v35\n\t"
    "v_exp_f32 v36, v36\n\t"
    "v_exp_f32 v37, v37\n\t"
    "v_mfma_f32_32x32x4_2b_bf16 v[64:95], v[120:121], v[24:25], 0 cbsz:1 abid:1\n\t"
    "ds_read_b128 v[108:111], v127 offset:35392\n\t"
    "v_exp_f32 v38, v38\n\t"
    "v_exp_f32 v39, v39\n\t"
    "v_exp_f32 v40, v40\n\t"
    "v_exp_f32 v41, v41\n\t"
    "v_exp_f32 v42, v42\n\t"
    "v_exp_f32 v43, v43\n\t"
    "v_exp_f32 v44, v44\n\t"
    "v_exp_f32 v45, v45\n\t"
    "v_smfmac_f32_32x32x32_bf16 v[128:143], v[104:107], v[32:39], v124\n\t"
    "ds_read_b128 v[116:119], v127 offset:36064\n\t"
    "s_add_u32 m0, m0, 0x1000\n\t"
    "s_add_u32 s67, s67, 0x1000\n\t"
    "buffer_load_dwordx4 v1, s[16:19], s67 offen sc1 lds\n\t"
    "v_exp_f32 v46, v46\n\t"
    "v_exp_f32 v47, v47\n\t"
    "v_exp_f32 v48, v48\n\t"
    "v_exp_f32 v49, v49\n\t"
    "v_smfmac_f32_32x32x32_bf16 v[128:143], v[112:115], v[40:47], v124\n\t"
    "v_add_u32 v126, 0xffff0a00, v126\n\t"
    "ds_read_b64 v[122:123], v126 offset:17792\n\t"
    "v_exp_f32 v50, v50\n\t"
    "v_exp_f32 v51, v51\n\t"
    "v_exp_f32 v52, v52\n\t"
    "v_exp_f32 v53, v53\n\t"
    "v_mfma_f32_32x32x16_bf16 v[64:79], v[100:103], v[16:19], v[64:79]\n\t"
    "buffer_load_dwordx4 v1, s[16:19], s67 offen offset:1024 sc1 lds\n\t"
    "v_exp_f32 v54, v54\n\t"
    "v_exp_f32 v55, v55\n\t"
    "v_exp_f32 v56, v56\n\t"
    "v_exp_f32 v57, v57\n\t"
    "v_smfmac_f32_32x32x32_bf16 v[144:159], v[104:107], v[48:55], v124\n\t"
    "v_exp_f32 v58, v58\n\t"
    "v_exp_f32 v59, v59\n\t"
    "v_exp_f32 v60, v60\n\t"
    "v_exp_f32 v61, v61\n\t"
    "v_mfma_f32_32x32x16_bf16 v[80:95], v[100:103], v[20:23], v[80:95]\n\t"
    "buffer_load_dwordx4 v1, s[16:19], s67 offen offset:2048 sc1 lds\n\t"
    "v_exp_f32 v62, v62\n\t"
    "v_exp_f32 v63, v63\n\t"
    "v_exp_f32 v64, v64\n\t"
    "v_exp_f32 v65, v65\n\t"
    "v_smfmac_f32_32x32x32_bf16 v[144:159], v[112:115], v[56:63], v124\n\t"
    "s_waitcnt lgkmcnt(0)\n\t"
    "ds_read_b128 v[100:103], v125 offset:16768\n\t"
    "v_exp_f32 v66, v66\n\t"
    "v_exp_f32 v67, v67\n\t"
    "v_exp_f32 v68, v68\n\t"
    "v_exp_f32 v69, v69\n\t"
    "v_mfma_f32_32x32x4_2b_bf16 v[32:63], v[122:123], v[24:25], 0 cbsz:1 abid:0\n\t"
    "v_add_u32 v127, 0xffff0a00, v127\n\t"
    "ds_read_b128 v[104:107], v127 offset:18304\n\t"
    "v_exp_f32 v70, v70\n\t"
    "v_exp_f32 v71, v71\n\t"
    "v_exp_f32 v72, v72\n\t"
    "v_exp_f32 v73, v73\n\t"
    "v_exp_f32 v74, v74\n\t"
    "v_exp_f32 v75, v75\n\t"
    "v_exp_f32 v76, v76\n\t"
    "v_exp_f32 v77, v77\n\t"
    "v_smfmac_f32_32x32x32_bf16 v[128:143], v[108:111], v[64:71], v124\n\t"
    "ds_read_b128 v[112:115], v127 offset:18976\n\t"
    "buffer_load_dwordx4 v1, s[16:19], s67 offen offset:3072 sc1 lds\n\t"
    "v_exp_f32 v78, v78\n\t"
    "v_exp_f32 v79, v79\n\t"
    "v_exp_f32 v80, v80\n\t"
    "v_exp_f32 v81, v81\n\t"
    "v_smfmac_f32_32x32x32_bf16 v[128:143], v[116:119], v[72:79], v124\n\t"
    "v_exp_f32 v82, v82\n\t"
    "v_exp_f32 v83, v83\n\t"
    "v_exp_f32 v84, v84\n\t"
    "v_exp_f32 v85, v85\n\t"
    "v_mfma_f32_32x32x16_bf16 v[32:47], v[96:99], v[16:19], v[32:47]\n\t"
    "s_add_u32 m0, m0, 0x1000\n\t"
    "s_add_u32 s67, s67, 0x1000\n\t"
    "buffer_load_dwordx4 v1, s[16:19], s67 offen sc1 lds\n\t"
    "v_exp_f32 v86, v86\n\t"
    "v_exp_f32 v87, v87\n\t"
    "v_exp_f32 v88, v88\n\t"
    "v_exp_f32 v89, v89\n\t"
    "v_smfmac_f32_32x32x32_bf16 v[144:159], v[108:111], v[80:87], v124\n\t"
    "v_exp_f32 v90, v90\n\t"
    "v_exp_f32 v91, v91\n\t"
    "v_exp_f32 v92, v92\n\t"
    "v_exp_f32 v93, v93\n\t"
    "v_mfma_f32_32x32x16_bf16 v[48:63], v[96:99], v[20:23], v[48:63]\n\t"
    "buffer_load_dwordx4 v1, s[16:19], s67 offen offset:1024 sc1 lds\n\t"
    "v_exp_f32 v94, v94\n\t"
    "v_exp_f32 v95, v95\n\t"
    "v_exp_f32 v32, v32\n\t"
    "v_exp_f32 v33, v33\n\t"
    "v_smfmac_f32_32x32x32_bf16 v[144:159], v[116:119], v[88:95], v124\n\t"
    "s_waitcnt lgkmcnt(0)\n\t"
    "v_exp_f32 v34, v34\n\t"
    "v_exp_f32 v35, v35\n\t"
    "v_exp_f32 v36, v36\n\t"
    "v_exp_f32 v37, v37\n\t"
    "v_mfma_f32_32x32x4_2b_bf16 v[64:95], v[122:123], v[24:25], 0 cbsz:1 abid:1\n\t"
    "ds_read_b128 v[108:111], v127 offset:19648\n\t"
    "v_exp_f32 v38, v38\n\t"
    "v_exp_f32 v39, v39\n\t"
    "v_exp_f32 v40, v40\n\t"
    "v_exp_f32 v41, v41\n\t"
    "v_exp_f32 v42, v42\n\t"
    "v_exp_f32 v43, v43\n\t"
    "v_exp_f32 v44, v44\n\t"
    "v_exp_f32 v45, v45\n\t"
    "v_smfmac_f32_32x32x32_bf16 v[128:143], v[104:107], v[32:39], v124\n\t"
    "ds_read_b128 v[116:119], v127 offset:20320\n\t"
    "buffer_load_dwordx4 v1, s[16:19], s67 offen offset:2048 sc1 lds\n\t"
    "v_exp_f32 v46, v46\n\t"
    "v_exp_f32 v47, v47\n\t"
    "v_exp_f32 v48, v48\n\t"
    "v_exp_f32 v49, v49\n\t"
    "v_smfmac_f32_32x32x32_bf16 v[128:143], v[112:115], v[40:47], v124\n\t"
    "v_exp_f32 v50, v50\n\t"
    "v_exp_f32 v51, v51\n\t"
    "v_exp_f32 v52, v52\n\t"
    "v_exp_f32 v53, v53\n\t"
    "v_mfma_f32_32x32x16_bf16 v[64:79], v[100:103], v[16:19], v[64:79]\n\t"
    "v_exp_f32 v54, v54\n\t"
    "v_exp_f32 v55, v55\n\t"
    "v_exp_f32 v56, v56\n\t"
    "v_exp_f32 v57, v57\n\t"
    "v_smfmac_f32_32x32x32_bf16 v[144:159], v[104:107], v[48:55], v124\n\t"
    "v_exp_f32 v58, v58\n\t"
    "v_exp_f32 v59, v59\n\t"
    "v_exp_f32 v60, v60\n\t"
    "v_exp_f32 v61, v61\n\t"
    "v_mfma_f32_32x32x16_bf16 v[80:95], v[100:103], v[20:23], v[80:95]\n\t"
    "v_exp_f32 v62, v62\n\t"
    "v_exp_f32 v63, v63\n\t"
    "v_exp_f32 v64, v64\n\t"
    "v_exp_f32 v65, v65\n\t"
    "v_smfmac_f32_32x32x32_bf16 v[144:159], v[112:115], v[56:63], v124\n\t"
    "s_waitcnt lgkmcnt(0)\n\t"
    "v_exp_f32 v66, v66\n\t"
    "v_exp_f32 v67, v67\n\t"
    "v_exp_f32 v68, v68\n\t"
    "v_exp_f32 v69, v69\n\t"
    "v_exp_f32 v70, v70\n\t"
    "v_exp_f32 v71, v71\n\t"
    "v_exp_f32 v72, v72\n\t"
    "v_exp_f32 v73, v73\n\t"
    "v_exp_f32 v74, v74\n\t"
    "v_exp_f32 v75, v75\n\t"
    "v_exp_f32 v76, v76\n\t"
    "v_exp_f32 v77, v77\n\t"
    "v_smfmac_f32_32x32x32_bf16 v[128:143], v[108:111], v[64:71], v124\n\t"
    "v_exp_f32 v78, v78\n\t"
    "v_exp_f32 v79, v79\n\t"
    "v_exp_f32 v80, v80\n\t"
    "v_exp_f32 v81, v81\n\t"
    "v_smfmac_f32_32x32x32_bf16 v[128:143], v[116:119], v[72:79], v124\n\t"
    "v_exp_f32 v82, v82\n\t"
    "v_exp_f32 v83, v83\n\t"
    "v_exp_f32 v84, v84\n\t"
    "v_exp_f32 v85, v85\n\t"
    "v_exp_f32 v86, v86\n\t"
    "v_exp_f32 v87, v87\n\t"
    "v_exp_f32 v88, v88\n\t"
    "v_exp_f32 v89, v89\n\t"
    "v_smfmac_f32_32x32x32_bf16 v[144:159], v[108:111], v[80:87], v124\n\t"
    "v_exp_f32 v90, v90\n\t"
    "v_exp_f32 v91, v91\n\t"
    "v_exp_f32 v92, v92\n\t"
    "v_exp_f32 v93, v93\n\t"
    "v_exp_f32 v94, v94\n\t"
    "v_exp_f32 v95, v95\n\t"
    "s_nop 1\n\t"
    "v_smfmac_f32_32x32x32_bf16 v[144:159], v[116:119], v[88:95], v124\n\t"
    "v_rcp_f32 v36, v140\n\t"
    "v_add_u32 v32, 0xcf800000, v140\n\t"
    "v_mul_f32 v128, v128, v36\n\t"
    "v_mul_f32 v129, v129, v36\n\t"
    "v_mul_f32 v130, v130, v36\n\t"
    "v_mul_f32 v131, v131, v36\n\t"
    "v_mul_f32 v132, v132, v36\n\t"
    "v_mul_f32 v133, v133, v36\n\t"
    "v_mul_f32 v134, v134, v36\n\t"
    "v_mul_f32 v135, v135, v36\n\t"
    "v_mul_f32 v136, v136, v36\n\t"
    "v_mul_f32 v137, v137, v36\n\t"
    "v_mul_f32 v138, v138, v36\n\t"
    "v_mul_f32 v139, v139, v36\n\t"
    "v_cvt_pk_bf16_f32 v40, v128, v129\n\t"
    "v_cvt_pk_bf16_f32 v41, v130, v131\n\t"
    "v_cvt_pk_bf16_f32 v42, v132, v133\n\t"
    "v_cvt_pk_bf16_f32 v43, v134, v135\n\t"
    "buffer_store_dwordx4 v[40:43], v26, s[12:15], s78 offen sc1\n\t"
    "v_cvt_pk_bf16_f32 v48, v136, v137\n\t"
    "v_cvt_pk_bf16_f32 v49, v138, v139\n\t"
    "v_add_u32 v33, 0xcf800000, v156\n\t"
    "v_max_u32 v32, v32, v33\n\t"
    "v_cmp_lt_u32 vcc, 0x2d000000, v32\n\t"
    "s_nop 0\n\t"
    "s_cbranch_vccnz slow28_%=\n\t"
    "back31_%=:\n\t"
    "v_rcp_f32 v36, v156\n\t"
    "s_nop 0\n\t"
    "v_pk_mul_f32 v[144:145], v[144:145], v[36:37] op_sel_hi:[1,0]\n\t"
    "v_pk_mul_f32 v[146:147], v[146:147], v[36:37] op_sel_hi:[1,0]\n\t"
    "v_pk_mul_f32 v[148:149], v[148:149], v[36:37] op_sel_hi:[1,0]\n\t"
    "v_pk_mul_f32 v[150:151], v[150:151], v[36:37] op_sel_hi:[1,0]\n\t"
    "v_pk_mul_f32 v[152:153], v[152:153], v[36:37] op_sel_hi:[1,0]\n\t"
    "v_pk_mul_f32 v[154:155], v[154:155], v[36:37] op_sel_hi:[1,0]\n\t"
    "v_cvt_pk_bf16_f32 v44, v144, v145\n\t"
    "v_cvt_pk_bf16_f32 v45, v146, v147\n\t"
    "v_cvt_pk_bf16_f32 v46, v148, v149\n\t"
    "v_cvt_pk_bf16_f32 v47, v150, v151\n\t"
    "buffer_store_dwordx4 v[44:47], v26, s[12:15], s79 offen sc1\n\t"
    "v_cvt_pk_bf16_f32 v50, v152, v153\n\t"
    "v_cvt_pk_bf16_f32 v51, v154, v155\n\t"
    "s_mov_b32 exec_hi, 0\n\t"
    "buffer_store_dwordx4 v[48:51], v27, s[12:15], s80 offen sc1\n\t"
    "s_mov_b64 exec, -1\n\t"
    "v_lshlrev_b32 v104, 16, v4\n\t"
    "v_and_b32 v105, 0xffff0000, v4\n\t"
    "v_lshlrev_b32 v106, 16, v5\n\t"
    "v_and_b32 v107, 0xffff0000, v5\n\t"
    "v_lshlrev_b32 v108, 16, v6\n\t"
    "v_and_b32 v109, 0xffff0000, v6\n\t"
    "v_lshlrev_b32 v110, 16, v7\n\t"
    "v_and_b32 v111, 0xffff0000, v7\n\t"
    "v_lshlrev_b32 v112, 16, v8\n\t"
    "v_and_b32 v113, 0xffff0000, v8\n\t"
    "v_lshlrev_b32 v114, 16, v9\n\t"
    "v_and_b32 v115, 0xffff0000, v9\n\t"
    "v_lshlrev_b32 v116, 16, v10\n\t"
    "v_and_b32 v117, 0xffff0000, v10\n\t"
    "v_lshlrev_b32 v118, 16, v11\n\t"
    "v_and_b32 v119, 0xffff0000, v11\n\t"
    "v_lshlrev_b32 v120, 16, v12\n\t"
    "v_and_b32 v121, 0xffff0000, v12\n\t"
    "v_lshlrev_b32 v122, 16, v13\n\t"
    "v_and_b32 v123, 0xffff0000, v13\n\t"
    "s_add_u32 s62, s47, s72\n\t"
    "s_add_u32 s63, s47, s73\n\t"
    "s_mul_i32 s64, s34, 0x80\n\t"
    "s_mul_i32 s53, s33, 0x400\n\t"
    "s_add_u32 s64, s64, s53\n\t"
    "s_add_u32 s64, s64, s47\n\t"
    "s_add_u32 s64, s64, 0x14000\n\t"
    "s_mov_b32 s65, 0x3f803f80\n\t"
    "v_and_b32 v24, 15, v0\n\t"
    "v_lshlrev_b32 v24, 2, v24\n\t"
    "v_lshrrev_b32 v30, 5, v0\n\t"
    "v_lshl_add_u32 v24, v30, 6, v24\n\t"
    "s_mul_i32 s54, s33, 0x1000\n\t"
    "s_add_u32 s54, s54, s46\n\t"
    "s_add_u32 s55, s54, 0x4000\n\t"
    "s_add_u32 s56, s54, 0x8000\n\t"
    "s_add_u32 s57, s54, 0xc000\n\t"
    "s_mul_i32 s58, s33, 0x800\n\t"
    "s_add_u32 s58, s58, s46\n\t"
    "s_add_u32 s58, s58, 0x10000\n\t"
    "s_add_u32 s59, s58, 0x2000\n\t"
    "s_mul_i32 s50, s33, 0x3c00\n\t"
    "s_add_u32 s50, s50, 0x13000\n\t"
    "v_add_u32 v25, s50, v1\n\t"
    "s_mul_i32 s50, s86, 0x3c00\n\t"
    "s_add_u32 s50, s50, 0x13000\n\t"
    "v_add_u32 v164, s50, v1\n\t"
    "s_mul_i32 s50, s85, 0x3c00\n\t"
    "s_add_u32 s50, s50, 0x14400\n\t"
    "v_add_u32 v165, s50, v1\n\t"
    "s_mul_i32 s50, s84, 0x3c00\n\t"
    "s_add_u32 s50, s50, 0x15800\n\t"
    "v_add_u32 v166, s50, v1\n\t"
    "s_cmp_eq_u32 s33, 0\n\t"
    "s_waitcnt vmcnt(0)\n\t"
    "s_barrier\n\t"
    "s_cbranch_scc0 L32_%=\n\t"
    "s_mov_b64 exec, 1\n\t"
    "buffer_store_dwordx2 v[2:3], v1, s[12:15], s76 offen sc1\n\t"
    "s_mov_b64 exec, -1\n\t"
    "L32_%=:\n\t"
    "s_mov_b32 exec_lo, 0x1010101\n\t"
    "s_mov_b32 exec_hi, 0x1010101\n\t"
    "buffer_load_dword v29, v26, s[12:15], s78 offen sc1\n\t"
    "buffer_load_dword v29, v27, s[12:15], s80 offen sc1\n\t"
    "buffer_load_dword v29, v26, s[12:15], s79 offen sc1\n\t"
    "s_mov_b64 exec, -1\n\t"
    "v_mov_b32 v92, 0x3f803f80\n\t"
    "v_mov_b32 v93, 0x3f803f80\n\t"
    "v_mov_b32 v94, 0x3f803f80\n\t"
    "v_mov_b32 v95, 0x3f803f80\n\t"
    "s_nop 1\n\t"
    "v_mfma_f32_16x16x32_bf16 v[84:87], v[92:95], a[100:103], 0\n\t"
    "v_mfma_f32_16x16x32_bf16 v[88:91], v[92:95], a[104:107], 0\n\t"
    "v_mfma_f32_16x16x32_bf16 v[84:87], v[92:95], a[108:111], v[84:87]\n\t"
    "v_mfma_f32_16x16x32_bf16 v[88:91], v[92:95], a[112:115], v[88:91]\n\t"
    "v_mfma_f32_16x16x32_bf16 v[84:87], v[92:95], a[116:119], v[84:87]\n\t"
    "v_mfma_f32_16x16x32_bf16 v[88:91], v[92:95], a[120:123], v[88:91]\n\t"
    "v_mfma_f32_16x16x32_bf16 v[84:87], v[92:95], a[124:127], v[84:87]\n\t"
    "v_mfma_f32_16x16x32_bf16 v[88:91], v[92:95], a[128:131], v[88:91]\n\t"
    "v_mfma_f32_16x16x32_bf16 v[84:87], v[92:95], a[132:135], v[84:87]\n\t"
    "v_mfma_f32_16x16x32_bf16 v[88:91], v[92:95], a[136:139], v[88:91]\n\t"
    "v_mfma_f32_16x16x32_bf16 v[84:87], v[92:95], a[140:143], v[84:87]\n\t"
    "v_mfma_f32_16x16x32_bf16 v[88:91], v[92:95], a[144:147], v[88:91]\n\t"
    "v_mfma_f32_16x16x32_bf16 v[84:87], v[92:95], a[148:151], v[84:87]\n\t"
    "v_mfma_f32_16x16x32_bf16 v[88:91], v[92:95], a[152:155], v[88:91]\n\t"
    "v_mfma_f32_16x16x32_bf16 v[84:87], v[92:95], a[156:159], v[84:87]\n\t"
    "v_mfma_f32_16x16x32_bf16 v[88:91], v[92:95], a[160:163], v[88:91]\n\t"
    "v_mfma_f32_16x16x32_bf16 v[84:87], v[92:95], a[164:167], v[84:87]\n\t"
    "v_mfma_f32_16x16x32_bf16 v[88:91], v[92:95], a[168:171], v[88:91]\n\t"
    "v_mfma_f32_16x16x32_bf16 v[84:87], v[92:95], a[172:175], v[84:87]\n\t"
    "v_mfma_f32_16x16x32_bf16 v[88:91], v[92:95], a[176:179], v[88:91]\n\t"
    "s_cmp_eq_u32 s33, 0\n\t"
    "s_cbranch_scc0 L33_%=\n\t"
    "buffer_load_dwordx2 v[16:17], v15, s[12:15], s49 offen sc1\n\t"
    "L33_%=:\n\t"
    "v_mfma_f32_16x16x32_bf16 v[84:87], v[92:95], a[180:183], v[84:87]\n\t"
    "v_mfma_f32_16x16x32_bf16 v[88:91], v[92:95], a[184:187], v[88:91]\n\t"
    "v_mfma_f32_16x16x32_bf16 v[84:87], v[92:95], a[188:191], v[84:87]\n\t"
    "v_mfma_f32_16x16x32_bf16 v[88:91], v[92:95], a[192:195], v[88:91]\n\t"
    "v_mfma_f32_16x16x32_bf16 v[84:87], v[92:95], a[196:199], v[84:87]\n\t"
    "v_mfma_f32_16x16x32_bf16 v[88:91], v[92:95], a[200:203], v[88:91]\n\t"
    "v_mfma_f32_16x16x32_bf16 v[84:87], v[92:95], a[204:207], v[84:87]\n\t"
    "v_mfma_f32_16x16x32_bf16 v[88:91], v[92:95], a[208:211], v[88:91]\n\t"
    "v_mfma_f32_16x16x32_bf16 v[84:87], v[92:95], a[212:215], v[84:87]\n\t"
    "v_mfma_f32_16x16x32_bf16 v[88:91], v[92:95], a[216:219], v[88:91]\n\t"
    "v_mfma_f32_16x16x32_bf16 v[84:87], v[92:95], a[220:223], v[84:87]\n\t"
    "v_mfma_f32_16x16x32_bf16 v[88:91], v[92:95], a[224:227], v[88:91]\n\t"
    "v_mfma_f32_16x16x32_bf16 v[84:87], v[92:95], a[228:231], v[84:87]\n\t"
    "v_mfma_f32_16x16x32_bf16 v[88:91], v[92:95], a[232:235], v[88:91]\n\t"
    "v_mfma_f32_16x16x32_bf16 v[84:87], v[92:95], a[236:239], v[84:87]\n\t"
    "v_mfma_f32_16x16x32_bf16 v[88:91], v[92:95], a[240:243], v[88:91]\n\t"
    "v_mfma_f32_16x16x32_bf16 v[84:87], v[92:95], a[244:247], v[84:87]\n\t"
    "v_mfma_f32_16x16x32_bf16 v[88:91], v[92:95], a[248:251], v[88:91]\n\t"
    "v_mfma_f32_16x16x32_bf16 v[84:87], v[92:95], a[252:255], v[84:87]\n\t"
    "v_mfma_f32_16x16x32_bf16 v[88:91], v[92:95], v[252:255], v[88:91]\n\t"
    "s_nop 6\n\t"
    "v_mov_b32 v26, v84\n\t"
    "v_mov_b32 v27, v88\n\t"
    "s_cmp_eq_u32 s33, 0\n\t"
    "s_cbranch_scc0 L34_%=\n\t"
    "s_waitcnt vmcnt(0)\n\t"
    "v_cmp_ne_u64 vcc, v[2:3], v[16:17]\n\t"
    "s_nop 0\n\t"
    "s_cbranch_vccz L35_%=\n\t"
    "poll36_%=:\n\t"
    "s_sleep 2\n\t"
    "buffer_load_dwordx2 v[16:17], v15, s[12:15], s49 offen sc1\n\t"
    "s_waitcnt vmcnt(0)\n\t"
    "v_cmp_ne_u64 vcc, v[2:3], v[16:17]\n\t"
    "s_nop 0\n\t"
    "s_cbranch_vccz L35_%=\n\t"
    "s_sub_u32 s37, s37, 1\n\t"
    "s_cbranch_scc0 poll36_%=\n\t"
    "s_mov_b32 s37, 0\n\t"
    "L35_%=:\n\t"
    "L34_%=:\n\t"
    "s_barrier\n\t"
    "buffer_load_dwordx4 v[16:19], v1, s[12:15], s54 offen sc1\n\t"
    "s_nop 11\n\t"
    "buffer_load_dwordx4 v[20:23], v1, s[12:15], s54 offen offset:1024 sc1\n\t"
    "s_nop 11\n\t"
    "buffer_load_dwordx4 v[32:35], v1, s[12:15], s54 offen offset:2048 sc1\n\t"
    "s_nop 11\n\t"
    "buffer_load_dwordx4 v[36:39], v1, s[12:15], s54 offen offset:3072 sc1\n\t"
    "s_nop 11\n\t"
    "buffer_load_dwordx4 v[40:43], v1, s[12:15], s58 offen sc1\n\t"
    "s_nop 11\n\t"
    "buffer_load_dwordx4 v[80:83], v1, s[12:15], s58 offen offset:1024 sc1\n\t"
    "s_waitcnt vmcnt(5)\n\t"
    "v_mfma_f32_16x16x32_bf16 v[124:127], a[0:3], v[16:19], 0\n\t"
    "v_mfma_f32_16x16x32_bf16 v[128:131], a[4:7], v[16:19], 0\n\t"
    "buffer_load_dwordx4 v[44:47], v1, s[12:15], s55 offen sc1\n\t"
    "v_mfma_f32_16x16x32_bf16 v[132:135], a[8:11], v[16:19], 0\n\t"
    "v_mfma_f32_16x16x32_bf16 v[136:139], a[12:15], v[16:19], 0\n\t"
    "v_mfma_f32_16x16x32_bf16 v[140:143], a[16:19], v[16:19], 0\n\t"
    "s_waitcnt vmcnt(5)\n\t"
    "v_mfma_f32_16x16x32_bf16 v[124:127], a[20:23], v[20:23], v[124:127]\n\t"
    "v_mfma_f32_16x16x32_bf16 v[128:131], a[24:27], v[20:23], v[128:131]\n\t"
    "buffer_load_dwordx4 v[48:51], v1, s[12:15], s55 offen offset:1024 sc1\n\t"
    "v_mfma_f32_16x16x32_bf16 v[132:135], a[28:31], v[20:23], v[132:135]\n\t"
    "v_mfma_f32_16x16x32_bf16 v[136:139], a[32:35], v[20:23], v[136:139]\n\t"
    "v_mfma_f32_16x16x32_bf16 v[140:143], a[36:39], v[20:23], v[140:143]\n\t"
    "s_waitcnt vmcnt(5)\n\t"
    "v_mfma_f32_16x16x32_bf16 v[124:127], a[40:43], v[32:35], v[124:127]\n\t"
    "v_mfma_f32_16x16x32_bf16 v[128:131], a[44:47], v[32:35], v[128:131]\n\t"
    "buffer_load_dwordx4 v[52:55], v1, s[12:15], s55 offen offset:2048 sc1\n\t"
    "v_mfma_f32_16x16x32_bf16 v[132:135], a[48:51], v[32:35], v[132:135]\n\t"
    "v_mfma_f32_16x16x32_bf16 v[136:139], a[52:55], v[32:35], v[136:139]\n\t"
    "v_mfma_f32_16x16x32_bf16 v[140:143], a[56:59], v[32:35], v[140:143]\n\t"
    "s_waitcnt vmcnt(5)\n\t"
    "v_mfma_f32_16x16x32_bf16 v[124:127], a[60:63], v[36:39], v[124:127]\n\t"
    "v_mfma_f32_16x16x32_bf16 v[128:131], a[64:67], v[36:39], v[128:131]\n\t"
    "buffer_load_dwordx4 v[56:59], v1, s[12:15], s55 offen offset:3072 sc1\n\t"
    "v_mfma_f32_16x16x32_bf16 v[132:135], a[68:71], v[36:39], v[132:135]\n\t"
    "v_mfma_f32_16x16x32_bf16 v[136:139], a[72:75], v[36:39], v[136:139]\n\t"
    "v_mfma_f32_16x16x32_bf16 v[140:143], a[76:79], v[36:39], v[140:143]\n\t"
    "s_waitcnt vmcnt(4)\n\t"
    "v_swap_b32 v42, v80\n\t"
    "v_swap_b32 v43, v81\n\t"
    "s_nop 1\n\t"
    "v_mfma_f32_16x16x32_bf16 v[124:127], a[80:83], v[40:43], v[124:127]\n\t"
    "v_mfma_f32_16x16x32_bf16 v[128:131], a[84:87], v[40:43], v[128:131]\n\t"
    "buffer_load_dwordx4 v[60:63], v1, s[12:15], s59 offen sc1\n\t"
    "v_mfma_f32_16x16x32_bf16 v[132:135], a[88:91], v[40:43], v[132:135]\n\t"
    "v_mfma_f32_16x16x32_bf16 v[136:139], a[92:95], v[40:43], v[136:139]\n\t"
    "v_mfma_f32_16x16x32_bf16 v[140:143], a[96:99], v[40:43], v[140:143]\n\t"
    "s_waitcnt vmcnt(4)\n\t"
    "v_mfma_f32_16x16x32_bf16 v[144:147], a[0:3], v[44:47], 0\n\t"
    "v_mfma_f32_16x16x32_bf16 v[148:151], a[4:7], v[44:47], 0\n\t"
    "buffer_load_dwordx4 v[100:103], v1, s[12:15], s59 offen offset:1024 sc1\n\t"
    "v_mfma_f32_16x16x32_bf16 v[152:155], a[8:11], v[44:47], 0\n\t"
    "ds_write_b128 v25, v[124:127]\n\t"
    "v_mfma_f32_16x16x32_bf16 v[156:159], a[12:15], v[44:47], 0\n\t"
    "v_mfma_f32_16x16x32_bf16 v[160:163], a[16:19], v[44:47], 0\n\t"
    "s_waitcnt vmcnt(4)\n\t"
    "v_mfma_f32_16x16x32_bf16 v[144:147], a[20:23], v[48:51], v[144:147]\n\t"
    "v_mfma_f32_16x16x32_bf16 v[148:151], a[24:27], v[48:51], v[148:151]\n\t"
    "buffer_load_dwordx4 v[64:67], v1, s[12:15], s56 offen sc1\n\t"
    "v_mfma_f32_16x16x32_bf16 v[152:155], a[28:31], v[48:51], v[152:155]\n\t"
    "ds_write_b128 v25, v[128:131] offset:1024\n\t"
    "v_mfma_f32_16x16x32_bf16 v[156:159], a[32:35], v[48:51], v[156:159]\n\t"
    "v_mfma_f32_16x16x32_bf16 v[160:163], a[36:39], v[48:51], v[160:163]\n\t"
    "s_waitcnt vmcnt(4)\n\t"
    "v_mfma_f32_16x16x32_bf16 v[144:147], a[40:43], v[52:55], v[144:147]\n\t"
    "v_mfma_f32_16x16x32_bf16 v[148:151], a[44:47], v[52:55], v[148:151]\n\t"
    "buffer_load_dwordx4 v[68:71], v1, s[12:15], s56 offen offset:1024 sc1\n\t"
    "v_mfma_f32_16x16x32_bf16 v[152:155], a[48:51], v[52:55], v[152:155]\n\t"
    "ds_write_b128 v25, v[132:135] offset:2048\n\t"
    "v_mfma_f32_16x16x32_bf16 v[156:159], a[52:55], v[52:55], v[156:159]\n\t"
    "v_mfma_f32_16x16x32_bf16 v[160:163], a[56:59], v[52:55], v[160:163]\n\t"
    "s_waitcnt vmcnt(4)\n\t"
    "v_mfma_f32_16x16x32_bf16 v[144:147], a[60:63], v[56:59], v[144:147]\n\t"
    "v_mfma_f32_16x16x32_bf16 v[148:151], a[64:67], v[56:59], v[148:151]\n\t"
    "buffer_load_dwordx4 v[72:75], v1, s[12:15], s56 offen offset:2048 sc1\n\t"
    "v_mfma_f32_16x16x32_bf16 v[152:155], a[68:71], v[56:59], v[152:155]\n\t"
    "ds_write_b128 v25, v[136:139] offset:3072\n\t"
    "v_mfma_f32_16x16x32_bf16 v[156:159], a[72:75], v[56:59], v[156:159]\n\t"
    "v_mfma_f32_16x16x32_bf16 v[160:163], a[76:79], v[56:59], v[160:163]\n\t"
    "s_waitcnt vmcnt(3)\n\t"
    "v_swap_b32 v62, v100\n\t"
    "v_swap_b32 v63, v101\n\t"
    "s_nop 1\n\t"
    "v_mfma_f32_16x16x32_bf16 v[144:147], a[80:83], v[60:63], v[144:147]\n\t"
    "v_mfma_f32_16x16x32_bf16 v[148:151], a[84:87], v[60:63], v[148:151]\n\t"
    "buffer_load_dwordx4 v[76:79], v1, s[12:15], s56 offen offset:3072 sc1\n\t"
    "v_mfma_f32_16x16x32_bf16 v[152:155], a[88:91], v[60:63], v[152:155]\n\t"
    "ds_write_b128 v25, v[140:143] offset:4096\n\t"
    "v_mfma_f32_16x16x32_bf16 v[156:159], a[92:95], v[60:63], v[156:159]\n\t"
    "v_mfma_f32_16x16x32_bf16 v[160:163], a[96:99], v[60:63], v[160:163]\n\t"
    "s_waitcnt vmcnt(3)\n\t"
    "v_mfma_f32_16x16x32_bf16 v[124:127], a[0:3], v[64:67], 0\n\t"
    "v_mfma_f32_16x16x32_bf16 v[128:131], a[4:7], v[64:67], 0\n\t"
    "buffer_load_dwordx4 v[84:87], v1, s[12:15], s57 offen sc1\n\t"
    "v_mfma_f32_16x16x32_bf16 v[132:135], a[8:11], v[64:67], 0\n\t"
    "ds_write_b128 v25, v[144:147] offset:5120\n\t"
    "v_mfma_f32_16x16x32_bf16 v[136:139], a[12:15], v[64:67], 0\n\t"
    "v_mfma_f32_16x16x32_bf16 v[140:143], a[16:19], v[64:67], 0\n\t"
    "s_waitcnt vmcnt(3)\n\t"
    "v_mfma_f32_16x16x32_bf16 v[124:127], a[20:23], v[68:71], v[124:127]\n\t"
    "v_mfma_f32_16x16x32_bf16 v[128:131], a[24:27], v[68:71], v[128:131]\n\t"
    "buffer_load_dwordx4 v[88:91], v1, s[12:15], s57 offen offset:1024 sc1\n\t"
    "v_mfma_f32_16x16x32_bf16 v[132:135], a[28:31], v[68:71], v[132:135]\n\t"
    "ds_write_b128 v25, v[148:151] offset:6144\n\t"
    "v_mfma_f32_16x16x32_bf16 v[136:139], a[32:35], v[68:71], v[136:139]\n\t"
    "v_mfma_f32_16x16x32_bf16 v[140:143], a[36:39], v[68:71], v[140:143]\n\t"
    "s_waitcnt vmcnt(3)\n\t"
    "v_mfma_f32_16x16x32_bf16 v[124:127], a[40:43], v[72:75], v[124:127]\n\t"
    "v_mfma_f32_16x16x32_bf16 v[128:131], a[44:47], v[72:75], v[128:131]\n\t"
    "buffer_load_dwordx4 v[92:95], v1, s[12:15], s57 offen offset:2048 sc1\n\t"
    "v_mfma_f32_16x16x32_bf16 v[132:135], a[48:51], v[72:75], v[132:135]\n\t"
    "ds_write_b128 v25, v[152:155] offset:7168\n\t"
    "v_mfma_f32_16x16x32_bf16 v[136:139], a[52:55], v[72:75], v[136:139]\n\t"
    "v_mfma_f32_16x16x32_bf16 v[140:143], a[56:59], v[72:75], v[140:143]\n\t"
    "s_waitcnt vmcnt(3)\n\t"
    "v_mfma_f32_16x16x32_bf16 v[124:127], a[60:63], v[76:79], v[124:127]\n\t"
    "v_mfma_f32_16x16x32_bf16 v[128:131], a[64:67], v[76:79], v[128:131]\n\t"
    "buffer_load_dwordx4 v[96:99], v1, s[12:15], s57 offen offset:3072 sc1\n\t"
    "v_mfma_f32_16x16x32_bf16 v[132:135], a[68:71], v[76:79], v[132:135]\n\t"
    "ds_write_b128 v25, v[156:159] offset:8192\n\t"
    "v_mfma_f32_16x16x32_bf16 v[136:139], a[72:75], v[76:79], v[136:139]\n\t"
    "v_mfma_f32_16x16x32_bf16 v[140:143], a[76:79], v[76:79], v[140:143]\n\t"
    "v_mfma_f32_16x16x32_bf16 v[124:127], a[80:83], v[80:83], v[124:127]\n\t"
    "v_mfma_f32_16x16x32_bf16 v[128:131], a[84:87], v[80:83], v[128:131]\n\t"
    "ds_write_b128 v25, v[160:163] offset:9216\n\t"
    "v_mfma_f32_16x16x32_bf16 v[132:135], a[88:91], v[80:83], v[132:135]\n\t"
    "v_mfma_f32_16x16x32_bf16 v[136:139], a[92:95], v[80:83], v[136:139]\n\t"
    "v_mfma_f32_16x16x32_bf16 v[140:143], a[96:99], v[80:83], v[140:143]\n\t"
    "s_waitcnt vmcnt(3)\n\t"
    "v_mfma_f32_16x16x32_bf16 v[104:107], a[0:3], v[84:87], v[104:107]\n\t"
    "v_mfma_f32_16x16x32_bf16 v[108:111], a[4:7], v[84:87], v[108:111]\n\t"
    "v_mfma_f32_16x16x32_bf16 v[112:115], a[8:11], v[84:87], v[112:115]\n\t"
    "ds_write_b128 v25, v[124:127] offset:10240\n\t"
    "v_mfma_f32_16x16x32_bf16 v[116:119], a[12:15], v[84:87], v[116:119]\n\t"
    "v_mfma_f32_16x16x32_bf16 v[120:123], a[16:19], v[84:87], v[120:123]\n\t"
    "ds_write_b128 v25, v[128:131] offset:11264\n\t"
    "s_waitcnt vmcnt(2)\n\t"
    "v_mfma_f32_16x16x32_bf16 v[104:107], a[20:23], v[88:91], v[104:107]\n\t"
    "v_mfma_f32_16x16x32_bf16 v[108:111], a[24:27], v[88:91], v[108:111]\n\t"
    "ds_write_b128 v25, v[132:135] offset:12288\n\t"
    "v_mfma_f32_16x16x32_bf16 v[112:115], a[28:31], v[88:91], v[112:115]\n\t"
    "v_mfma_f32_16x16x32_bf16 v[116:119], a[32:35], v[88:91], v[116:119]\n\t"
    "ds_write_b128 v25, v[136:139] offset:13312\n\t"
    "v_mfma_f32_16x16x32_bf16 v[120:123], a[36:39], v[88:91], v[120:123]\n\t"
    "s_waitcnt vmcnt(1)\n\t"
    "v_mfma_f32_16x16x32_bf16 v[104:107], a[40:43], v[92:95], v[104:107]\n\t"
    "ds_write_b128 v25, v[140:143] offset:14336\n\t"
    "v_mfma_f32_16x16x32_bf16 v[108:111], a[44:47], v[92:95], v[108:111]\n\t"
    "pad7_%=:\n\t"
    "s_cbranch_execz vb9_%=\n\t"
    "v_mfma_f32_16x16x32_bf16 v[112:115], a[48:51], v[92:95], v[112:115]\n\t"
    "v_mfma_f32_16x16x32_bf16 v[116:119], a[52:55], v[92:95], v[116:119]\n\t"
    "v_mfma_f32_16x16x32_bf16 v[120:123], a[56:59], v[92:95], v[120:123]\n\t"
    "s_waitcnt lgkmcnt(0)\n\t"
    "s_barrier\n\t"
    "s_waitcnt vmcnt(0)\n\t"
    "v_mfma_f32_16x16x32_bf16 v[104:107], a[60:63], v[96:99], v[104:107]\n\t"
    "ds_read_b128 v[16:19], v164\n\t"
    "ds_read_b128 v[20:23], v164 offset:1024\n\t"
    "v_mfma_f32_16x16x32_bf16 v[108:111], a[64:67], v[96:99], v[108:111]\n\t"
    "ds_read_b128 v[32:35], v164 offset:2048\n\t"
    "ds_read_b128 v[36:39], v164 offset:3072\n\t"
    "v_mfma_f32_16x16x32_bf16 v[112:115], a[68:71], v[96:99], v[112:115]\n\t"
    "ds_read_b128 v[40:43], v164 offset:4096\n\t"
    "ds_read_b128 v[44:47], v165\n\t"
    "v_mfma_f32_16x16x32_bf16 v[116:119], a[72:75], v[96:99], v[116:119]\n\t"
    "ds_read_b128 v[48:51], v165 offset:1024\n\t"
    "ds_read_b128 v[52:55], v165 offset:2048\n\t"
    "v_mfma_f32_16x16x32_bf16 v[120:123], a[76:79], v[96:99], v[120:123]\n\t"
    "ds_read_b128 v[56:59], v165 offset:3072\n\t"
    "v_mfma_f32_16x16x32_bf16 v[104:107], a[80:83], v[100:103], v[104:107]\n\t"
    "ds_read_b128 v[60:63], v165 offset:4096\n\t"
    "ds_read_b128 v[64:67], v166\n\t"
    "v_mfma_f32_16x16x32_bf16 v[108:111], a[84:87], v[100:103], v[108:111]\n\t"
    "ds_read_b128 v[68:71], v166 offset:1024\n\t"
    "ds_read_b128 v[72:75], v166 offset:2048\n\t"
    "v_mfma_f32_16x16x32_bf16 v[112:115], a[88:91], v[100:103], v[112:115]\n\t"
    "ds_read_b128 v[76:79], v166 offset:3072\n\t"
    "ds_read_b128 v[80:83], v166 offset:4096\n\t"
    "v_mfma_f32_16x16x32_bf16 v[116:119], a[92:95], v[100:103], v[116:119]\n\t"
    "v_mfma_f32_16x16x32_bf16 v[120:123], a[96:99], v[100:103], v[120:123]\n\t"
    "s_waitcnt lgkmcnt(0)\n\t"
    "v_pk_add_f32 v[16:17], v[16:17], v[44:45]\n\t"
    "v_pk_add_f32 v[18:19], v[18:19], v[46:47]\n\t"
    "v_pk_add_f32 v[20:21], v[20:21], v[48:49]\n\t"
    "v_pk_add_f32 v[22:23], v[22:23], v[50:51]\n\t"
    "v_pk_add_f32 v[32:33], v[32:33], v[52:53]\n\t"
    "v_pk_add_f32 v[34:35], v[34:35], v[54:55]\n\t"
    "v_pk_add_f32 v[36:37], v[36:37], v[56:57]\n\t"
    "v_pk_add_f32 v[38:39], v[38:39], v[58:59]\n\t"
    "v_pk_add_f32 v[40:41], v[40:41], v[60:61]\n\t"
    "v_pk_add_f32 v[42:43], v[42:43], v[62:63]\n\t"
    "v_pk_add_f32 v[16:17], v[16:17], v[64:65]\n\t"
    "v_pk_add_f32 v[18:19], v[18:19], v[66:67]\n\t"
    "v_pk_add_f32 v[20:21], v[20:21], v[68:69]\n\t"
    "v_pk_add_f32 v[22:23], v[22:23], v[70:71]\n\t"
    "v_pk_add_f32 v[32:33], v[32:33], v[72:73]\n\t"
    "v_pk_add_f32 v[34:35], v[34:35], v[74:75]\n\t"
    "v_pk_add_f32 v[36:37], v[36:37], v[76:77]\n\t"
    "v_pk_add_f32 v[38:39], v[38:39], v[78:79]\n\t"
    "v_pk_add_f32 v[40:41], v[40:41], v[80:81]\n\t"
    "v_pk_add_f32 v[42:43], v[42:43], v[82:83]\n\t"
    "v_pk_add_f32 v[104:105], v[104:105], v[16:17]\n\t"
    "v_pk_add_f32 v[106:107], v[106:107], v[18:19]\n\t"
    "v_pk_add_f32 v[108:109], v[108:109], v[20:21]\n\t"
    "v_pk_add_f32 v[110:111], v[110:111], v[22:23]\n\t"
    "v_pk_add_f32 v[112:113], v[112:113], v[32:33]\n\t"
    "v_pk_add_f32 v[114:115], v[114:115], v[34:35]\n\t"
    "v_pk_add_f32 v[116:117], v[116:117], v[36:37]\n\t"
    "v_pk_add_f32 v[118:119], v[118:119], v[38:39]\n\t"
    "v_pk_add_f32 v[120:121], v[120:121], v[40:41]\n\t"
    "v_pk_add_f32 v[122:123], v[122:123], v[42:43]\n\t"
    "v_cvt_pk_bf16_f32 v4, v104, v105\n\t"
    "v_cvt_pk_bf16_f32 v5, v106, v107\n\t"
    "v_cvt_pk_bf16_f32 v6, v108, v109\n\t"
    "v_cvt_pk_bf16_f32 v7, v110, v111\n\t"
    "v_cvt_pk_bf16_f32 v8, v112, v113\n\t"
    "v_cvt_pk_bf16_f32 v9, v114, v115\n\t"
    "v_cvt_pk_bf16_f32 v10, v116, v117\n\t"
    "v_cvt_pk_bf16_f32 v11, v118, v119\n\t"
    "v_cvt_pk_bf16_f32 v12, v120, v121\n\t"
    "v_cvt_pk_bf16_f32 v13, v122, v123\n\t"
    "buffer_store_dwordx4 v[4:7], v1, s[12:15], s62 offen sc1\n\t"
    "buffer_store_dwordx4 v[8:11], v1, s[12:15], s62 offen offset:1024 sc1\n\t"
    "buffer_store_dwordx2 v[12:13], v14, s[12:15], s63 offen sc1\n\t"
    "v_dot2_f32_bf16 v16, v4, s65, 0\n\t"
    "v_dot2_f32_bf16 v17, v4, v4, 0\n\t"
    "v_dot2_f32_bf16 v16, v5, s65, v16\n\t"
    "v_dot2_f32_bf16 v17, v5, v5, v17\n\t"
    "v_dot2_f32_bf16 v16, v6, s65, v16\n\t"
    "v_dot2_f32_bf16 v17, v6, v6, v17\n\t"
    "v_dot2_f32_bf16 v16, v7, s65, v16\n\t"
    "v_dot2_f32_bf16 v17, v7, v7, v17\n\t"
    "v_dot2_f32_bf16 v16, v8, s65, v16\n\t"
    "v_dot2_f32_bf16 v17, v8, v8, v17\n\t"
    "v_dot2_f32_bf16 v16, v9, s65, v16\n\t"
    "v_dot2_f32_bf16 v17, v9, v9, v17\n\t"
    "v_dot2_f32_bf16 v16, v10, s65, v16\n\t"
    "v_dot2_f32_bf16 v17, v10, v10, v17\n\t"
    "v_dot2_f32_bf16 v16, v11, s65, v16\n\t"
    "v_dot2_f32_bf16 v17, v11, v11, v17\n\t"
    "v_dot2_f32_bf16 v16, v12, s65, v16\n\t"
    "v_dot2_f32_bf16 v17, v12, v12, v17\n\t"
    "v_dot2_f32_bf16 v16, v13, s65, v16\n\t"
    "v_dot2_f32_bf16 v17, v13, v13, v17\n\t"
    "s_nop 2\n\t"
    "v_permlane32_swap_b32 v16, v17\n\t"
    "v_add_f32 v16, v16, v17\n\t"
    "v_mov_b32 v17, v16\n\t"
    "s_nop 1\n\t"
    "v_permlane16_swap_b32 v16, v17\n\t"
    "v_add_f32 v16, v16, v17\n\t"
    "buffer_store_dword v16, v24, s[12:15], s64 offen sc1\n\t"
    "s_mul_i32 s50, s82, 64\n\t"
    "s_mul_i32 s53, s34, 64\n\t"
    "s_add_u32 s50, s50, s53\n\t"
    "v_and_b32 v120, 3, v0\n\t"
    "v_lshlrev_b32 v120, 1, v120\n\t"
    "v_bfe_u32 v30, v0, 2, 2\n\t"
    "v_lshl_add_u32 v120, v30, 4, v120\n\t"
    "v_add_u32 v120, s50, v120\n\t"
    "buffer_load_ushort v92, v120, s[0:3], 0 offen offset:0\n\t"
    "s_mul_i32 s50, s33, 0x4c00\n\t"
    "v_add_u32 v25, s50, v1\n\t"
    "ds_read_b128 a[0:3], v25\n\t"
    "buffer_load_ushort v93, v120, s[0:3], 0 offen offset:8\n\t"
    "ds_read_b128 a[4:7], v25 offset:1024\n\t"
    "ds_read_b128 a[8:11], v25 offset:2048\n\t"
    "ds_read_b128 a[12:15], v25 offset:3072\n\t"
    "buffer_load_ushort v94, v120, s[0:3], 0 offen offset:512\n\t"
    "ds_read_b128 a[16:19], v25 offset:4096\n\t"
    "ds_read_b128 a[20:23], v25 offset:5120\n\t"
    "ds_read_b128 a[24:27], v25 offset:6144\n\t"
    "buffer_load_ushort v95, v120, s[0:3], 0 offen offset:520\n\t"
    "ds_read_b128 a[28:31], v25 offset:7168\n\t"
    "ds_read_b128 a[32:35], v25 offset:8192\n\t"
    "ds_read_b128 a[36:39], v25 offset:9216\n\t"
    "s_mul_i32 s66, s34, 0x43800\n\t"
    "s_mul_i32 s32, s82, 0x1400\n\t"
    "s_add_u32 s66, s66, s32\n\t"
    "s_add_u32 s66, s66, 0x408000\n\t"
    "buffer_load_dwordx4 a[76:79], v1, s[12:15], s66 offen sc1\n\t"
    "ds_read_b128 a[40:43], v25 offset:10240\n\t"
    "ds_read_b128 a[44:47], v25 offset:11264\n\t"
    "buffer_load_dwordx4 a[80:83], v1, s[12:15], s66 offen offset:1024 sc1\n\t"
    "ds_read_b128 a[48:51], v25 offset:12288\n\t"
    "ds_read_b128 a[52:55], v25 offset:13312\n\t"
    "ds_read_b128 a[56:59], v25 offset:14336\n\t"
    "buffer_load_dwordx4 a[84:87], v1, s[12:15], s66 offen offset:2048 sc1\n\t"
    "ds_read_b128 a[60:63], v25 offset:15360\n\t"
    "ds_read_b128 a[64:67], v25 offset:16384\n\t"
    "ds_read_b128 a[68:71], v25 offset:17408\n\t"
    "buffer_load_dwordx4 a[88:91], v1, s[12:15], s66 offen offset:3072 sc1\n\t"
    "ds_read_b128 a[72:75], v25 offset:18432\n\t"
    "s_mul_i32 s50, s84, 0x5000\n\t"
    "s_add_u32 s50, s50, 0x13000\n\t"
    "v_add_u32 v116, s50, v1\n\t"
    "s_mul_i32 s50, s85, 0x5000\n\t"
    "s_add_u32 s50, s50, 0x13000\n\t"
    "v_add_u32 v117, s50, v1\n\t"
    "s_mul_i32 s50, s86, 0x5000\n\t"
    "s_add_u32 s50, s50, 0x13000\n\t"
    "s_add_u32 s66, s66, 0x1000\n\t"
    "buffer_load_dwordx4 a[92:95], v1, s[12:15], s66 offen sc1\n\t"
    "v_add_u32 v118, s50, v1\n\t"
    "s_mul_i32 s50, s87, 0x5000\n\t"
    "s_add_u32 s50, s50, 0x13000\n\t"
    "v_add_u32 v119, s50, v1\n\t"
    "s_mul_i32 s54, s84, 0x5000\n\t"
    "s_add_u32 s54, s54, s47\n\t"
    "s_mul_i32 s55, s84, 0x5000\n\t"
    "s_add_u32 s55, s55, 0x13000\n\t"
    "s_mul_i32 s56, s84, 0x400\n\t"
    "s_add_u32 s56, s56, s47\n\t"
    "s_add_u32 s56, s56, 0x14000\n\t"
    "buffer_load_dwordx4 a[96:99], v1, s[12:15], s66 offen offset:1024 sc1\n\t"
    "s_mul_i32 m0, s84, 0x400\n\t"
    "s_add_u32 m0, m0, 0x27000\n\t"
    "s_mov_b32 s50, m0\n\t"
    "v_and_b32 v88, 15, v0\n\t"
    "v_lshlrev_b32 v88, 2, v88\n\t"
    "v_lshrrev_b32 v30, 4, v0\n\t"
    "v_lshl_add_u32 v88, v30, 8, v88\n\t"
    "v_add_u32 v88, s50, v88\n\t"
    "s_nop 3\n\t"
    "buffer_load_dwordx4 v[164:167], v1, s[12:15], s66 offen offset:2048 sc1\n\t"
    "s_nop 11\n\t"
    "buffer_load_dwordx4 v[160:163], v1, s[12:15], s66 offen offset:3072 sc1\n\t"
    "s_cmp_eq_u32 s33, 0\n\t"
    "s_waitcnt vmcnt(12)\n\t"
    "s_barrier\n\t"
    "s_cbranch_scc0 L37_%=\n\t"
    "s_mov_b64 exec, 1\n\t"
    "buffer_store_dwordx2 v[2:3], v1, s[12:15], s76 offen offset:128 sc1\n\t"
    "s_mov_b64 exec, -1\n\t"
    "L37_%=:\n\t"
    "s_mov_b32 exec_lo, 0x1010101\n\t"
    "s_mov_b32 exec_hi, 0x1010101\n\t"
    "buffer_load_dword v29, v24, s[12:15], s64 offen sc1\n\t"
    "buffer_load_dword v29, v1, s[12:15], s62 offen sc1\n\t"
    "buffer_load_dword v29, v1, s[12:15], s62 offen offset:1024 sc1\n\t"
    "buffer_load_dword v29, v14, s[12:15], s63 offen sc1\n\t"
    "s_mov_b64 exec, -1\n\t"
    "s_waitcnt lgkmcnt(0)\n\t"
    "v_mov_b32 v56, 0x3f803f80\n\t"
    "v_mov_b32 v57, 0x3f803f80\n\t"
    "v_mov_b32 v58, 0x3f803f80\n\t"
    "v_mov_b32 v59, 0x3f803f80\n\t"
    "s_nop 1\n\t"
    "v_mfma_f32_16x16x32_bf16 v[48:51], v[56:59], v[248:251], 0\n\t"
    "v_mfma_f32_16x16x32_bf16 v[52:55], v[56:59], v[244:247], 0\n\t"
    "v_mfma_f32_16x16x32_bf16 v[48:51], v[56:59], v[240:243], v[48:51]\n\t"
    "v_mfma_f32_16x16x32_bf16 v[52:55], v[56:59], v[236:239], v[52:55]\n\t"
    "v_mfma_f32_16x16x32_bf16 v[48:51], v[56:59], v[232:235], v[48:51]\n\t"
    "v_mfma_f32_16x16x32_bf16 v[52:55], v[56:59], v[228:231], v[52:55]\n\t"
    "v_mfma_f32_16x16x32_bf16 v[48:51], v[56:59], v[224:227], v[48:51]\n\t"
    "v_mfma_f32_16x16x32_bf16 v[52:55], v[56:59], v[220:223], v[52:55]\n\t"
    "v_mfma_f32_16x16x32_bf16 v[48:51], v[56:59], v[216:219], v[48:51]\n\t"
    "v_mfma_f32_16x16x32_bf16 v[52:55], v[56:59], v[212:215], v[52:55]\n\t"
    "v_mfma_f32_16x16x32_bf16 v[48:51], v[56:59], v[208:211], v[48:51]\n\t"
    "v_mfma_f32_16x16x32_bf16 v[52:55], v[56:59], v[204:207], v[52:55]\n\t"
    "v_mfma_f32_16x16x32_bf16 v[48:51], v[56:59], v[200:203], v[48:51]\n\t"
    "v_mfma_f32_16x16x32_bf16 v[52:55], v[56:59], v[196:199], v[52:55]\n\t"
    "v_mfma_f32_16x16x32_bf16 v[48:51], v[56:59], v[192:195], v[48:51]\n\t"
    "v_mfma_f32_16x16x32_bf16 v[52:55], v[56:59], v[188:191], v[52:55]\n\t"
    "s_cmp_eq_u32 s33, 0\n\t"
    "s_cbranch_scc0 L38_%=\n\t"
    "buffer_load_dwordx2 v[122:123], v15, s[12:15], s49 offen offset:128 sc1\n\t"
    "L38_%=:\n\t"
    "v_mfma_f32_16x16x32_bf16 v[48:51], v[56:59], v[184:187], v[48:51]\n\t"
    "v_mfma_f32_16x16x32_bf16 v[52:55], v[56:59], v[180:183], v[52:55]\n\t"
    "v_mfma_f32_16x16x32_bf16 v[48:51], v[56:59], v[176:179], v[48:51]\n\t"
    "v_mfma_f32_16x16x32_bf16 v[52:55], v[56:59], v[172:175], v[52:55]\n\t"
    "v_mfma_f32_16x16x32_bf16 v[48:51], v[56:59], v[168:171], v[48:51]\n\t"
    "v_mfma_f32_16x16x32_bf16 v[52:55], v[56:59], a[0:3], v[52:55]\n\t"
    "v_mfma_f32_16x16x32_bf16 v[48:51], v[56:59], a[4:7], v[48:51]\n\t"
    "v_mfma_f32_16x16x32_bf16 v[52:55], v[56:59], a[8:11], v[52:55]\n\t"
    "v_mfma_f32_16x16x32_bf16 v[48:51], v[56:59], a[12:15], v[48:51]\n\t"
    "v_mfma_f32_16x16x32_bf16 v[52:55], v[56:59], a[16:19], v[52:55]\n\t"
    "v_mfma_f32_16x16x32_bf16 v[48:51], v[56:59], a[20:23], v[48:51]\n\t"
    "v_mfma_f32_16x16x32_bf16 v[52:55], v[56:59], a[24:27], v[52:55]\n\t"
    "v_mfma_f32_16x16x32_bf16 v[48:51], v[56:59], a[28:31], v[48:51]\n\t"
    "v_mfma_f32_16x16x32_bf16 v[52:55], v[56:59], a[32:35], v[52:55]\n\t"
    "v_mfma_f32_16x16x32_bf16 v[48:51], v[56:59], a[36:39], v[48:51]\n\t"
    "v_mfma_f32_16x16x32_bf16 v[52:55], v[56:59], a[40:43], v[52:55]\n\t"
    "v_mfma_f32_16x16x32_bf16 v[48:51], v[56:59], a[44:47], v[48:51]\n\t"
    "v_mfma_f32_16x16x32_bf16 v[52:55], v[56:59], a[48:51], v[52:55]\n\t"
    "v_mfma_f32_16x16x32_bf16 v[48:51], v[56:59], a[52:55], v[48:51]\n\t"
    "v_mfma_f32_16x16x32_bf16 v[52:55], v[56:59], a[56:59], v[52:55]\n\t"
    "v_mfma_f32_16x16x32_bf16 v[48:51], v[56:59], a[60:63], v[48:51]\n\t"
    "v_mfma_f32_16x16x32_bf16 v[52:55], v[56:59], a[64:67], v[52:55]\n\t"
    "v_mfma_f32_16x16x32_bf16 v[48:51], v[56:59], a[68:71], v[48:51]\n\t"
    "v_mfma_f32_16x16x32_bf16 v[52:55], v[56:59], a[72:75], v[52:55]\n\t"
    "s_cmp_eq_u32 s33, 0\n\t"
    "s_cbranch_scc0 L39_%=\n\t"
    "s_waitcnt vmcnt(0)\n\t"
    "v_cmp_ne_u64 vcc, v[2:3], v[122:123]\n\t"
    "s_nop 0\n\t"
    "s_cbranch_vccz L40_%=\n\t"
    "poll41_%=:\n\t"
    "s_sleep 2\n\t"
    "buffer_load_dwordx2 v[122:123], v15, s[12:15], s49 offen offset:128 sc1\n\t"
    "s_waitcnt vmcnt(0)\n\t"
    "v_cmp_ne_u64 vcc, v[2:3], v[122:123]\n\t"
    "s_nop 0\n\t"
    "s_cbranch_vccz L40_%=\n\t"
    "s_sub_u32 s37, s37, 1\n\t"
    "s_cbranch_scc0 poll41_%=\n\t"
    "s_mov_b32 s37, 0\n\t"
    "L40_%=:\n\t"
    "L39_%=:\n\t"
    "s_barrier\n\t"
    "buffer_load_dwordx4 v1, s[12:15], s56 offen sc1 lds\n\t"
    "s_mov_b32 m0, s55\n\t"
    "v_cmp_gt_u32 vcc, 16, v0\n\t"
    "v_cmp_gt_u32 s[42:43], 32, v0\n\t"
    "s_waitcnt vmcnt(13)\n\t"
    "v_cndmask_b32 v40, 0, v26, vcc\n\t"
    "v_cndmask_b32 v92, 0, v92, s[42:43]\n\t"
    "v_and_b32 v41, 0xffff0000, v40\n\t"
    "v_sub_f32 v40, v40, v41\n\t"
    "buffer_load_dwordx4 v1, s[12:15], s54 offen sc1 lds\n\t"
    "v_cvt_pk_bf16_f32 v132, v41, v41\n\t"
    "v_cvt_pk_bf16_f32 v133, v40, 0\n\t"
    "v_lshl_or_b32 v133, v92, 16, v133\n\t"
    "v_cndmask_b32 v44, 0, v27, vcc\n\t"
    "v_cndmask_b32 v93, 0, v93, s[42:43]\n\t"
    "v_and_b32 v45, 0xffff0000, v44\n\t"
    "v_sub_f32 v44, v44, v45\n\t"
    "v_cvt_pk_bf16_f32 v134, v45, v45\n\t"
    "buffer_load_dwordx4 v1, s[12:15], s54 offen offset:1024 sc1 lds\n\t"
    "v_cvt_pk_bf16_f32 v135, v44, 0\n\t"
    "v_lshl_or_b32 v135, v93, 16, v135\n\t"
    "v_cndmask_b32 v48, 0, v48, vcc\n\t"
    "v_cndmask_b32 v94, 0, v94, s[42:43]\n\t"
    "v_and_b32 v49, 0xffff0000, v48\n\t"
    "v_sub_f32 v48, v48, v49\n\t"
    "v_cvt_pk_bf16_f32 v136, v49, v49\n\t"
    "v_cvt_pk_bf16_f32 v137, v48, 0\n\t"
    "buffer_load_dwordx4 v1, s[12:15], s54 offen offset:2048 sc1 lds\n\t"
    "v_lshl_or_b32 v137, v94, 16, v137\n\t"
    "v_cndmask_b32 v52, 0, v52, vcc\n\t"
    "v_cndmask_b32 v95, 0, v95, s[42:43]\n\t"
    "v_and_b32 v53, 0xffff0000, v52\n\t"
    "v_sub_f32 v52, v52, v53\n\t"
    "v_cvt_pk_bf16_f32 v138, v53, v53\n\t"
    "v_cvt_pk_bf16_f32 v139, v52, 0\n\t"
    "v_lshl_or_b32 v139, v95, 16, v139\n\t"
    "buffer_load_dwordx4 v1, s[12:15], s54 offen offset:3072 sc1 lds\n\t"
    "s_mul_i32 s51, s82, 0x400\n\t"
    "s_mul_i32 s53, s34, 0x400\n\t"
    "s_add_u32 s51, s51, s53\n\t"
    "s_add_u32 s51, s51, s48\n\t"
    "s_mul_i32 s58, s84, 0xd800\n\t"
    "s_add_u32 s58, s58, s51\n\t"
    "s_add_u32 s62, s58, 0x2000\n\t"
    "s_mul_i32 s59, s85, 0xd800\n\t"
    "s_add_u32 m0, m0, 0x1000\n\t"
    "s_add_u32 s54, s54, 0x1000\n\t"
    "buffer_load_dwordx4 v1, s[12:15], s54 offen sc1 lds\n\t"
    "s_add_u32 s59, s59, s51\n\t"
    "s_add_u32 s63, s59, 0x2000\n\t"
    "s_mul_i32 s60, s86, 0xd800\n\t"
    "s_add_u32 s60, s60, s51\n\t"
    "s_add_u32 s64, s60, 0x2000\n\t"
    "s_mul_i32 s61, s87, 0xd800\n\t"
    "buffer_load_dwordx4 v1, s[12:15], s54 offen offset:1024 sc1 lds\n\t"
    "s_add_u32 s61, s61, s51\n\t"
    "s_add_u32 s65, s61, 0x2000\n\t"
    "s_nop 5\n\t"
    "buffer_load_dwordx4 v1, s[12:15], s54 offen offset:2048 sc1 lds\n\t"
    "s_nop 7\n\t"
    "buffer_load_dwordx4 v1, s[12:15], s54 offen offset:3072 sc1 lds\n\t"
    "s_nop 7\n\t"
    "s_add_u32 m0, m0, 0x1000\n\t"
    "s_add_u32 s54, s54, 0x1000\n\t"
    "buffer_load_dwordx4 v1, s[12:15], s54 offen sc1 lds\n\t"
    "s_waitcnt vmcnt(8)\n\t"
    "buffer_load_dwordx4 v1, s[12:15], s54 offen offset:1024 sc1 lds\n\t"
    "ds_read_b128 v[100:103], v116\n\t"
    "s_waitcnt vmcnt(8)\n\t"
    "buffer_load_dwordx4 v1, s[12:15], s54 offen offset:2048 sc1 lds\n\t"
    "ds_read_b128 v[104:107], v116 offset:1024\n\t"
    "s_waitcnt lgkmcnt(1)\n\t"
    "v_mfma_f32_16x16x32_bf16 v[16:19], a[100:103], v[100:103], 0\n\t"
    "ds_read2_b32 v[124:125], v88 offset0:0 offset1:32\n\t"
    "ds_read2_b32 v[126:127], v88 offset0:16 offset1:48\n\t"
    "v_mfma_f32_16x16x32_bf16 v[20:23], a[104:107], v[100:103], 0\n\t"
    "buffer_load_dwordx4 v1, s[12:15], s54 offen offset:3072 sc1 lds\n\t"
    "v_mfma_f32_16x16x32_bf16 v[32:35], v[248:251], v[100:103], 0\n\t"
    "v_mfma_f32_16x16x32_bf16 v[36:39], v[244:247], v[100:103], 0\n\t"
    "s_waitcnt vmcnt(9)\n\t"
    "ds_read_b128 v[108:111], v116 offset:2048\n\t"
    "s_waitcnt lgkmcnt(3)\n\t"
    "v_mfma_f32_16x16x32_bf16 v[16:19], a[108:111], v[104:107], v[16:19]\n\t"
    "v_mfma_f32_16x16x32_bf16 v[20:23], a[112:115], v[104:107], v[20:23]\n\t"
    "s_add_u32 m0, m0, 0x1000\n\t"
    "s_add_u32 s54, s54, 0x1000\n\t"
    "buffer_load_dwordx4 v1, s[12:15], s54 offen sc1 lds\n\t"
    "v_mfma_f32_16x16x32_bf16 v[32:35], v[240:243], v[104:107], v[32:35]\n\t"
    "s_waitcnt lgkmcnt(1)\n\t"
    "v_add_f32 v124, v124, v125\n\t"
    "v_mfma_f32_16x16x32_bf16 v[36:39], v[236:239], v[104:107], v[36:39]\n\t"
    "s_waitcnt vmcnt(9)\n\t"
    "ds_read_b128 v[112:115], v116 offset:3072\n\t"
    "s_waitcnt lgkmcnt(1)\n\t"
    "v_mfma_f32_16x16x32_bf16 v[16:19], a[116:119], v[108:111], v[16:19]\n\t"
    "v_add_f32 v126, v126, v127\n\t"
    "v_mov_b32 v125, v124\n\t"
    "v_mfma_f32_16x16x32_bf16 v[20:23], a[120:123], v[108:111], v[20:23]\n\t"
    "buffer_load_dwordx4 v1, s[12:15], s54 offen offset:1024 sc1 lds\n\t"
    "v_mfma_f32_16x16x32_bf16 v[32:35], v[232:235], v[108:111], v[32:35]\n\t"
    "v_mov_b32 v127, v126\n\t"
    "v_permlane32_swap_b32 v124, v125\n\t"
    "v_mfma_f32_16x16x32_bf16 v[36:39], v[228:231], v[108:111], v[36:39]\n\t"
    "s_waitcnt vmcnt(9)\n\t"
    "ds_read_b128 v[100:103], v116 offset:4096\n\t"
    "s_waitcnt lgkmcnt(1)\n\t"
    "v_mfma_f32_16x16x32_bf16 v[16:19], a[124:127], v[112:115], v[16:19]\n\t"
    "v_permlane32_swap_b32 v126, v127\n\t"
    "v_add_f32 v124, v124, v125\n\t"
    "v_mfma_f32_16x16x32_bf16 v[20:23], a[128:131], v[112:115], v[20:23]\n\t"
    "buffer_load_dwordx4 v1, s[12:15], s54 offen offset:2048 sc1 lds\n\t"
    "v_mfma_f32_16x16x32_bf16 v[32:35], v[224:227], v[112:115], v[32:35]\n\t"
    "v_add_f32 v126, v126, v127\n\t"
    "v_mov_b32 v125, v124\n\t"
    "v_mfma_f32_16x16x32_bf16 v[36:39], v[220:223], v[112:115], v[36:39]\n\t"
    "s_waitcnt vmcnt(9)\n\t"
    "ds_read_b128 v[104:107], v116 offset:5120\n\t"
    "s_waitcnt lgkmcnt(1)\n\t"
    "v_mfma_f32_16x16x32_bf16 v[16:19], a[132:135], v[100:103], v[16:19]\n\t"
    "v_mov_b32 v127, v126\n\t"
    "v_permlane16_swap_b32 v124, v125\n\t"
    "v_mfma_f32_16x16x32_bf16 v[20:23], a[136:139], v[100:103], v[20:23]\n\t"
    "buffer_load_dwordx4 v1, s[12:15], s54 offen offset:3072 sc1 lds\n\t"
    "v_mfma_f32_16x16x32_bf16 v[32:35], v[216:219], v[100:103], v[32:35]\n\t"
    "v_permlane16_swap_b32 v126, v127\n\t"
    "v_add_f32 v124, v124, v125\n\t"
    "v_mfma_f32_16x16x32_bf16 v[36:39], v[212:215], v[100:103], v[36:39]\n\t"
    "s_waitcnt vmcnt(9)\n\t"
    "ds_read_b128 v[108:111], v116 offset:6144\n\t"
    "s_waitcnt lgkmcnt(1)\n\t"
    "v_mfma_f32_16x16x32_bf16 v[16:19], a[140:143], v[104:107], v[16:19]\n\t"
    "v_add_f32 v126, v126, v127\n\t"
    "v_mul_f32 v124, 0xbacccccd, v124\n\t"
    "v_mfma_f32_16x16x32_bf16 v[20:23], a[144:147], v[104:107], v[20:23]\n\t"
    "s_add_u32 m0, m0, 0x1000\n\t"
    "s_add_u32 s54, s54, 0x1000\n\t"
    "buffer_load_dwordx4 v1, s[12:15], s54 offen sc1 lds\n\t"
    "v_mfma_f32_16x16x32_bf16 v[32:35], v[208:211], v[104:107], v[32:35]\n\t"
    "v_mul_f32 v126, 0x3acccccd, v126\n\t"
    "v_fma_f32 v126, -v124, v124, v126\n\t"
    "v_mfma_f32_16x16x32_bf16 v[36:39], v[204:207], v[104:107], v[36:39]\n\t"
    "s_waitcnt vmcnt(9)\n\t"
    "ds_read_b128 v[112:115], v116 offset:7168\n\t"
    "s_waitcnt lgkmcnt(1)\n\t"
    "v_mfma_f32_16x16x32_bf16 v[16:19], a[148:151], v[108:111], v[16:19]\n\t"
    "v_max_f32 v126, 0, v126\n\t"
    "v_add_f32 v88, 0x3727c5ac, v126\n\t"
    "v_mfma_f32_16x16x32_bf16 v[20:23], a[152:155], v[108:111], v[20:23]\n\t"
    "buffer_load_dwordx4 v1, s[12:15], s54 offen offset:1024 sc1 lds\n\t"
    "v_mfma_f32_16x16x32_bf16 v[32:35], v[200:203], v[108:111], v[32:35]\n\t"
    "v_rsq_f32 v88, v88\n\t"
    "v_mfma_f32_16x16x32_bf16 v[36:39], v[196:199], v[108:111], v[36:39]\n\t"
    "s_waitcnt vmcnt(9)\n\t"
    "ds_read_b128 v[100:103], v116 offset:8192\n\t"
    "s_waitcnt lgkmcnt(1)\n\t"
    "v_mfma_f32_16x16x32_bf16 v[16:19], a[156:159], v[112:115], v[16:19]\n\t"
    "v_mul_f32 v126, v126, v88\n\t"
    "v_mul_f32 v126, v126, v88\n\t"
    "v_mfma_f32_16x16x32_bf16 v[20:23], a[160:163], v[112:115], v[20:23]\n\t"
    "buffer_load_dwordx4 v1, s[12:15], s54 offen offset:2048 sc1 lds\n\t"
    "v_mfma_f32_16x16x32_bf16 v[32:35], v[192:195], v[112:115], v[32:35]\n\t"
    "v_add_f32 v126, 0x3727c5ac, v126\n\t"
    "v_mfma_f32_16x16x32_bf16 v[36:39], v[188:191], v[112:115], v[36:39]\n\t"
    "s_waitcnt vmcnt(9)\n\t"
    "ds_read_b128 v[104:107], v116 offset:9216\n\t"
    "s_waitcnt lgkmcnt(1)\n\t"
    "v_mfma_f32_16x16x32_bf16 v[16:19], a[164:167], v[100:103], v[16:19]\n\t"
    "v_rsq_f32 v126, v126\n\t"
    "v_mfma_f32_16x16x32_bf16 v[20:23], a[168:171], v[100:103], v[20:23]\n\t"
    "buffer_load_dwordx4 v1, s[12:15], s54 offen offset:3072 sc1 lds\n\t"
    "v_mfma_f32_16x16x32_bf16 v[32:35], v[184:187], v[100:103], v[32:35]\n\t"
    "v_mul_f32 v126, v126, v88\n\t"
    "v_rcp_f32 v84, v126\n\t"
    "v_mfma_f32_16x16x32_bf16 v[36:39], v[180:183], v[100:103], v[36:39]\n\t"
    "s_waitcnt vmcnt(9)\n\t"
    "ds_read_b128 v[108:111], v116 offset:10240\n\t"
    "s_waitcnt lgkmcnt(1)\n\t"
    "v_mfma_f32_16x16x32_bf16 v[16:19], a[172:175], v[104:107], v[16:19]\n\t"
    "v_and_b32 v88, 0xffff0000, v124\n\t"
    "v_mul_f32 v74, 0xc0135761, v126\n\t"
    "v_mfma_f32_16x16x32_bf16 v[20:23], a[176:179], v[104:107], v[20:23]\n\t"
    "v_sub_f32 v89, v124, v88\n\t"
    "v_and_b32 v90, 0xffff0000, v84\n\t"
    "v_mfma_f32_16x16x32_bf16 v[32:35], v[176:179], v[104:107], v[32:35]\n\t"
    "v_mul_f32 v125, v126, v126\n\t"
    "v_sub_f32 v91, v84, v90\n\t"
    "v_mfma_f32_16x16x32_bf16 v[36:39], v[172:175], v[104:107], v[36:39]\n\t"
    "s_waitcnt vmcnt(8)\n\t"
    "ds_read_b128 v[112:115], v116 offset:11264\n\t"
    "s_waitcnt lgkmcnt(1)\n\t"
    "v_mfma_f32_16x16x32_bf16 v[16:19], a[180:183], v[108:111], v[16:19]\n\t"
    "v_cmp_gt_u32 vcc, 16, v0\n\t"
    "v_mul_f32 v125, v125, v126\n\t"
    "v_mfma_f32_16x16x32_bf16 v[20:23], a[184:187], v[108:111], v[20:23]\n\t"
    "v_cvt_pk_bf16_f32 v72, v88, v89\n\t"
    "v_cndmask_b32 v90, v91, v90, vcc\n\t"
    "v_mfma_f32_16x16x32_bf16 v[32:35], v[168:171], v[108:111], v[32:35]\n\t"
    "v_mul_f32 v75, 0xbdd2d3e8, v125\n\t"
    "v_cvt_pk_bf16_f32 v73, v88, v90\n\t"
    "v_mfma_f32_16x16x32_bf16 v[36:39], a[0:3], v[108:111], v[36:39]\n\t"
    "s_waitcnt vmcnt(7)\n\t"
    "ds_read_b128 v[100:103], v116 offset:12288\n\t"
    "s_waitcnt lgkmcnt(1)\n\t"
    "v_mfma_f32_16x16x32_bf16 v[16:19], a[188:191], v[112:115], v[16:19]\n\t"
    "s_mul_i32 s50, s87, 0x4c00\n\t"
    "s_add_u32 s50, s50, 0x4400\n\t"
    "v_add_u32 v120, s50, v1\n\t"
    "v_lshl_add_u32 v121, v0, 2, s50\n\t"
    "v_mfma_f32_16x16x32_bf16 v[20:23], a[192:195], v[112:115], v[20:23]\n\t"
    "ds_write_b128 v120, v[72:75]\n\t"
    "v_mfma_f32_16x16x32_bf16 v[32:35], a[4:7], v[112:115], v[32:35]\n\t"
    "ds_write_b32 v121, v84 offset:1024\n\t"
    "v_mfma_f32_16x16x32_bf16 v[36:39], a[8:11], v[112:115], v[36:39]\n\t"
    "s_waitcnt vmcnt(6)\n\t"
    "ds_read_b128 v[104:107], v116 offset:13312\n\t"
    "s_waitcnt lgkmcnt(3)\n\t"
    "v_mfma_f32_16x16x32_bf16 v[16:19], a[196:199], v[100:103], v[16:19]\n\t"
    "v_mfma_f32_16x16x32_bf16 v[20:23], a[200:203], v[100:103], v[20:23]\n\t"
    "s_mul_i32 s67, s34, 0x43800\n\t"
    "s_mul_i32 s32, s82, 0x1400\n\t"
    "s_add_u32 s67, s67, s32\n\t"
    "s_add_u32 s67, s67, 0x415400\n\t"
    "s_mul_i32 m0, s33, 0x4c00\n\t"
    "s_nop 0\n\t"
    "buffer_load_dwordx4 v1, s[12:15], s67 offen sc1 lds\n\t"
    "v_mfma_f32_16x16x32_bf16 v[32:35], a[12:15], v[100:103], v[32:35]\n\t"
    "v_mfma_f32_16x16x32_bf16 v[36:39], a[16:19], v[100:103], v[36:39]\n\t"
    "s_waitcnt vmcnt(6)\n\t"
    "ds_read_b128 v[108:111], v116 offset:14336\n\t"
    "s_waitcnt lgkmcnt(1)\n\t"
    "v_mfma_f32_16x16x32_bf16 v[16:19], a[204:207], v[104:107], v[16:19]\n\t"
    "v_mfma_f32_16x16x32_bf16 v[20:23], a[208:211], v[104:107], v[20:23]\n\t"
    "buffer_load_dwordx4 v1, s[12:15], s67 offen offset:1024 sc1 lds\n\t"
    "v_mfma_f32_16x16x32_bf16 v[32:35], a[20:23], v[104:107], v[32:35]\n\t"
    "v_mfma_f32_16x16x32_bf16 v[36:39], a[24:27], v[104:107], v[36:39]\n\t"
    "s_waitcnt vmcnt(6)\n\t"
    "ds_read_b128 v[112:115], v116 offset:15360\n\t"
    "s_waitcnt lgkmcnt(1)\n\t"
    "v_mfma_f32_16x16x32_bf16 v[16:19], a[212:215], v[108:111], v[16:19]\n\t"
    "v_mfma_f32_16x16x32_bf16 v[20:23], a[216:219], v[108:111], v[20:23]\n\t"
    "buffer_load_dwordx4 v1, s[12:15], s67 offen offset:2048 sc1 lds\n\t"
    "v_mfma_f32_16x16x32_bf16 v[32:35], a[28:31], v[108:111], v[32:35]\n\t"
    "v_mfma_f32_16x16x32_bf16 v[36:39], a[32:35], v[108:111], v[36:39]\n\t"
    "s_waitcnt vmcnt(6)\n\t"
    "ds_read_b128 v[100:103], v116 offset:16384\n\t"
    "s_waitcnt lgkmcnt(1)\n\t"
    "v_mfma_f32_16x16x32_bf16 v[16:19], a[220:223], v[112:115], v[16:19]\n\t"
    "v_mfma_f32_16x16x32_bf16 v[20:23], a[224:227], v[112:115], v[20:23]\n\t"
    "buffer_load_dwordx4 v1, s[12:15], s67 offen offset:3072 sc1 lds\n\t"
    "v_mfma_f32_16x16x32_bf16 v[32:35], a[36:39], v[112:115], v[32:35]\n\t"
    "v_mfma_f32_16x16x32_bf16 v[36:39], a[40:43], v[112:115], v[36:39]\n\t"
    "s_waitcnt vmcnt(6)\n\t"
    "ds_read_b128 v[104:107], v116 offset:17408\n\t"
    "s_waitcnt lgkmcnt(1)\n\t"
    "v_mfma_f32_16x16x32_bf16 v[16:19], a[228:231], v[100:103], v[16:19]\n\t"
    "v_mfma_f32_16x16x32_bf16 v[20:23], a[232:235], v[100:103], v[20:23]\n\t"
    "s_add_u32 m0, m0, 0x1000\n\t"
    "s_add_u32 s67, s67, 0x1000\n\t"
    "buffer_load_dwordx4 v1, s[12:15], s67 offen sc1 lds\n\t"
    "v_mfma_f32_16x16x32_bf16 v[32:35], a[44:47], v[100:103], v[32:35]\n\t"
    "v_mfma_f32_16x16x32_bf16 v[36:39], a[48:51], v[100:103], v[36:39]\n\t"
    "s_waitcnt vmcnt(6)\n\t"
    "ds_read_b128 v[108:111], v116 offset:18432\n\t"
    "s_waitcnt lgkmcnt(1)\n\t"
    "v_mfma_f32_16x16x32_bf16 v[16:19], a[236:239], v[104:107], v[16:19]\n\t"
    "v_mfma_f32_16x16x32_bf16 v[20:23], a[240:243], v[104:107], v[20:23]\n\t"
    "buffer_load_dwordx4 v1, s[12:15], s67 offen offset:1024 sc1 lds\n\t"
    "v_mfma_f32_16x16x32_bf16 v[32:35], a[52:55], v[104:107], v[32:35]\n\t"
    "v_mfma_f32_16x16x32_bf16 v[36:39], a[56:59], v[104:107], v[36:39]\n\t"
    "s_waitcnt vmcnt(6)\n\t"
    "ds_read_b128 v[112:115], v116 offset:19456\n\t"
    "s_waitcnt lgkmcnt(1)\n\t"
    "v_mfma_f32_16x16x32_bf16 v[16:19], a[244:247], v[108:111], v[16:19]\n\t"
    "v_mfma_f32_16x16x32_bf16 v[20:23], a[248:251], v[108:111], v[20:23]\n\t"
    "buffer_load_dwordx4 v1, s[12:15], s67 offen offset:2048 sc1 lds\n\t"
    "v_mfma_f32_16x16x32_bf16 v[32:35], a[60:63], v[108:111], v[32:35]\n\t"
    "v_mfma_f32_16x16x32_bf16 v[36:39], a[64:67], v[108:111], v[36:39]\n\t"
    "s_barrier\n\t"
    "ds_read_b128 v[100:103], v117\n\t"
    "ds_read_b128 v[104:107], v117 offset:1024\n\t"
    "ds_read_b128 v[108:111], v117 offset:2048\n\t"
    "s_waitcnt lgkmcnt(3)\n\t"
    "v_mfma_f32_16x16x32_bf16 v[16:19], a[252:255], v[112:115], v[16:19]\n\t"
    "v_mfma_f32_16x16x32_bf16 v[20:23], v[252:255], v[112:115], v[20:23]\n\t"
    "buffer_load_dwordx4 v1, s[12:15], s67 offen offset:3072 sc1 lds\n\t"
    "v_mfma_f32_16x16x32_bf16 v[32:35], a[68:71], v[112:115], v[32:35]\n\t"
    "v_mfma_f32_16x16x32_bf16 v[36:39], a[72:75], v[112:115], v[36:39]\n\t"
    "ds_read_b128 v[112:115], v117 offset:3072\n\t"
    "v_mfma_f32_16x16x16_bf16 v[16:19], v[132:133], v[72:73], v[16:19]\n\t"
    "v_mfma_f32_16x16x16_bf16 v[20:23], v[134:135], v[72:73], v[20:23]\n\t"
    "v_mfma_f32_16x16x16_bf16 v[32:35], v[136:137], v[72:73], v[32:35]\n\t"
    "v_mfma_f32_16x16x16_bf16 v[36:39], v[138:139], v[72:73], v[36:39]\n\t"
    "s_waitcnt lgkmcnt(2)\n\t"
    "v_mfma_f32_16x16x32_bf16 v[40:43], a[100:103], v[100:103], 0\n\t"
    "s_mul_i32 s50, s84, 0x4c00\n\t"
    "s_add_u32 s50, s50, 0x4400\n\t"
    "v_add_u32 v120, s50, v1\n\t"
    "v_lshl_add_u32 v121, v0, 2, s50\n\t"
    "v_mfma_f32_16x16x32_bf16 v[44:47], a[104:107], v[100:103], 0\n\t"
    "ds_read_b128 v[76:79], v120\n\t"
    "ds_read_b32 v85, v121 offset:1024\n\t"
    "v_mfma_f32_16x16x32_bf16 v[48:51], v[248:251], v[100:103], 0\n\t"
    "v_mul_f32 v88, v16, v16\n\t"
    "v_mul_f32 v89, v17, v17\n\t"
    "v_mfma_f32_16x16x32_bf16 v[52:55], v[244:247], v[100:103], 0\n\t"
    "ds_read_b128 v[100:103], v117 offset:4096\n\t"
    "v_mul_f32 v90, v18, v18\n\t"
    "v_mfma_f32_16x16x32_bf16 v[40:43], a[108:111], v[104:107], v[40:43]\n\t"
    "v_mul_f32 v91, v19, v19\n\t"
    "v_fma_f32 v88, v88, v75, v74\n\t"
    "v_mfma_f32_16x16x32_bf16 v[44:47], a[112:115], v[104:107], v[44:47]\n\t"
    "s_add_u32 m0, m0, 0x1000\n\t"
    "s_add_u32 s67, s67, 0x1000\n\t"
    "buffer_load_dwordx4 v1, s[12:15], s67 offen sc1 lds\n\t"
    "v_mfma_f32_16x16x32_bf16 v[48:51], v[240:243], v[104:107], v[48:51]\n\t"
    "v_fma_f32 v89, v89, v75, v74\n\t"
    "v_fma_f32 v90, v90, v75, v74\n\t"
    "v_mfma_f32_16x16x32_bf16 v[52:55], v[236:239], v[104:107], v[52:55]\n\t"
    "ds_read_b128 v[104:107], v117 offset:5120\n\t"
    "s_waitcnt lgkmcnt(4)\n\t"
    "v_mfma_f32_16x16x32_bf16 v[40:43], a[116:119], v[108:111], v[40:43]\n\t"
    "v_fma_f32 v91, v91, v75, v74\n\t"
    "v_mul_f32 v88, v88, v16\n\t"
    "v_mfma_f32_16x16x32_bf16 v[44:47], a[120:123], v[108:111], v[44:47]\n\t"
    "buffer_load_dwordx4 v1, s[12:15], s67 offen offset:1024 sc1 lds\n\t"
    "v_mfma_f32_16x16x32_bf16 v[48:51], v[232:235], v[108:111], v[48:51]\n\t"
    "v_mul_f32 v89, v89, v17\n\t"
    "v_mul_f32 v90, v90, v18\n\t"
    "v_mfma_f32_16x16x32_bf16 v[52:55], v[228:231], v[108:111], v[52:55]\n\t"
    "ds_read_b128 v[108:111], v117 offset:6144\n\t"
    "v_mul_f32 v91, v91, v19\n\t"
    "v_mfma_f32_16x16x32_bf16 v[40:43], a[124:127], v[112:115], v[40:43]\n\t"
    "v_exp_f32 v88, v88\n\t"
    "v_mfma_f32_16x16x32_bf16 v[44:47], a[128:131], v[112:115], v[44:47]\n\t"
    "buffer_load_dwordx4 v1, s[12:15], s67 offen offset:2048 sc1 lds\n\t"
    "pad8_%=:\n\t"
    "s_cbranch_execz vb9_%=\n\t"
    "v_mfma_f32_16x16x32_bf16 v[48:51], v[224:227], v[112:115], v[48:51]\n\t"
    "v_exp_f32 v89, v89\n\t"
    "v_mfma_f32_16x16x32_bf16 v[52:55], v[220:223], v[112:115], v[52:55]\n\t"
    "ds_read_b128 v[112:115], v117 offset:7168\n\t"
    "s_waitcnt lgkmcnt(2)\n\t"
    "v_mfma_f32_16x16x32_bf16 v[40:43], a[132:135], v[100:103], v[40:43]\n\t"
    "v_exp_f32 v90, v90\n\t"
    "v_mfma_f32_16x16x32_bf16 v[44:47], a[136:139], v[100:103], v[44:47]\n\t"
    "buffer_load_dwordx4 v1, s[12:15], s67 offen offset:3072 sc1 lds\n\t"
    "v_mfma_f32_16x16x32_bf16 v[48:51], v[216:219], v[100:103], v[48:51]\n\t"
    "v_exp_f32 v91, v91\n\t"
    "v_mfma_f32_16x16x32_bf16 v[52:55], v[212:215], v[100:103], v[52:55]\n\t"
    "ds_read_b128 v[100:103], v117 offset:8192\n\t"
    "v_fma_f32 v88, v88, v84, v84\n\t"
    "v_mfma_f32_16x16x32_bf16 v[40:43], a[140:143], v[104:107], v[40:43]\n\t"
    "v_fma_f32 v89, v89, v84, v84\n\t"
    "v_fma_f32 v90, v90, v84, v84\n\t"
    "v_mfma_f32_16x16x32_bf16 v[44:47], a[144:147], v[104:107], v[44:47]\n\t"
    "v_fma_f32 v91, v91, v84, v84\n\t"
    "v_mfma_f32_16x16x32_bf16 v[48:51], v[208:211], v[104:107], v[48:51]\n\t"
    "v_rcp_f32 v88, v88\n\t"
    "v_mfma_f32_16x16x32_bf16 v[52:55], v[204:207], v[104:107], v[52:55]\n\t"
    "ds_read_b128 v[104:107], v117 offset:9216\n\t"
    "s_waitcnt lgkmcnt(2)\n\t"
    "v_mfma_f32_16x16x32_bf16 v[40:43], a[148:151], v[108:111], v[40:43]\n\t"
    "v_rcp_f32 v89, v89\n\t"
    "v_mfma_f32_16x16x32_bf16 v[44:47], a[152:155], v[108:111], v[44:47]\n\t"
    "s_add_u32 m0, m0, 0x1000\n\t"
    "s_add_u32 s67, s67, 0x1000\n\t"
    "buffer_load_dwordx4 v1, s[20:23], s67 offen sc1 lds\n\t"
    "v_mfma_f32_16x16x32_bf16 v[48:51], v[200:203], v[108:111], v[48:51]\n\t"
    "v_rcp_f32 v90, v90\n\t"
    "v_mfma_f32_16x16x32_bf16 v[52:55], v[196:199], v[108:111], v[52:55]\n\t"
    "ds_read_b128 v[108:111], v117 offset:10240\n\t"
    "v_mfma_f32_16x16x32_bf16 v[40:43], a[156:159], v[112:115], v[40:43]\n\t"
    "v_rcp_f32 v91, v91\n\t"
    "v_mfma_f32_16x16x32_bf16 v[44:47], a[160:163], v[112:115], v[44:47]\n\t"
    "buffer_load_dwordx4 v1, s[20:23], s67 offen offset:1024 sc1 lds\n\t"
    "v_mfma_f32_16x16x32_bf16 v[48:51], v[192:195], v[112:115], v[48:51]\n\t"
    "v_mul_f32 v88, v88, v16\n\t"
    "v_mul_f32 v89, v89, v17\n\t"
    "v_mfma_f32_16x16x32_bf16 v[52:55], v[188:191], v[112:115], v[52:55]\n\t"
    "ds_read_b128 v[112:115], v117 offset:11264\n\t"
    "s_waitcnt lgkmcnt(2)\n\t"
    "v_mfma_f32_16x16x32_bf16 v[40:43], a[164:167], v[100:103], v[40:43]\n\t"
    "v_mul_f32 v90, v90, v18\n\t"
    "v_mul_f32 v91, v91, v19\n\t"
    "v_mfma_f32_16x16x32_bf16 v[44:47], a[168:171], v[100:103], v[44:47]\n\t"
    "buffer_load_dwordx4 v1, s[20:23], s67 offen offset:2048 sc1 lds\n\t"
    "v_mfma_f32_16x16x32_bf16 v[48:51], v[184:187], v[100:103], v[48:51]\n\t"
    "v_cvt_pk_bf16_f32 v92, v88, v89\n\t"
    "v_cvt_pk_bf16_f32 v93, v90, v91\n\t"
    "v_mfma_f32_16x16x32_bf16 v[52:55], v[180:183], v[100:103], v[52:55]\n\t"
    "ds_read_b128 v[100:103], v117 offset:12288\n\t"
    "v_mul_f32 v88, v20, v20\n\t"
    "v_mfma_f32_16x16x32_bf16 v[40:43], a[172:175], v[104:107], v[40:43]\n\t"
    "v_mul_f32 v89, v21, v21\n\t"
    "v_mul_f32 v90, v22, v22\n\t"
    "v_mfma_f32_16x16x32_bf16 v[44:47], a[176:179], v[104:107], v[44:47]\n\t"
    "buffer_load_dwordx4 v1, s[20:23], s67 offen offset:3072 sc1 lds\n\t"
    "v_mfma_f32_16x16x32_bf16 v[48:51], v[176:179], v[104:107], v[48:51]\n\t"
    "v_mul_f32 v91, v23, v23\n\t"
    "v_fma_f32 v88, v88, v75, v74\n\t"
    "v_mfma_f32_16x16x32_bf16 v[52:55], v[172:175], v[104:107], v[52:55]\n\t"
    "ds_read_b128 v[104:107], v117 offset:13312\n\t"
    "s_waitcnt lgkmcnt(2)\n\t"
    "v_mfma_f32_16x16x32_bf16 v[40:43], a[180:183], v[108:111], v[40:43]\n\t"
    "v_fma_f32 v89, v89, v75, v74\n\t"
    "v_fma_f32 v90, v90, v75, v74\n\t"
    "v_mfma_f32_16x16x32_bf16 v[44:47], a[184:187], v[108:111], v[44:47]\n\t"
    "v_fma_f32 v91, v91, v75, v74\n\t"
    "v_mul_f32 v88, v88, v20\n\t"
    "v_mfma_f32_16x16x32_bf16 v[48:51], v[168:171], v[108:111], v[48:51]\n\t"
    "v_mul_f32 v89, v89, v21\n\t"
    "v_mul_f32 v90, v90, v22\n\t"
    "v_mfma_f32_16x16x32_bf16 v[52:55], a[0:3], v[108:111], v[52:55]\n\t"
    "ds_read_b128 v[108:111], v117 offset:14336\n\t"
    "v_mul_f32 v91, v91, v23\n\t"
    "v_mfma_f32_16x16x32_bf16 v[40:43], a[188:191], v[112:115], v[40:43]\n\t"
    "v_exp_f32 v88, v88\n\t"
    "v_mfma_f32_16x16x32_bf16 v[44:47], a[192:195], v[112:115], v[44:47]\n\t"
    "s_add_u32 m0, m0, 0x1000\n\t"
    "s_add_u32 s67, s67, 0x1000\n\t"
    "buffer_load_dwordx4 v1, s[20:23], s67 offen sc1 lds\n\t"
    "v_mfma_f32_16x16x32_bf16 v[48:51], a[4:7], v[112:115], v[48:51]\n\t"
    "v_exp_f32 v89, v89\n\t"
    "v_mfma_f32_16x16x32_bf16 v[52:55], a[8:11], v[112:115], v[52:55]\n\t"
    "ds_read_b128 v[112:115], v117 offset:15360\n\t"
    "s_waitcnt lgkmcnt(2)\n\t"
    "v_mfma_f32_16x16x32_bf16 v[40:43], a[196:199], v[100:103], v[40:43]\n\t"
    "v_exp_f32 v90, v90\n\t"
    "v_mfma_f32_16x16x32_bf16 v[44:47], a[200:203], v[100:103], v[44:47]\n\t"
    "s_add_u32 s66, s66, 0x1000\n\t"
    "buffer_load_dwordx4 v[156:159], v1, s[12:15], s66 offen sc1\n\t"
    "v_mfma_f32_16x16x32_bf16 v[48:51], a[12:15], v[100:103], v[48:51]\n\t"
    "v_exp_f32 v91, v91\n\t"
    "v_mfma_f32_16x16x32_bf16 v[52:55], a[16:19], v[100:103], v[52:55]\n\t"
    "ds_read_b128 v[100:103], v117 offset:16384\n\t"
    "v_fma_f32 v88, v88, v84, v84\n\t"
    "v_mfma_f32_16x16x32_bf16 v[40:43], a[204:207], v[104:107], v[40:43]\n\t"
    "v_fma_f32 v89, v89, v84, v84\n\t"
    "v_fma_f32 v90, v90, v84, v84\n\t"
    "v_mfma_f32_16x16x32_bf16 v[44:47], a[208:211], v[104:107], v[44:47]\n\t"
    "buffer_load_dwordx4 v[152:155], v1, s[12:15], s66 offen offset:1024 sc1\n\t"
    "v_mfma_f32_16x16x32_bf16 v[48:51], a[20:23], v[104:107], v[48:51]\n\t"
    "v_fma_f32 v91, v91, v84, v84\n\t"
    "v_mfma_f32_16x16x32_bf16 v[52:55], a[24:27], v[104:107], v[52:55]\n\t"
    "ds_read_b128 v[104:107], v117 offset:17408\n\t"
    "s_waitcnt lgkmcnt(2)\n\t"
    "v_mfma_f32_16x16x32_bf16 v[40:43], a[212:215], v[108:111], v[40:43]\n\t"
    "v_rcp_f32 v88, v88\n\t"
    "v_mfma_f32_16x16x32_bf16 v[44:47], a[216:219], v[108:111], v[44:47]\n\t"
    "buffer_load_dwordx4 v[148:151], v1, s[12:15], s66 offen offset:2048 sc1\n\t"
    "v_mfma_f32_16x16x32_bf16 v[48:51], a[28:31], v[108:111], v[48:51]\n\t"
    "v_rcp_f32 v89, v89\n\t"
    "v_mfma_f32_16x16x32_bf16 v[52:55], a[32:35], v[108:111], v[52:55]\n\t"
    "ds_read_b128 v[108:111], v117 offset:18432\n\t"
    "v_mfma_f32_16x16x32_bf16 v[40:43], a[220:223], v[112:115], v[40:43]\n\t"
    "v_rcp_f32 v90, v90\n\t"
    "v_mfma_f32_16x16x32_bf16 v[44:47], a[224:227], v[112:115], v[44:47]\n\t"
    "v_rcp_f32 v91, v91\n\t"
    "v_mfma_f32_16x16x32_bf16 v[48:51], a[36:39], v[112:115], v[48:51]\n\t"
    "v_mul_f32 v88, v88, v20\n\t"
    "v_mul_f32 v89, v89, v21\n\t"
    "v_mfma_f32_16x16x32_bf16 v[52:55], a[40:43], v[112:115], v[52:55]\n\t"
    "ds_read_b128 v[112:115], v117 offset:19456\n\t"
    "s_waitcnt lgkmcnt(2)\n\t"
    "v_mfma_f32_16x16x32_bf16 v[40:43], a[228:231], v[100:103], v[40:43]\n\t"
    "v_mul_f32 v90, v90, v22\n\t"
    "v_mul_f32 v91, v91, v23\n\t"
    "v_mfma_f32_16x16x32_bf16 v[44:47], a[232:235], v[100:103], v[44:47]\n\t"
    "buffer_load_dwordx4 v[144:147], v1, s[12:15], s66 offen offset:3072 sc1\n\t"
    "v_mfma_f32_16x16x32_bf16 v[48:51], a[44:47], v[100:103], v[48:51]\n\t"
    "v_cvt_pk_bf16_f32 v94, v88, v89\n\t"
    "v_cvt_pk_bf16_f32 v95, v90, v91\n\t"
    "v_mfma_f32_16x16x32_bf16 v[52:55], a[48:51], v[100:103], v[52:55]\n\t"
    "ds_read_b128 v[100:103], v118\n\t"
    "buffer_store_dwordx4 v[92:95], v1, s[12:15], s58 offen sc1\n\t"
    "v_mfma_f32_16x16x32_bf16 v[40:43], a[236:239], v[104:107], v[40:43]\n\t"
    "v_mul_f32 v88, v32, v32\n\t"
    "v_mul_f32 v89, v33, v33\n\t"
    "v_mfma_f32_16x16x32_bf16 v[44:47], a[240:243], v[104:107], v[44:47]\n\t"
    "s_add_u32 s66, s66, 0x1000\n\t"
    "buffer_load_dwordx4 v[140:143], v1, s[12:15], s66 offen sc1\n\t"
    "v_mfma_f32_16x16x32_bf16 v[48:51], a[52:55], v[104:107], v[48:51]\n\t"
    "v_mul_f32 v90, v34, v34\n\t"
    "v_mul_f32 v91, v35, v35\n\t"
    "v_mfma_f32_16x16x32_bf16 v[52:55], a[56:59], v[104:107], v[52:55]\n\t"
    "ds_read_b128 v[104:107], v119\n\t"
    "s_waitcnt lgkmcnt(2)\n\t"
    "v_mfma_f32_16x16x32_bf16 v[40:43], a[244:247], v[108:111], v[40:43]\n\t"
    "v_fma_f32 v88, v88, v75, v74\n\t"
    "v_fma_f32 v89, v89, v75, v74\n\t"
    "v_mfma_f32_16x16x32_bf16 v[44:47], a[248:251], v[108:111], v[44:47]\n\t"
    "buffer_load_dwordx4 v[128:131], v1, s[12:15], s66 offen offset:1024 sc1\n\t"
    "v_mfma_f32_16x16x32_bf16 v[48:51], a[60:63], v[108:111], v[48:51]\n\t"
    "v_fma_f32 v90, v90, v75, v74\n\t"
    "v_fma_f32 v91, v91, v75, v74\n\t"
    "v_mfma_f32_16x16x32_bf16 v[52:55], a[64:67], v[108:111], v[52:55]\n\t"
    "ds_read_b128 v[108:111], v118 offset:1024\n\t"
    "v_mul_f32 v88, v88, v32\n\t"
    "v_mfma_f32_16x16x32_bf16 v[40:43], a[252:255], v[112:115], v[40:43]\n\t"
    "v_mul_f32 v89, v89, v33\n\t"
    "v_mul_f32 v90, v90, v34\n\t"
    "v_mfma_f32_16x16x32_bf16 v[44:47], v[252:255], v[112:115], v[44:47]\n\t"
    "buffer_load_dwordx4 v[24:27], v1, s[12:15], s66 offen offset:2048 sc1\n\t"
    "v_mfma_f32_16x16x32_bf16 v[48:51], a[68:71], v[112:115], v[48:51]\n\t"
    "v_mul_f32 v91, v91, v35\n\t"
    "v_mfma_f32_16x16x32_bf16 v[52:55], a[72:75], v[112:115], v[52:55]\n\t"
    "ds_read_b128 v[112:115], v119 offset:1024\n\t"
    "v_exp_f32 v88, v88\n\t"
    "v_exp_f32 v89, v89\n\t"
    "v_exp_f32 v90, v90\n\t"
    "v_exp_f32 v91, v91\n\t"
    "v_fma_f32 v88, v88, v84, v84\n\t"
    "v_fma_f32 v89, v89, v84, v84\n\t"
    "v_fma_f32 v90, v90, v84, v84\n\t"
    "v_fma_f32 v91, v91, v84, v84\n\t"
    "v_rcp_f32 v88, v88\n\t"
    "v_rcp_f32 v89, v89\n\t"
    "v_rcp_f32 v90, v90\n\t"
    "v_rcp_f32 v91, v91\n\t"
    "v_mul_f32 v88, v88, v32\n\t"
    "v_mul_f32 v89, v89, v33\n\t"
    "v_mul_f32 v90, v90, v34\n\t"
    "v_mul_f32 v91, v91, v35\n\t"
    "v_cvt_pk_bf16_f32 v96, v88, v89\n\t"
    "v_cvt_pk_bf16_f32 v97, v90, v91\n\t"
    "v_mul_f32 v88, v36, v36\n\t"
    "v_mul_f32 v89, v37, v37\n\t"
    "v_mul_f32 v90, v38, v38\n\t"
    "v_mul_f32 v91, v39, v39\n\t"
    "v_fma_f32 v88, v88, v75, v74\n\t"
    "v_fma_f32 v89, v89, v75, v74\n\t"
    "v_fma_f32 v90, v90, v75, v74\n\t"
    "v_fma_f32 v91, v91, v75, v74\n\t"
    "v_mul_f32 v88, v88, v36\n\t"
    "v_mul_f32 v89, v89, v37\n\t"
    "v_mul_f32 v90, v90, v38\n\t"
    "v_mul_f32 v91, v91, v39\n\t"
    "v_exp_f32 v88, v88\n\t"
    "v_exp_f32 v89, v89\n\t"
    "v_exp_f32 v90, v90\n\t"
    "v_exp_f32 v91, v91\n\t"
    "v_fma_f32 v88, v88, v84, v84\n\t"
    "v_fma_f32 v89, v89, v84, v84\n\t"
    "v_fma_f32 v90, v90, v84, v84\n\t"
    "v_fma_f32 v91, v91, v84, v84\n\t"
    "v_rcp_f32 v88, v88\n\t"
    "v_rcp_f32 v89, v89\n\t"
    "v_rcp_f32 v90, v90\n\t"
    "v_rcp_f32 v91, v91\n\t"
    "v_mul_f32 v88, v88, v36\n\t"
    "v_mul_f32 v89, v89, v37\n\t"
    "v_mul_f32 v90, v90, v38\n\t"
    "v_mul_f32 v91, v91, v39\n\t"
    "v_cvt_pk_bf16_f32 v98, v88, v89\n\t"
    "v_cvt_pk_bf16_f32 v99, v90, v91\n\t"
    "buffer_store_dwordx4 v[96:99], v1, s[16:19], s62 offen sc1\n\t"
    "v_mfma_f32_16x16x16_bf16 v[40:43], v[132:133], v[76:77], v[40:43]\n\t"
    "v_mfma_f32_16x16x16_bf16 v[44:47], v[134:135], v[76:77], v[44:47]\n\t"
    "v_mfma_f32_16x16x16_bf16 v[48:51], v[136:137], v[76:77], v[48:51]\n\t"
    "v_mfma_f32_16x16x16_bf16 v[52:55], v[138:139], v[76:77], v[52:55]\n\t"
    "s_waitcnt lgkmcnt(2)\n\t"
    "v_mfma_f32_16x16x32_bf16 v[16:19], a[100:103], v[100:103], 0\n\t"
    "s_mul_i32 s50, s85, 0x4c00\n\t"
    "s_add_u32 s50, s50, 0x4400\n\t"
    "v_add_u32 v120, s50, v1\n\t"
    "v_lshl_add_u32 v121, v0, 2, s50\n\t"
    "v_mfma_f32_16x16x32_bf16 v[56:59], a[100:103], v[104:107], 0\n\t"
    "ds_read_b128 v[72:75], v120\n\t"
    "ds_read_b32 v84, v121 offset:1024\n\t"
    "v_mfma_f32_16x16x32_bf16 v[20:23], a[104:107], v[100:103], 0\n\t"
    "s_mul_i32 s50, s86, 0x4c00\n\t"
    "s_add_u32 s50, s50, 0x4400\n\t"
    "v_add_u32 v120, s50, v1\n\t"
    "v_lshl_add_u32 v121, v0, 2, s50\n\t"
    "v_mfma_f32_16x16x32_bf16 v[60:63], a[104:107], v[104:107], 0\n\t"
    "buffer_load_dwordx4 a[100:103], v1, s[12:15], s66 offen offset:3072 sc1\n\t"
    "v_mfma_f32_16x16x32_bf16 v[32:35], v[248:251], v[100:103], 0\n\t"
    "ds_read_b128 v[80:83], v120\n\t"
    "ds_read_b32 v86, v121 offset:1024\n\t"
    "v_mfma_f32_16x16x32_bf16 v[64:67], v[248:251], v[104:107], 0\n\t"
    "v_mul_f32 v88, v40, v40\n\t"
    "v_mul_f32 v89, v41, v41\n\t"
    "v_mfma_f32_16x16x32_bf16 v[36:39], v[244:247], v[100:103], 0\n\t"
    "ds_read_b128 v[100:103], v118 offset:2048\n\t"
    "v_mul_f32 v90, v42, v42\n\t"
    "v_mfma_f32_16x16x32_bf16 v[68:71], v[244:247], v[104:107], 0\n\t"
    "ds_read_b128 v[104:107], v119 offset:2048\n\t"
    "s_add_u32 s66, s66, 0x1000\n\t"
    "buffer_load_dwordx4 a[104:107], v1, s[12:15], s66 offen sc1\n\t"
    "s_waitcnt lgkmcnt(6)\n\t"
    "v_mfma_f32_16x16x32_bf16 v[16:19], a[108:111], v[108:111], v[16:19]\n\t"
    "v_mul_f32 v91, v43, v43\n\t"
    "v_fma_f32 v88, v88, v79, v78\n\t"
    "v_mfma_f32_16x16x32_bf16 v[56:59], a[108:111], v[112:115], v[56:59]\n\t"
    "v_fma_f32 v89, v89, v79, v78\n\t"
    "v_fma_f32 v90, v90, v79, v78\n\t"
    "v_mfma_f32_16x16x32_bf16 v[20:23], a[112:115], v[108:111], v[20:23]\n\t"
    "v_fma_f32 v91, v91, v79, v78\n\t"
    "v_mul_f32 v88, v88, v40\n\t"
    "v_mfma_f32_16x16x32_bf16 v[60:63], a[112:115], v[112:115], v[60:63]\n\t"
    "buffer_load_dwordx4 a[108:111], v1, s[12:15], s66 offen offset:1024 sc1\n\t"
    "v_mfma_f32_16x16x32_bf16 v[32:35], v[240:243], v[108:111], v[32:35]\n\t"
    "v_mul_f32 v89, v89, v41\n\t"
    "v_mul_f32 v90, v90, v42\n\t"
    "v_mfma_f32_16x16x32_bf16 v[64:67], v[240:243], v[112:115], v[64:67]\n\t"
    "v_mul_f32 v91, v91, v43\n\t"
    "v_mfma_f32_16x16x32_bf16 v[36:39], v[236:239], v[108:111], v[36:39]\n\t"
    "ds_read_b128 v[108:111], v118 offset:3072\n\t"
    "v_mfma_f32_16x16x32_bf16 v[68:71], v[236:239], v[112:115], v[68:71]\n\t"
    "ds_read_b128 v[112:115], v119 offset:3072\n\t"
    "s_waitcnt lgkmcnt(2)\n\t"
    "v_mfma_f32_16x16x32_bf16 v[16:19], a[116:119], v[100:103], v[16:19]\n\t"
    "v_exp_f32 v88, v88\n\t"
    "v_mfma_f32_16x16x32_bf16 v[56:59], a[116:119], v[104:107], v[56:59]\n\t"
    "v_exp_f32 v89, v89\n\t"
    "v_mfma_f32_16x16x32_bf16 v[20:23], a[120:123], v[100:103], v[20:23]\n\t"
    "v_exp_f32 v90, v90\n\t"
    "v_mfma_f32_16x16x32_bf16 v[60:63], a[120:123], v[104:107], v[60:63]\n\t"
    "buffer_load_dwordx4 a[112:115], v1, s[12:15], s66 offen offset:2048 sc1\n\t"
    "v_mfma_f32_16x16x32_bf16 v[32:35], v[232:235], v[100:103], v[32:35]\n\t"
    "v_exp_f32 v91, v91\n\t"
    "v_mfma_f32_16x16x32_bf16 v[64:67], v[232:235], v[104:107], v[64:67]\n\t"
    "v_fma_f32 v88, v88, v85, v85\n\t"
    "v_fma_f32 v89, v89, v85, v85\n\t"
    "v_mfma_f32_16x16x32_bf16 v[36:39], v[228:231], v[100:103], v[36:39]\n\t"
    "ds_read_b128 v[100:103], v118 offset:4096\n\t"
    "v_fma_f32 v90, v90, v85, v85\n\t"
    "v_mfma_f32_16x16x32_bf16 v[68:71], v[228:231], v[104:107], v[68:71]\n\t"
    "ds_read_b128 v[104:107], v119 offset:4096\n\t"
    "buffer_load_dwordx4 a[116:119], v1, s[12:15], s66 offen offset:3072 sc1\n\t"
    "s_waitcnt lgkmcnt(2)\n\t"
    "v_mfma_f32_16x16x32_bf16 v[16:19], a[124:127], v[108:111], v[16:19]\n\t"
    "v_fma_f32 v91, v91, v85, v85\n\t"
    "v_mfma_f32_16x16x32_bf16 v[56:59], a[124:127], v[112:115], v[56:59]\n\t"
    "v_rcp_f32 v88, v88\n\t"
    "v_mfma_f32_16x16x32_bf16 v[20:23], a[128:131], v[108:111], v[20:23]\n\t"
    "v_rcp_f32 v89, v89\n\t"
    "v_mfma_f32_16x16x32_bf16 v[60:63], a[128:131], v[112:115], v[60:63]\n\t"
    "s_add_u32 s66, s66, 0x1000\n\t"
    "buffer_load_dwordx4 a[120:123], v1, s[12:15], s66 offen sc1\n\t"
    "v_mfma_f32_16x16x32_bf16 v[32:35], v[224:227], v[108:111], v[32:35]\n\t"
    "v_rcp_f32 v90, v90\n\t"
    "v_mfma_f32_16x16x32_bf16 v[64:67], v[224:227], v[112:115], v[64:67]\n\t"
    "v_rcp_f32 v91, v91\n\t"
    "v_mfma_f32_16x16x32_bf16 v[36:39], v[220:223], v[108:111], v[36:39]\n\t"
    "ds_read_b128 v[108:111], v118 offset:5120\n\t"
    "v_mul_f32 v88, v88, v40\n\t"
    "v_mfma_f32_16x16x32_bf16 v[68:71], v[220:223], v[112:115], v[68:71]\n\t"
    "ds_read_b128 v[112:115], v119 offset:5120\n\t"
    "s_waitcnt lgkmcnt(2)\n\t"
    "v_mfma_f32_16x16x32_bf16 v[16:19], a[132:135], v[100:103], v[16:19]\n\t"
    "v_mul_f32 v89, v89, v41\n\t"
    "v_mul_f32 v90, v90, v42\n\t"
    "v_mfma_f32_16x16x32_bf16 v[56:59], a[132:135], v[104:107], v[56:59]\n\t"
    "v_mul_f32 v91, v91, v43\n\t"
    "v_cvt_pk_bf16_f32 v92, v88, v89\n\t"
    "v_mfma_f32_16x16x32_bf16 v[20:23], a[136:139], v[100:103], v[20:23]\n\t"
    "v_cvt_pk_bf16_f32 v93, v90, v91\n\t"
    "v_mul_f32 v88, v44, v44\n\t"
    "v_mfma_f32_16x16x32_bf16 v[60:63], a[136:139], v[104:107], v[60:63]\n\t"
    "buffer_load_dwordx4 a[124:127], v1, s[12:15], s66 offen offset:1024 sc1\n\t"
    "v_mfma_f32_16x16x32_bf16 v[32:35], v[216:219], v[100:103], v[32:35]\n\t"
    "v_mul_f32 v89, v45, v45\n\t"
    "v_mul_f32 v90, v46, v46\n\t"
    "v_mfma_f32_16x16x32_bf16 v[64:67], v[216:219], v[104:107], v[64:67]\n\t"
    "v_mul_f32 v91, v47, v47\n\t"
    "v_fma_f32 v88, v88, v79, v78\n\t"
    "v_mfma_f32_16x16x32_bf16 v[36:39], v[212:215], v[100:103], v[36:39]\n\t"
    "ds_read_b128 v[100:103], v118 offset:6144\n\t"
    "v_fma_f32 v89, v89, v79, v78\n\t"
    "v_mfma_f32_16x16x32_bf16 v[68:71], v[212:215], v[104:107], v[68:71]\n\t"
    "ds_read_b128 v[104:107], v119 offset:6144\n\t"
    "buffer_load_dwordx4 a[128:131], v1, s[12:15], s66 offen offset:2048 sc1\n\t"
    "s_waitcnt lgkmcnt(2)\n\t"
    "v_mfma_f32_16x16x32_bf16 v[16:19], a[140:143], v[108:111], v[16:19]\n\t"
    "v_fma_f32 v90, v90, v79, v78\n\t"
    "v_fma_f32 v91, v91, v79, v78\n\t"
    "v_mfma_f32_16x16x32_bf16 v[56:59], a[140:143], v[112:115], v[56:59]\n\t"
    "v_mul_f32 v88, v88, v44\n\t"
    "v_mul_f32 v89, v89, v45\n\t"
    "v_mfma_f32_16x16x32_bf16 v[20:23], a[144:147], v[108:111], v[20:23]\n\t"
    "v_mul_f32 v90, v90, v46\n\t"
    "v_mul_f32 v91, v91, v47\n\t"
    "v_mfma_f32_16x16x32_bf16 v[60:63], a[144:147], v[112:115], v[60:63]\n\t"
    "buffer_load_dwordx4 a[132:135], v1, s[12:15], s66 offen offset:3072 sc1\n\t"
    "v_mfma_f32_16x16x32_bf16 v[32:35], v[208:211], v[108:111], v[32:35]\n\t"
    "v_exp_f32 v88, v88\n\t"
    "v_mfma_f32_16x16x32_bf16 v[64:67], v[208:211], v[112:115], v[64:67]\n\t"
    "v_exp_f32 v89, v89\n\t"
    "v_mfma_f32_16x16x32_bf16 v[36:39], v[204:207], v[108:111], v[36:39]\n\t"
    "ds_read_b128 v[108:111], v118 offset:7168\n\t"
    "v_mfma_f32_16x16x32_bf16 v[68:71], v[204:207], v[112:115], v[68:71]\n\t"
    "ds_read_b128 v[112:115], v119 offset:7168\n\t"
    "s_waitcnt lgkmcnt(2)\n\t"
    "v_mfma_f32_16x16x32_bf16 v[16:19], a[148:151], v[100:103], v[16:19]\n\t"
    "v_exp_f32 v90, v90\n\t"
    "v_mfma_f32_16x16x32_bf16 v[56:59], a[148:151], v[104:107], v[56:59]\n\t"
    "v_exp_f32 v91, v91\n\t"
    "v_mfma_f32_16x16x32_bf16 v[20:23], a[152:155], v[100:103], v[20:23]\n\t"
    "v_fma_f32 v88, v88, v85, v85\n\t"
    "v_fma_f32 v89, v89, v85, v85\n\t"
    "v_mfma_f32_16x16x32_bf16 v[60:63], a[152:155], v[104:107], v[60:63]\n\t"
    "s_add_u32 s66, s66, 0x1000\n\t"
    "buffer_load_dwordx4 a[136:139], v1, s[12:15], s66 offen sc1\n\t"
    "v_mfma_f32_16x16x32_bf16 v[32:35], v[200:203], v[100:103], v[32:35]\n\t"
    "v_fma_f32 v90, v90, v85, v85\n\t"
    "v_fma_f32 v91, v91, v85, v85\n\t"
    "v_mfma_f32_16x16x32_bf16 v[64:67], v[200:203], v[104:107], v[64:67]\n\t"
    "v_rcp_f32 v88, v88\n\t"
    "v_mfma_f32_16x16x32_bf16 v[36:39], v[196:199], v[100:103], v[36:39]\n\t"
    "ds_read_b128 v[100:103], v118 offset:8192\n\t"
    "v_mfma_f32_16x16x32_bf16 v[68:71], v[196:199], v[104:107], v[68:71]\n\t"
    "ds_read_b128 v[104:107], v119 offset:8192\n\t"
    "buffer_load_dwordx4 a[140:143], v1, s[12:15], s66 offen offset:1024 sc1\n\t"
    "s_waitcnt lgkmcnt(2)\n\t"
    "v_mfma_f32_16x16x32_bf16 v[16:19], a[156:159], v[108:111], v[16:19]\n\t"
    "v_rcp_f32 v89, v89\n\t"
    "v_mfma_f32_16x16x32_bf16 v[56:59], a[156:159], v[112:115], v[56:59]\n\t"
    "v_rcp_f32 v90, v90\n\t"
    "v_mfma_f32_16x16x32_bf16 v[20:23], a[160:163], v[108:111], v[20:23]\n\t"
    "v_rcp_f32 v91, v91\n\t"
    "v_mfma_f32_16x16x32_bf16 v[60:63], a[160:163], v[112:115], v[60:63]\n\t"
    "buffer_load_dwordx4 a[144:147], v1, s[12:15], s66 offen offset:2048 sc1\n\t"
    "v_mfma_f32_16x16x32_bf16 v[32:35], v[192:195], v[108:111], v[32:35]\n\t"
    "v_mul_f32 v88, v88, v44\n\t"
    "v_mul_f32 v89, v89, v45\n\t"
    "v_mfma_f32_16x16x32_bf16 v[64:67], v[192:195], v[112:115], v[64:67]\n\t"
    "v_mul_f32 v90, v90, v46\n\t"
    "v_mul_f32 v91, v91, v47\n\t"
    "v_mfma_f32_16x16x32_bf16 v[36:39], v[188:191], v[108:111], v[36:39]\n\t"
    "ds_read_b128 v[108:111], v118 offset:9216\n\t"
    "v_cvt_pk_bf16_f32 v94, v88, v89\n\t"
    "v_mfma_f32_16x16x32_bf16 v[68:71], v[188:191], v[112:115], v[68:71]\n\t"
    "ds_read_b128 v[112:115], v119 offset:9216\n\t"
    "s_waitcnt lgkmcnt(2)\n\t"
    "v_mfma_f32_16x16x32_bf16 v[16:19], a[164:167], v[100:103], v[16:19]\n\t"
    "v_cvt_pk_bf16_f32 v95, v90, v91\n\t"
    "buffer_store_dwordx4 v[92:95], v1, s[12:15], s59 offen sc1\n\t"
    "v_mfma_f32_16x16x32_bf16 v[56:59], a[164:167], v[104:107], v[56:59]\n\t"
    "v_mul_f32 v88, v48, v48\n\t"
    "v_mul_f32 v89, v49, v49\n\t"
    "v_mfma_f32_16x16x32_bf16 v[20:23], a[168:171], v[100:103], v[20:23]\n\t"
    "v_mul_f32 v90, v50, v50\n\t"
    "v_mul_f32 v91, v51, v51\n\t"
    "v_mfma_f32_16x16x32_bf16 v[60:63], a[168:171], v[104:107], v[60:63]\n\t"
    "buffer_load_dwordx4 a[148:151], v1, s[12:15], s66 offen offset:3072 sc1\n\t"
    "v_mfma_f32_16x16x32_bf16 v[32:35], v[184:187], v[100:103], v[32:35]\n\t"
    "v_fma_f32 v88, v88, v79, v78\n\t"
    "v_fma_f32 v89, v89, v79, v78\n\t"
    "v_mfma_f32_16x16x32_bf16 v[64:67], v[184:187], v[104:107], v[64:67]\n\t"
    "v_fma_f32 v90, v90, v79, v78\n\t"
    "v_fma_f32 v91, v91, v79, v78\n\t"
    "v_mfma_f32_16x16x32_bf16 v[36:39], v[180:183], v[100:103], v[36:39]\n\t"
    "ds_read_b128 v[100:103], v118 offset:10240\n\t"
    "v_mul_f32 v88, v88, v48\n\t"
    "v_mfma_f32_16x16x32_bf16 v[68:71], v[180:183], v[104:107], v[68:71]\n\t"
    "ds_read_b128 v[104:107], v119 offset:10240\n\t"
    "s_add_u32 s66, s66, 0x1000\n\t"
    "buffer_load_dwordx4 a[152:155], v1, s[12:15], s66 offen sc1\n\t"
    "s_waitcnt lgkmcnt(2)\n\t"
    "v_mfma_f32_16x16x32_bf16 v[16:19], a[172:175], v[108:111], v[16:19]\n\t"
    "v_mul_f32 v89, v89, v49\n\t"
    "v_mul_f32 v90, v90, v50\n\t"
    "v_mfma_f32_16x16x32_bf16 v[56:59], a[172:175], v[112:115], v[56:59]\n\t"
    "v_mul_f32 v91, v91, v51\n\t"
    "v_mfma_f32_16x16x32_bf16 v[20:23], a[176:179], v[108:111], v[20:23]\n\t"
    "v_exp_f32 v88, v88\n\t"
    "v_mfma_f32_16x16x32_bf16 v[60:63], a[176:179], v[112:115], v[60:63]\n\t"
    "buffer_load_dwordx4 a[156:159], v1, s[12:15], s66 offen offset:1024 sc1\n\t"
    "v_mfma_f32_16x16x32_bf16 v[32:35], v[176:179], v[108:111], v[32:35]\n\t"
    "v_exp_f32 v89, v89\n\t"
    "v_mfma_f32_16x16x32_bf16 v[64:67], v[176:179], v[112:115], v[64:67]\n\t"
    "v_exp_f32 v90, v90\n\t"
    "v_mfma_f32_16x16x32_bf16 v[36:39], v[172:175], v[108:111], v[36:39]\n\t"
    "ds_read_b128 v[108:111], v118 offset:11264\n\t"
    "v_mfma_f32_16x16x32_bf16 v[68:71], v[172:175], v[112:115], v[68:71]\n\t"
    "ds_read_b128 v[112:115], v119 offset:11264\n\t"
    "s_waitcnt lgkmcnt(2)\n\t"
    "v_mfma_f32_16x16x32_bf16 v[16:19], a[180:183], v[100:103], v[16:19]\n\t"
    "v_exp_f32 v91, v91\n\t"
    "v_mfma_f32_16x16x32_bf16 v[56:59], a[180:183], v[104:107], v[56:59]\n\t"
    "v_fma_f32 v88, v88, v85, v85\n\t"
    "v_fma_f32 v89, v89, v85, v85\n\t"
    "v_mfma_f32_16x16x32_bf16 v[20:23], a[184:187], v[100:103], v[20:23]\n\t"
    "v_fma_f32 v90, v90, v85, v85\n\t"
    "v_fma_f32 v91, v91, v85, v85\n\t"
    "v_mfma_f32_16x16x32_bf16 v[60:63], a[184:187], v[104:107], v[60:63]\n\t"
    "buffer_load_dwordx4 a[160:163], v1, s[12:15], s66 offen offset:2048 sc1\n\t"
    "v_mfma_f32_16x16x32_bf16 v[32:35], v[168:171], v[100:103], v[32:35]\n\t"
    "v_rcp_f32 v88, v88\n\t"
    "v_mfma_f32_16x16x32_bf16 v[64:67], v[168:171], v[104:107], v[64:67]\n\t"
    "v_rcp_f32 v89, v89\n\t"
    "v_mfma_f32_16x16x32_bf16 v[36:39], a[0:3], v[100:103], v[36:39]\n\t"
    "ds_read_b128 v[100:103], v118 offset:12288\n\t"
    "v_mfma_f32_16x16x32_bf16 v[68:71], a[0:3], v[104:107], v[68:71]\n\t"
    "ds_read_b128 v[104:107], v119 offset:12288\n\t"
    "buffer_load_dwordx4 a[0:3], v1, s[12:15], s66 offen offset:3072 sc1\n\t"
    "s_waitcnt lgkmcnt(2)\n\t"
    "v_mfma_f32_16x16x32_bf16 v[16:19], a[188:191], v[108:111], v[16:19]\n\t"
    "v_rcp_f32 v90, v90\n\t"
    "v_mfma_f32_16x16x32_bf16 v[56:59], a[188:191], v[112:115], v[56:59]\n\t"
    "v_rcp_f32 v91, v91\n\t"
    "v_mfma_f32_16x16x32_bf16 v[20:23], a[192:195], v[108:111], v[20:23]\n\t"
    "v_mul_f32 v88, v88, v48\n\t"
    "v_mul_f32 v89, v89, v49\n\t"
    "v_mfma_f32_16x16x32_bf16 v[60:63], a[192:195], v[112:115], v[60:63]\n\t"
    "s_add_u32 s66, s66, 0x1000\n\t"
    "buffer_load_dwordx4 a[164:167], v1, s[12:15], s66 offen sc1\n\t"
    "v_mfma_f32_16x16x32_bf16 v[32:35], a[4:7], v[108:111], v[32:35]\n\t"
    "v_mul_f32 v90, v90, v50\n\t"
    "v_mul_f32 v91, v91, v51\n\t"
    "v_mfma_f32_16x16x32_bf16 v[64:67], a[4:7], v[112:115], v[64:67]\n\t"
    "v_cvt_pk_bf16_f32 v96, v88, v89\n\t"
    "v_cvt_pk_bf16_f32 v97, v90, v91\n\t"
    "v_mfma_f32_16x16x32_bf16 v[36:39], a[8:11], v[108:111], v[36:39]\n\t"
    "ds_read_b128 v[108:111], v118 offset:13312\n\t"
    "v_mul_f32 v88, v52, v52\n\t"
    "v_mfma_f32_16x16x32_bf16 v[68:71], a[8:11], v[112:115], v[68:71]\n\t"
    "ds_read_b128 v[112:115], v119 offset:13312\n\t"
    "s_waitcnt lgkmcnt(2)\n\t"
    "v_mfma_f32_16x16x32_bf16 v[16:19], a[196:199], v[100:103], v[16:19]\n\t"
    "v_mul_f32 v89, v53, v53\n\t"
    "v_mul_f32 v90, v54, v54\n\t"
    "v_mfma_f32_16x16x32_bf16 v[56:59], a[196:199], v[104:107], v[56:59]\n\t"
    "v_mul_f32 v91, v55, v55\n\t"
    "v_fma_f32 v88, v88, v79, v78\n\t"
    "v_mfma_f32_16x16x32_bf16 v[20:23], a[200:203], v[100:103], v[20:23]\n\t"
    "v_fma_f32 v89, v89, v79, v78\n\t"
    "v_fma_f32 v90, v90, v79, v78\n\t"
    "v_mfma_f32_16x16x32_bf16 v[60:63], a[200:203], v[104:107], v[60:63]\n\t"
    "buffer_load_dwordx4 a[4:7], v1, s[12:15], s66 offen offset:1024 sc1\n\t"
    "v_mfma_f32_16x16x32_bf16 v[32:35], a[12:15], v[100:103], v[32:35]\n\t"
    "v_fma_f32 v91, v91, v79, v78\n\t"
    "v_mul_f32 v88, v88, v52\n\t"
    "v_mfma_f32_16x16x32_bf16 v[64:67], a[12:15], v[104:107], v[64:67]\n\t"
    "v_mul_f32 v89, v89, v53\n\t"
    "v_mul_f32 v90, v90, v54\n\t"
    "v_mfma_f32_16x16x32_bf16 v[36:39], a[16:19], v[100:103], v[36:39]\n\t"
    "ds_read_b128 v[100:103], v118 offset:14336\n\t"
    "v_mul_f32 v91, v91, v55\n\t"
    "v_mfma_f32_16x16x32_bf16 v[68:71], a[16:19], v[104:107], v[68:71]\n\t"
    "ds_read_b128 v[104:107], v119 offset:14336\n\t"
    "buffer_load_dwordx4 a[8:11], v1, s[12:15], s66 offen offset:2048 sc1\n\t"
    "s_waitcnt lgkmcnt(2)\n\t"
    "v_mfma_f32_16x16x32_bf16 v[16:19], a[204:207], v[108:111], v[16:19]\n\t"
    "v_exp_f32 v88, v88\n\t"
    "v_mfma_f32_16x16x32_bf16 v[56:59], a[204:207], v[112:115], v[56:59]\n\t"
    "v_exp_f32 v89, v89\n\t"
    "v_mfma_f32_16x16x32_bf16 v[20:23], a[208:211], v[108:111], v[20:23]\n\t"
    "v_exp_f32 v90, v90\n\t"
    "v_mfma_f32_16x16x32_bf16 v[60:63], a[208:211], v[112:115], v[60:63]\n\t"
    "buffer_load_dwordx4 a[12:15], v1, s[12:15], s66 offen offset:3072 sc1\n\t"
    "v_mfma_f32_16x16x32_bf16 v[32:35], a[20:23], v[108:111], v[32:35]\n\t"
    "v_exp_f32 v91, v91\n\t"
    "v_mfma_f32_16x16x32_bf16 v[64:67], a[20:23], v[112:115], v[64:67]\n\t"
    "v_fma_f32 v88, v88, v85, v85\n\t"
    "v_fma_f32 v89, v89, v85, v85\n\t"
    "v_mfma_f32_16x16x32_bf16 v[36:39], a[24:27], v[108:111], v[36:39]\n\t"
    "ds_read_b128 v[108:111], v118 offset:15360\n\t"
    "v_fma_f32 v90, v90, v85, v85\n\t"
    "v_mfma_f32_16x16x32_bf16 v[68:71], a[24:27], v[112:115], v[68:71]\n\t"
    "ds_read_b128 v[112:115], v119 offset:15360\n\t"
    "s_waitcnt lgkmcnt(2)\n\t"
    "v_mfma_f32_16x16x32_bf16 v[16:19], a[212:215], v[100:103], v[16:19]\n\t"
    "v_fma_f32 v91, v91, v85, v85\n\t"
    "v_mfma_f32_16x16x32_bf16 v[56:59], a[212:215], v[104:107], v[56:59]\n\t"
    "v_rcp_f32 v88, v88\n\t"
    "v_mfma_f32_16x16x32_bf16 v[20:23], a[216:219], v[100:103], v[20:23]\n\t"
    "v_rcp_f32 v89, v89\n\t"
    "v_mfma_f32_16x16x32_bf16 v[60:63], a[216:219], v[104:107], v[60:63]\n\t"
    "s_add_u32 s66, s66, 0x1000\n\t"
    "buffer_load_dwordx4 a[16:19], v1, s[12:15], s66 offen sc1\n\t"
    "v_mfma_f32_16x16x32_bf16 v[32:35], a[28:31], v[100:103], v[32:35]\n\t"
    "v_rcp_f32 v90, v90\n\t"
    "v_mfma_f32_16x16x32_bf16 v[64:67], a[28:31], v[104:107], v[64:67]\n\t"
    "v_rcp_f32 v91, v91\n\t"
    "v_mfma_f32_16x16x32_bf16 v[36:39], a[32:35], v[100:103], v[36:39]\n\t"
    "ds_read_b128 v[100:103], v118 offset:16384\n\t"
    "v_mul_f32 v88, v88, v52\n\t"
    "v_mfma_f32_16x16x32_bf16 v[68:71], a[32:35], v[104:107], v[68:71]\n\t"
    "ds_read_b128 v[104:107], v119 offset:16384\n\t"
    "buffer_load_dwordx4 a[20:23], v1, s[12:15], s66 offen offset:1024 sc1\n\t"
    "s_waitcnt lgkmcnt(2)\n\t"
    "v_mfma_f32_16x16x32_bf16 v[16:19], a[220:223], v[108:111], v[16:19]\n\t"
    "v_mul_f32 v89, v89, v53\n\t"
    "v_mul_f32 v90, v90, v54\n\t"
    "v_mfma_f32_16x16x32_bf16 v[56:59], a[220:223], v[112:115], v[56:59]\n\t"
    "v_mul_f32 v91, v91, v55\n\t"
    "v_cvt_pk_bf16_f32 v98, v88, v89\n\t"
    "v_mfma_f32_16x16x32_bf16 v[20:23], a[224:227], v[108:111], v[20:23]\n\t"
    "v_cvt_pk_bf16_f32 v99, v90, v91\n\t"
    "buffer_store_dwordx4 v[96:99], v1, s[16:19], s63 offen sc1\n\t"
    "v_mfma_f32_16x16x32_bf16 v[60:63], a[224:227], v[112:115], v[60:63]\n\t"
    "buffer_load_dwordx4 a[24:27], v1, s[12:15], s66 offen offset:2048 sc1\n\t"
    "v_mfma_f32_16x16x32_bf16 v[32:35], a[36:39], v[108:111], v[32:35]\n\t"
    "v_mfma_f32_16x16x32_bf16 v[64:67], a[36:39], v[112:115], v[64:67]\n\t"
    "v_mfma_f32_16x16x32_bf16 v[36:39], a[40:43], v[108:111], v[36:39]\n\t"
    "ds_read_b128 v[108:111], v118 offset:17408\n\t"
    "v_mfma_f32_16x16x32_bf16 v[68:71], a[40:43], v[112:115], v[68:71]\n\t"
    "ds_read_b128 v[112:115], v119 offset:17408\n\t"
    "s_waitcnt lgkmcnt(2)\n\t"
    "v_mfma_f32_16x16x32_bf16 v[16:19], a[228:231], v[100:103], v[16:19]\n\t"
    "v_mfma_f32_16x16x32_bf16 v[56:59], a[228:231], v[104:107], v[56:59]\n\t"
    "v_mfma_f32_16x16x32_bf16 v[20:23], a[232:235], v[100:103], v[20:23]\n\t"
    "v_mfma_f32_16x16x32_bf16 v[60:63], a[232:235], v[104:107], v[60:63]\n\t"
    "buffer_load_dwordx4 a[28:31], v1, s[12:15], s66 offen offset:3072 sc1\n\t"
    "v_mfma_f32_16x16x32_bf16 v[32:35], a[44:47], v[100:103], v[32:35]\n\t"
    "v_mfma_f32_16x16x32_bf16 v[64:67], a[44:47], v[104:107], v[64:67]\n\t"
    "v_mfma_f32_16x16x32_bf16 v[36:39], a[48:51], v[100:103], v[36:39]\n\t"
    "ds_read_b128 v[100:103], v118 offset:18432\n\t"
    "v_mfma_f32_16x16x32_bf16 v[68:71], a[48:51], v[104:107], v[68:71]\n\t"
    "ds_read_b128 v[104:107], v119 offset:18432\n\t"
    "s_add_u32 s66, s66, 0x1000\n\t"
    "buffer_load_dwordx4 a[32:35], v1, s[12:15], s66 offen sc1\n\t"
    "s_waitcnt lgkmcnt(2)\n\t"
    "v_mfma_f32_16x16x32_bf16 v[16:19], a[236:239], v[108:111], v[16:19]\n\t"
    "v_mfma_f32_16x16x32_bf16 v[56:59], a[236:239], v[112:115], v[56:59]\n\t"
    "v_mfma_f32_16x16x32_bf16 v[20:23], a[240:243], v[108:111], v[20:23]\n\t"
    "v_mfma_f32_16x16x32_bf16 v[60:63], a[240:243], v[112:115], v[60:63]\n\t"
    "buffer_load_dwordx4 a[36:39], v1, s[12:15], s66 offen offset:1024 sc1\n\t"
    "v_mfma_f32_16x16x32_bf16 v[32:35], a[52:55], v[108:111], v[32:35]\n\t"
    "v_mfma_f32_16x16x32_bf16 v[64:67], a[52:55], v[112:115], v[64:67]\n\t"
    "v_mfma_f32_16x16x32_bf16 v[36:39], a[56:59], v[108:111], v[36:39]\n\t"
    "ds_read_b128 v[108:111], v118 offset:19456\n\t"
    "v_mfma_f32_16x16x32_bf16 v[68:71], a[56:59], v[112:115], v[68:71]\n\t"
    "ds_read_b128 v[112:115], v119 offset:19456\n\t"
    "s_waitcnt lgkmcnt(2)\n\t"
    "v_mfma_f32_16x16x32_bf16 v[16:19], a[244:247], v[100:103], v[16:19]\n\t"
    "v_mfma_f32_16x16x32_bf16 v[56:59], a[244:247], v[104:107], v[56:59]\n\t"
    "v_mfma_f32_16x16x32_bf16 v[20:23], a[248:251], v[100:103], v[20:23]\n\t"
    "v_mfma_f32_16x16x32_bf16 v[60:63], a[248:251], v[104:107], v[60:63]\n\t"
    "buffer_load_dwordx4 a[40:43], v1, s[12:15], s66 offen offset:2048 sc1\n\t"
    "v_mfma_f32_16x16x32_bf16 v[32:35], a[60:63], v[100:103], v[32:35]\n\t"
    "v_mfma_f32_16x16x32_bf16 v[64:67], a[60:63], v[104:107], v[64:67]\n\t"
    "v_mfma_f32_16x16x32_bf16 v[36:39], a[64:67], v[100:103], v[36:39]\n\t"
    "v_mfma_f32_16x16x32_bf16 v[68:71], a[64:67], v[104:107], v[68:71]\n\t"
    "buffer_load_dwordx4 a[44:47], v1, s[12:15], s66 offen offset:3072 sc1\n\t"
    "s_waitcnt lgkmcnt(0)\n\t"
    "v_mfma_f32_16x16x32_bf16 v[16:19], a[252:255], v[108:111], v[16:19]\n\t"
    "pad9_%=:\n\t"
    "s_cbranch_execz vb9_%=\n\t"
    "v_mfma_f32_16x16x32_bf16 v[56:59], a[252:255], v[112:115], v[56:59]\n\t"
    "v_mfma_f32_16x16x32_bf16 v[20:23], v[252:255], v[108:111], v[20:23]\n\t"
    "v_mfma_f32_16x16x32_bf16 v[60:63], v[252:255], v[112:115], v[60:63]\n\t"
    "s_add_u32 s66, s66, 0x1000\n\t"
    "buffer_load_dwordx4 a[48:51], v1, s[12:15], s66 offen sc1\n\t"
    "v_mfma_f32_16x16x32_bf16 v[32:35], a[68:71], v[108:111], v[32:35]\n\t"
    "v_mfma_f32_16x16x32_bf16 v[64:67], a[68:71], v[112:115], v[64:67]\n\t"
    "v_mfma_f32_16x16x32_bf16 v[36:39], a[72:75], v[108:111], v[36:39]\n\t"
    "v_mfma_f32_16x16x32_bf16 v[68:71], a[72:75], v[112:115], v[68:71]\n\t"
    "s_cmp_eq_u32 s33, 0\n\t"
    "s_waitcnt vmcnt(7)\n\t"
    "s_barrier\n\t"
    "s_cbranch_scc0 L42_%=\n\t"
    "s_mov_b64 exec, 1\n\t"
    "buffer_store_dwordx2 v[2:3], v1, s[12:15], s76 offen offset:256 sc1\n\t"
    "s_mov_b64 exec, -1\n\t"
    "L42_%=:\n\t"
    "s_mov_b32 s54, s58\n\t"
    "s_mul_i32 s53, s34, 0xfffffc00\n\t"
    "s_add_u32 s54, s54, s53\n\t"
    "s_mov_b32 s55, s59\n\t"
    "s_mul_i32 s53, s34, 0xfffffc00\n\t"
    "s_add_u32 s55, s55, s53\n\t"
    "s_mov_b32 s56, s60\n\t"
    "s_mul_i32 s53, s34, 0xfffffc00\n\t"
    "s_add_u32 s56, s56, s53\n\t"
    "s_mov_b32 s57, s61\n\t"
    "s_mul_i32 s53, s34, 0xfffffc00\n\t"
    "s_add_u32 s57, s57, s53\n\t"
    "v_mfma_f32_16x16x16_bf16 v[16:19], v[132:133], v[72:73], v[16:19]\n\t"
    "v_mfma_f32_16x16x16_bf16 v[20:23], v[134:135], v[72:73], v[20:23]\n\t"
    "v_mfma_f32_16x16x16_bf16 v[32:35], v[136:137], v[72:73], v[32:35]\n\t"
    "v_mfma_f32_16x16x16_bf16 v[36:39], v[138:139], v[72:73], v[36:39]\n\t"
    "v_mfma_f32_16x16x16_bf16 v[56:59], v[132:133], v[80:81], v[56:59]\n\t"
    "v_mfma_f32_16x16x16_bf16 v[60:63], v[134:135], v[80:81], v[60:63]\n\t"
    "v_mfma_f32_16x16x16_bf16 v[64:67], v[136:137], v[80:81], v[64:67]\n\t"
    "v_mfma_f32_16x16x16_bf16 v[68:71], v[138:139], v[80:81], v[68:71]\n\t"
    "v_mov_b32 v108, v74\n\t"
    "buffer_load_dwordx4 a[196:199], v1, s[12:15], s66 offen offset:1024 sc1\n\t"
    "v_mov_b32 v109, v74\n\t"
    "v_mov_b32 v110, v75\n\t"
    "v_mov_b32 v111, v75\n\t"
    "v_mov_b32 v112, v84\n\t"
    "v_mov_b32 v113, v84\n\t"
    "v_pk_mul_f32 v[116:117], v[16:17], v[16:17]\n\t"
    "v_pk_mul_f32 v[118:119], v[18:19], v[18:19]\n\t"
    "v_pk_mul_f32 v[120:121], v[20:21], v[20:21]\n\t"
    "v_pk_mul_f32 v[122:123], v[22:23], v[22:23]\n\t"
    "v_pk_fma_f32 v[116:117], v[116:117], v[110:111], v[108:109]\n\t"
    "v_pk_fma_f32 v[118:119], v[118:119], v[110:111], v[108:109]\n\t"
    "v_pk_fma_f32 v[120:121], v[120:121], v[110:111], v[108:109]\n\t"
    "v_pk_fma_f32 v[122:123], v[122:123], v[110:111], v[108:109]\n\t"
    "v_pk_mul_f32 v[116:117], v[116:117], v[16:17]\n\t"
    "buffer_load_dwordx4 a[200:203], v1, s[12:15], s66 offen offset:2048 sc1\n\t"
    "v_pk_mul_f32 v[118:119], v[118:119], v[18:19]\n\t"
    "v_pk_mul_f32 v[120:121], v[120:121], v[20:21]\n\t"
    "v_pk_mul_f32 v[122:123], v[122:123], v[22:23]\n\t"
    "v_exp_f32 v116, v116\n\t"
    "v_exp_f32 v117, v117\n\t"
    "v_exp_f32 v118, v118\n\t"
    "v_exp_f32 v119, v119\n\t"
    "v_exp_f32 v120, v120\n\t"
    "v_exp_f32 v121, v121\n\t"
    "v_exp_f32 v122, v122\n\t"
    "v_exp_f32 v123, v123\n\t"
    "v_pk_fma_f32 v[116:117], v[116:117], v[112:113], v[112:113]\n\t"
    "v_pk_fma_f32 v[118:119], v[118:119], v[112:113], v[112:113]\n\t"
    "v_pk_fma_f32 v[120:121], v[120:121], v[112:113], v[112:113]\n\t"
    "buffer_load_dwordx4 a[204:207], v1, s[12:15], s66 offen offset:3072 sc1\n\t"
    "v_pk_fma_f32 v[122:123], v[122:123], v[112:113], v[112:113]\n\t"
    "v_rcp_f32 v116, v116\n\t"
    "v_rcp_f32 v117, v117\n\t"
    "v_rcp_f32 v118, v118\n\t"
    "v_rcp_f32 v119, v119\n\t"
    "v_rcp_f32 v120, v120\n\t"
    "v_rcp_f32 v121, v121\n\t"
    "v_rcp_f32 v122, v122\n\t"
    "v_rcp_f32 v123, v123\n\t"
    "v_pk_mul_f32 v[116:117], v[116:117], v[16:17]\n\t"
    "v_pk_mul_f32 v[118:119], v[118:119], v[18:19]\n\t"
    "v_pk_mul_f32 v[120:121], v[120:121], v[20:21]\n\t"
    "v_pk_mul_f32 v[122:123], v[122:123], v[22:23]\n\t"
    "v_cvt_pk_bf16_f32 v100, v116, v117\n\t"
    "s_add_u32 s66, s66, 0x1000\n\t"
    "buffer_load_dwordx4 a[208:211], v1, s[12:15], s66 offen sc1\n\t"
    "v_cvt_pk_bf16_f32 v101, v118, v119\n\t"
    "v_cvt_pk_bf16_f32 v102, v120, v121\n\t"
    "v_cvt_pk_bf16_f32 v103, v122, v123\n\t"
    "v_pk_mul_f32 v[116:117], v[32:33], v[32:33]\n\t"
    "v_pk_mul_f32 v[118:119], v[34:35], v[34:35]\n\t"
    "v_pk_mul_f32 v[120:121], v[36:37], v[36:37]\n\t"
    "v_pk_mul_f32 v[122:123], v[38:39], v[38:39]\n\t"
    "v_pk_fma_f32 v[116:117], v[116:117], v[110:111], v[108:109]\n\t"
    "v_pk_fma_f32 v[118:119], v[118:119], v[110:111], v[108:109]\n\t"
    "v_pk_fma_f32 v[120:121], v[120:121], v[110:111], v[108:109]\n\t"
    "v_pk_fma_f32 v[122:123], v[122:123], v[110:111], v[108:109]\n\t"
    "v_pk_mul_f32 v[116:117], v[116:117], v[32:33]\n\t"
    "v_pk_mul_f32 v[118:119], v[118:119], v[34:35]\n\t"
    "v_pk_mul_f32 v[120:121], v[120:121], v[36:37]\n\t"
    "buffer_load_dwordx4 a[212:215], v1, s[12:15], s66 offen offset:1024 sc1\n\t"
    "v_pk_mul_f32 v[122:123], v[122:123], v[38:39]\n\t"
    "v_exp_f32 v116, v116\n\t"
    "v_exp_f32 v117, v117\n\t"
    "v_exp_f32 v118, v118\n\t"
    "v_exp_f32 v119, v119\n\t"
    "v_exp_f32 v120, v120\n\t"
    "v_exp_f32 v121, v121\n\t"
    "v_exp_f32 v122, v122\n\t"
    "v_exp_f32 v123, v123\n\t"
    "v_pk_fma_f32 v[116:117], v[116:117], v[112:113], v[112:113]\n\t"
    "v_pk_fma_f32 v[118:119], v[118:119], v[112:113], v[112:113]\n\t"
    "v_pk_fma_f32 v[120:121], v[120:121], v[112:113], v[112:113]\n\t"
    "v_pk_fma_f32 v[122:123], v[122:123], v[112:113], v[112:113]\n\t"
    "v_rcp_f32 v116, v116\n\t"
    "buffer_load_dwordx4 a[216:219], v1, s[12:15], s66 offen offset:2048 sc1\n\t"
    "v_rcp_f32 v117, v117\n\t"
    "v_rcp_f32 v118, v118\n\t"
    "v_rcp_f32 v119, v119\n\t"
    "v_rcp_f32 v120, v120\n\t"
    "v_rcp_f32 v121, v121\n\t"
    "v_rcp_f32 v122, v122\n\t"
    "v_rcp_f32 v123, v123\n\t"
    "v_pk_mul_f32 v[116:117], v[116:117], v[32:33]\n\t"
    "v_pk_mul_f32 v[118:119], v[118:119], v[34:35]\n\t"
    "v_pk_mul_f32 v[120:121], v[120:121], v[36:37]\n\t"
    "v_pk_mul_f32 v[122:123], v[122:123], v[38:39]\n\t"
    "v_cvt_pk_bf16_f32 v104, v116, v117\n\t"
    "v_cvt_pk_bf16_f32 v105, v118, v119\n\t"
    "v_cvt_pk_bf16_f32 v106, v120, v121\n\t"
    "buffer_load_dwordx4 a[220:223], v1, s[12:15], s66 offen offset:3072 sc1\n\t"
    "v_cvt_pk_bf16_f32 v107, v122, v123\n\t"
    "s_cmp_eq_u32 s33, 0\n\t"
    "s_cbranch_scc0 L43_%=\n\t"
    "buffer_load_dwordx2 v[108:109], v15, s[12:15], s49 offen offset:256 sc1\n\t"
    "L43_%=:\n\t"
    "buffer_store_dwordx4 v[100:103], v1, s[12:15], s60 offen sc1\n\t"
    "buffer_store_dwordx4 v[104:107], v1, s[16:19], s64 offen sc1\n\t"
    "v_mov_b32 v132, v82\n\t"
    "s_add_u32 s66, s66, 0x1000\n\t"
    "buffer_load_dwordx4 a[224:227], v1, s[12:15], s66 offen sc1\n\t"
    "v_mov_b32 v133, v82\n\t"
    "v_mov_b32 v134, v83\n\t"
    "v_mov_b32 v135, v83\n\t"
    "v_mov_b32 v136, v86\n\t"
    "v_mov_b32 v137, v86\n\t"
    "v_pk_mul_f32 v[168:169], v[56:57], v[56:57]\n\t"
    "v_pk_mul_f32 v[170:171], v[58:59], v[58:59]\n\t"
    "v_pk_mul_f32 v[172:173], v[60:61], v[60:61]\n\t"
    "v_pk_mul_f32 v[174:175], v[62:63], v[62:63]\n\t"
    "v_pk_fma_f32 v[168:169], v[168:169], v[134:135], v[132:133]\n\t"
    "v_pk_fma_f32 v[170:171], v[170:171], v[134:135], v[132:133]\n\t"
    "v_pk_fma_f32 v[172:173], v[172:173], v[134:135], v[132:133]\n\t"
    "v_pk_fma_f32 v[174:175], v[174:175], v[134:135], v[132:133]\n\t"
    "v_pk_mul_f32 v[168:169], v[168:169], v[56:57]\n\t"
    "v_pk_mul_f32 v[170:171], v[170:171], v[58:59]\n\t"
    "v_pk_mul_f32 v[172:173], v[172:173], v[60:61]\n\t"
    "v_pk_mul_f32 v[174:175], v[174:175], v[62:63]\n\t"
    "v_exp_f32 v168, v168\n\t"
    "v_exp_f32 v169, v169\n\t"
    "v_exp_f32 v170, v170\n\t"
    "v_exp_f32 v171, v171\n\t"
    "v_exp_f32 v172, v172\n\t"
    "v_exp_f32 v173, v173\n\t"
    "v_exp_f32 v174, v174\n\t"
    "v_exp_f32 v175, v175\n\t"
    "v_pk_fma_f32 v[168:169], v[168:169], v[136:137], v[136:137]\n\t"
    "v_pk_fma_f32 v[170:171], v[170:171], v[136:137], v[136:137]\n\t"
    "v_pk_fma_f32 v[172:173], v[172:173], v[136:137], v[136:137]\n\t"
    "v_pk_fma_f32 v[174:175], v[174:175], v[136:137], v[136:137]\n\t"
    "v_rcp_f32 v168, v168\n\t"
    "v_rcp_f32 v169, v169\n\t"
    "v_rcp_f32 v170, v170\n\t"
    "v_rcp_f32 v171, v171\n\t"
    "v_rcp_f32 v172, v172\n\t"
    "v_rcp_f32 v173, v173\n\t"
    "v_rcp_f32 v174, v174\n\t"
    "v_rcp_f32 v175, v175\n\t"
    "v_pk_mul_f32 v[168:169], v[168:169], v[56:57]\n\t"
    "v_pk_mul_f32 v[170:171], v[170:171], v[58:59]\n\t"
    "v_pk_mul_f32 v[172:173], v[172:173], v[60:61]\n\t"
    "v_pk_mul_f32 v[174:175], v[174:175], v[62:63]\n\t"
    "v_cvt_pk_bf16_f32 v92, v168, v169\n\t"
    "v_cvt_pk_bf16_f32 v93, v170, v171\n\t"
    "v_cvt_pk_bf16_f32 v94, v172, v173\n\t"
    "v_cvt_pk_bf16_f32 v95, v174, v175\n\t"
    "v_pk_mul_f32 v[168:169], v[64:65], v[64:65]\n\t"
    "v_pk_mul_f32 v[170:171], v[66:67], v[66:67]\n\t"
    "buffer_store_dwordx4 v[92:95], v1, s[12:15], s61 offen sc1\n\t"
    "v_pk_mul_f32 v[172:173], v[68:69], v[68:69]\n\t"
    "v_pk_mul_f32 v[174:175], v[70:71], v[70:71]\n\t"
    "v_pk_fma_f32 v[168:169], v[168:169], v[134:135], v[132:133]\n\t"
    "v_pk_fma_f32 v[170:171], v[170:171], v[134:135], v[132:133]\n\t"
    "v_pk_fma_f32 v[172:173], v[172:173], v[134:135], v[132:133]\n\t"
    "v_pk_fma_f32 v[174:175], v[174:175], v[134:135], v[132:133]\n\t"
    "v_pk_mul_f32 v[168:169], v[168:169], v[64:65]\n\t"
    "v_pk_mul_f32 v[170:171], v[170:171], v[66:67]\n\t"
    "v_pk_mul_f32 v[172:173], v[172:173], v[68:69]\n\t"
    "v_pk_mul_f32 v[174:175], v[174:175], v[70:71]\n\t"
    "v_exp_f32 v168, v168\n\t"
    "v_exp_f32 v169, v169\n\t"
    "v_exp_f32 v170, v170\n\t"
    "v_exp_f32 v171, v171\n\t"
    "v_exp_f32 v172, v172\n\t"
    "v_exp_f32 v173, v173\n\t"
    "v_exp_f32 v174, v174\n\t"
    "v_exp_f32 v175, v175\n\t"
    "v_pk_fma_f32 v[168:169], v[168:169], v[136:137], v[136:137]\n\t"
    "v_pk_fma_f32 v[170:171], v[170:171], v[136:137], v[136:137]\n\t"
    "v_pk_fma_f32 v[172:173], v[172:173], v[136:137], v[136:137]\n\t"
    "v_pk_fma_f32 v[174:175], v[174:175], v[136:137], v[136:137]\n\t"
    "v_rcp_f32 v168, v168\n\t"
    "v_rcp_f32 v169, v169\n\t"
    "v_rcp_f32 v170, v170\n\t"
    "v_rcp_f32 v171, v171\n\t"
    "v_rcp_f32 v172, v172\n\t"
    "v_rcp_f32 v173, v173\n\t"
    "v_rcp_f32 v174, v174\n\t"
    "v_rcp_f32 v175, v175\n\t"
    "v_pk_mul_f32 v[168:169], v[168:169], v[64:65]\n\t"
    "v_pk_mul_f32 v[170:171], v[170:171], v[66:67]\n\t"
    "v_pk_mul_f32 v[172:173], v[172:173], v[68:69]\n\t"
    "v_pk_mul_f32 v[174:175], v[174:175], v[70:71]\n\t"
    "v_cvt_pk_bf16_f32 v96, v168, v169\n\t"
    "v_cvt_pk_bf16_f32 v97, v170, v171\n\t"
    "v_cvt_pk_bf16_f32 v98, v172, v173\n\t"
    "v_cvt_pk_bf16_f32 v99, v174, v175\n\t"
    "buffer_store_dwordx4 v[96:99], v1, s[16:19], s65 offen sc1\n\t"
    "s_cmp_eq_u32 s33, 0\n\t"
    "s_cbranch_scc0 L44_%=\n\t"
    "s_waitcnt vmcnt(5)\n\t"
    "v_cmp_ne_u64 vcc, v[2:3], v[108:109]\n\t"
    "s_nop 0\n\t"
    "s_cbranch_vccz L45_%=\n\t"
    "poll46_%=:\n\t"
    "s_sleep 2\n\t"
    "buffer_load_dwordx2 v[108:109], v15, s[12:15], s49 offen offset:256 sc1\n\t"
    "s_waitcnt vmcnt(0)\n\t"
    "v_cmp_ne_u64 vcc, v[2:3], v[108:109]\n\t"
    "s_nop 0\n\t"
    "s_cbranch_vccz L45_%=\n\t"
    "s_sub_u32 s37, s37, 1\n\t"
    "s_cbranch_scc0 poll46_%=\n\t"
    "s_mov_b32 s37, 0\n\t"
    "L45_%=:\n\t"
    "L44_%=:\n\t"
    "s_barrier\n\t"
    "buffer_load_dwordx4 a[52:55], v1, s[12:15], s54 offen sc1\n\t"
    "s_mul_i32 s50, s33, 0x4c00\n\t"
    "v_add_u32 v31, s50, v1\n\t"
    "ds_read_b128 a[228:231], v31\n\t"
    "ds_read_b128 a[232:235], v31 offset:1024\n\t"
    "ds_read_b128 a[236:239], v31 offset:2048\n\t"
    "buffer_load_dwordx4 a[56:59], v1, s[12:15], s54 offen offset:1024 sc1\n\t"
    "ds_read_b128 a[240:243], v31 offset:3072\n\t"
    "ds_read_b128 a[244:247], v31 offset:4096\n\t"
    "ds_read_b128 a[248:251], v31 offset:5120\n\t"
    "buffer_load_dwordx4 a[60:63], v1, s[12:15], s54 offen offset:2048 sc1\n\t"
    "ds_read_b128 a[252:255], v31 offset:6144\n\t"
    "ds_read_b128 v[252:255], v31 offset:7168\n\t"
    "ds_read_b128 v[248:251], v31 offset:8192\n\t"
    "buffer_load_dwordx4 a[64:67], v1, s[12:15], s54 offen offset:3072 sc1\n\t"
    "ds_read_b128 v[244:247], v31 offset:9216\n\t"
    "ds_read_b128 v[240:243], v31 offset:10240\n\t"
    "ds_read_b128 v[236:239], v31 offset:11264\n\t"
    "s_add_u32 s54, s54, 0x1000\n\t"
    "buffer_load_dwordx4 a[68:71], v1, s[12:15], s54 offen sc1\n\t"
    "ds_read_b128 v[232:235], v31 offset:12288\n\t"
    "ds_read_b128 v[228:231], v31 offset:13312\n\t"
    "ds_read_b128 v[224:227], v31 offset:14336\n\t"
    "buffer_load_dwordx4 a[72:75], v1, s[12:15], s54 offen offset:1024 sc1\n\t"
    "ds_read_b128 v[220:223], v31 offset:15360\n\t"
    "ds_read_b128 v[216:219], v31 offset:16384\n\t"
    "s_mul_i32 s50, s33, 0x4c00\n\t"
    "v_add_u32 v58, s50, v1\n\t"
    "s_mul_i32 s50, s86, 0x4c00\n\t"
    "v_add_u32 v100, s50, v1\n\t"
    "buffer_load_dwordx4 a[168:171], v1, s[12:15], s54 offen offset:2048 sc1\n\t"
    "s_mul_i32 s50, s85, 0x4c00\n\t"
    "s_add_u32 s50, s50, 0x1400\n\t"
    "v_add_u32 v101, s50, v1\n\t"
    "s_mul_i32 s50, s84, 0x4c00\n\t"
    "s_add_u32 s50, s50, 0x2800\n\t"
    "v_add_u32 v102, s50, v1\n\t"
    "v_lshrrev_b32 v104, 4, v0\n\t"
    "v_lshlrev_b32 v104, 4, v104\n\t"
    "v_lshrrev_b32 v105, 4, v0\n\t"
    "v_lshlrev_b32 v105, 3, v105\n\t"
    "buffer_load_dwordx4 v[32:35], v104, s[28:31], s70 offen\n\t"
    "buffer_load_dwordx4 a[172:175], v1, s[12:15], s54 offen offset:3072 sc1\n\t"
    "buffer_load_dwordx4 v[36:39], v104, s[28:31], s70 offen offset:64\n\t"
    "buffer_load_dwordx2 v[40:41], v105, s[28:31], s71 offen\n\t"
    "s_add_u32 s44, s44, 0x2a0000\n\t"
    "s_add_u32 s62, s44, s72\n\t"
    "s_add_u32 s63, s44, s73\n\t"
    "s_mul_i32 s64, s34, 0x200\n\t"
    "s_mul_i32 s53, s33, 0x80\n\t"
    "s_add_u32 s64, s64, s53\n\t"
    "s_add_u32 s64, s64, s44\n\t"
    "s_add_u32 s64, s64, 0x14000\n\t"
    "s_add_u32 s54, s54, 0x1000\n\t"
    "buffer_load_dwordx4 a[176:179], v1, s[12:15], s54 offen sc1\n\t"
    "s_mov_b32 s65, 0x3f803f80\n\t"
    "v_and_b32 v59, 15, v0\n\t"
    "v_lshlrev_b32 v59, 2, v59\n\t"
    "v_lshrrev_b32 v30, 5, v0\n\t"
    "v_lshl_add_u32 v59, v30, 6, v59\n\t"
    "s_nop 5\n\t"
    "buffer_load_dwordx4 a[180:183], v1, s[12:15], s54 offen offset:1024 sc1\n\t"
    "s_cmp_eq_u32 s33, 0\n\t"
    "s_waitcnt vmcnt(13)\n\t"
    "s_barrier\n\t"
    "s_cbranch_scc0 L47_%=\n\t"
    "s_mov_b64 exec, 1\n\t"
    "buffer_store_dwordx2 v[2:3], v1, s[12:15], s76 offen offset:384 sc1\n\t"
    "s_mov_b64 exec, -1\n\t"
    "L47_%=:\n\t"
    "s_waitcnt vmcnt(12)\n\t"
    "v_mfma_f32_16x16x32_bf16 v[60:63], a[76:79], a[52:55], 0\n\t"
    "v_mfma_f32_16x16x32_bf16 v[64:67], a[80:83], a[52:55], 0\n\t"
    "buffer_load_dwordx4 a[184:187], v1, s[12:15], s54 offen offset:2048 sc1\n\t"
    "v_mfma_f32_16x16x32_bf16 v[68:71], a[84:87], a[52:55], 0\n\t"
    "v_mfma_f32_16x16x32_bf16 v[72:75], a[88:91], a[52:55], 0\n\t"
    "v_mfma_f32_16x16x32_bf16 v[76:79], a[92:95], a[52:55], 0\n\t"
    "buffer_load_dwordx4 a[188:191], v1, s[12:15], s54 offen offset:3072 sc1\n\t"
    "s_waitcnt vmcnt(13)\n\t"
    "v_mfma_f32_16x16x32_bf16 v[60:63], a[96:99], a[56:59], v[60:63]\n\t"
    "v_mfma_f32_16x16x32_bf16 v[64:67], v[164:167], a[56:59], v[64:67]\n\t"
    "s_add_u32 s54, s54, 0x1000\n\t"
    "buffer_load_dwordx4 a[192:195], v1, s[12:15], s54 offen sc1\n\t"
    "v_mfma_f32_16x16x32_bf16 v[68:71], v[160:163], a[56:59], v[68:71]\n\t"
    "v_mfma_f32_16x16x32_bf16 v[72:75], v[156:159], a[56:59], v[72:75]\n\t"
    "v_mfma_f32_16x16x32_bf16 v[76:79], v[152:155], a[56:59], v[76:79]\n\t"
    "buffer_load_dwordx4 a[52:55], v1, s[20:23], s54 offen offset:1024 sc1\n\t"
    "s_waitcnt vmcnt(14)\n\t"
    "v_mfma_f32_16x16x32_bf16 v[60:63], v[148:151], a[60:63], v[60:63]\n\t"
    "v_mfma_f32_16x16x32_bf16 v[64:67], v[144:147], a[60:63], v[64:67]\n\t"
    "buffer_load_dwordx4 a[56:59], v1, s[12:15], s55 offen sc1\n\t"
    "v_mfma_f32_16x16x32_bf16 v[68:71], v[140:143], a[60:63], v[68:71]\n\t"
    "v_mfma_f32_16x16x32_bf16 v[72:75], v[128:131], a[60:63], v[72:75]\n\t"
    "v_mfma_f32_16x16x32_bf16 v[76:79], v[24:27], a[60:63], v[76:79]\n\t"
    "s_waitcnt vmcnt(14)\n\t"
    "v_mfma_f32_16x16x32_bf16 v[60:63], a[100:103], a[64:67], v[60:63]\n\t"
    "v_mfma_f32_16x16x32_bf16 v[64:67], a[104:107], a[64:67], v[64:67]\n\t"
    "buffer_load_dwordx4 a[60:63], v1, s[12:15], s55 offen offset:1024 sc1\n\t"
    "v_mfma_f32_16x16x32_bf16 v[68:71], a[108:111], a[64:67], v[68:71]\n\t"
    "v_mfma_f32_16x16x32_bf16 v[72:75], a[112:115], a[64:67], v[72:75]\n\t"
    "v_mfma_f32_16x16x32_bf16 v[76:79], a[116:119], a[64:67], v[76:79]\n\t"
    "s_waitcnt vmcnt(14)\n\t"
    "v_mfma_f32_16x16x32_bf16 v[60:63], a[120:123], a[68:71], v[60:63]\n\t"
    "v_mfma_f32_16x16x32_bf16 v[64:67], a[124:127], a[68:71], v[64:67]\n\t"
    "buffer_load_dwordx4 a[64:67], v1, s[12:15], s55 offen offset:2048 sc1\n\t"
    "v_mfma_f32_16x16x32_bf16 v[68:71], a[128:131], a[68:71], v[68:71]\n\t"
    "v_mfma_f32_16x16x32_bf16 v[72:75], a[132:135], a[68:71], v[72:75]\n\t"
    "v_mfma_f32_16x16x32_bf16 v[76:79], a[136:139], a[68:71], v[76:79]\n\t"
    "s_waitcnt vmcnt(14)\n\t"
    "v_mfma_f32_16x16x32_bf16 v[60:63], a[140:143], a[72:75], v[60:63]\n\t"
    "v_mfma_f32_16x16x32_bf16 v[64:67], a[144:147], a[72:75], v[64:67]\n\t"
    "buffer_load_dwordx4 a[68:71], v1, s[12:15], s55 offen offset:3072 sc1\n\t"
    "v_mfma_f32_16x16x32_bf16 v[68:71], a[148:151], a[72:75], v[68:71]\n\t"
    "v_mfma_f32_16x16x32_bf16 v[72:75], a[152:155], a[72:75], v[72:75]\n\t"
    "v_mfma_f32_16x16x32_bf16 v[76:79], a[156:159], a[72:75], v[76:79]\n\t"
    "s_waitcnt vmcnt(14)\n\t"
    "v_mfma_f32_16x16x32_bf16 v[60:63], a[160:163], a[168:171], v[60:63]\n\t"
    "v_mfma_f32_16x16x32_bf16 v[64:67], a[0:3], a[168:171], v[64:67]\n\t"
    "s_add_u32 s55, s55, 0x1000\n\t"
    "buffer_load_dwordx4 a[72:75], v1, s[12:15], s55 offen sc1\n\t"
    "v_mfma_f32_16x16x32_bf16 v[68:71], a[164:167], a[168:171], v[68:71]\n\t"
    "v_mfma_f32_16x16x32_bf16 v[72:75], a[4:7], a[168:171], v[72:75]\n\t"
    "v_mfma_f32_16x16x32_bf16 v[76:79], a[8:11], a[168:171], v[76:79]\n\t"
    "s_waitcnt vmcnt(13)\n\t"
    "v_mfma_f32_16x16x32_bf16 v[60:63], a[12:15], a[172:175], v[60:63]\n\t"
    "v_mfma_f32_16x16x32_bf16 v[64:67], a[16:19], a[172:175], v[64:67]\n\t"
    "buffer_load_dwordx4 a[168:171], v1, s[12:15], s55 offen offset:1024 sc1\n\t"
    "v_mfma_f32_16x16x32_bf16 v[68:71], a[20:23], a[172:175], v[68:71]\n\t"
    "v_mfma_f32_16x16x32_bf16 v[72:75], a[24:27], a[172:175], v[72:75]\n\t"
    "v_mfma_f32_16x16x32_bf16 v[76:79], a[28:31], a[172:175], v[76:79]\n\t"
    "s_waitcnt vmcnt(11)\n\t"
    "v_mfma_f32_16x16x32_bf16 v[60:63], a[32:35], a[176:179], v[60:63]\n\t"
    "v_mfma_f32_16x16x32_bf16 v[64:67], a[36:39], a[176:179], v[64:67]\n\t"
    "buffer_load_dwordx4 a[172:175], v1, s[12:15], s55 offen offset:2048 sc1\n\t"
    "v_mfma_f32_16x16x32_bf16 v[68:71], a[40:43], a[176:179], v[68:71]\n\t"
    "v_mfma_f32_16x16x32_bf16 v[72:75], a[44:47], a[176:179], v[72:75]\n\t"
    "v_mfma_f32_16x16x32_bf16 v[76:79], a[48:51], a[176:179], v[76:79]\n\t"
    "s_waitcnt vmcnt(11)\n\t"
    "v_mfma_f32_16x16x32_bf16 v[60:63], a[196:199], a[180:183], v[60:63]\n\t"
    "v_mfma_f32_16x16x32_bf16 v[64:67], a[200:203], a[180:183], v[64:67]\n\t"
    "buffer_load_dwordx4 a[176:179], v1, s[12:15], s55 offen offset:3072 sc1\n\t"
    "v_mfma_f32_16x16x32_bf16 v[68:71], a[204:207], a[180:183], v[68:71]\n\t"
    "v_mfma_f32_16x16x32_bf16 v[72:75], a[208:211], a[180:183], v[72:75]\n\t"
    "v_mfma_f32_16x16x32_bf16 v[76:79], a[212:215], a[180:183], v[76:79]\n\t"
    "s_waitcnt vmcnt(11)\n\t"
    "v_mfma_f32_16x16x32_bf16 v[60:63], a[216:219], a[184:187], v[60:63]\n\t"
    "v_mfma_f32_16x16x32_bf16 v[64:67], a[220:223], a[184:187], v[64:67]\n\t"
    "s_add_u32 s55, s55, 0x1000\n\t"
    "buffer_load_dwordx4 a[180:183], v1, s[12:15], s55 offen sc1\n\t"
    "v_mfma_f32_16x16x32_bf16 v[68:71], a[224:227], a[184:187], v[68:71]\n\t"
    "s_waitcnt lgkmcnt(15)\n\t"
    "v_mfma_f32_16x16x32_bf16 v[72:75], a[228:231], a[184:187], v[72:75]\n\t"
    "v_mfma_f32_16x16x32_bf16 v[76:79], a[232:235], a[184:187], v[76:79]\n\t"
    "buffer_load_dwordx2 v[104:105], v15, s[12:15], s49 offen offset:384 sc1\n\t"
    "s_waitcnt vmcnt(12) lgkmcnt(14)\n\t"
    "v_mfma_f32_16x16x32_bf16 v[60:63], a[236:239], a[188:191], v[60:63]\n\t"
    "s_waitcnt lgkmcnt(13)\n\t"
    "v_mfma_f32_16x16x32_bf16 v[64:67], a[240:243], a[188:191], v[64:67]\n\t"
    "buffer_load_dwordx4 a[184:187], v1, s[12:15], s55 offen offset:1024 sc1\n\t"
    "s_waitcnt lgkmcnt(12)\n\t"
    "v_mfma_f32_16x16x32_bf16 v[68:71], a[244:247], a[188:191], v[68:71]\n\t"
    "s_waitcnt lgkmcnt(11)\n\t"
    "v_mfma_f32_16x16x32_bf16 v[72:75], a[248:251], a[188:191], v[72:75]\n\t"
    "s_waitcnt lgkmcnt(10)\n\t"
    "v_mfma_f32_16x16x32_bf16 v[76:79], a[252:255], a[188:191], v[76:79]\n\t"
    "s_waitcnt vmcnt(12) lgkmcnt(9)\n\t"
    "v_mfma_f32_16x16x32_bf16 v[60:63], v[252:255], a[192:195], v[60:63]\n\t"
    "s_waitcnt lgkmcnt(8)\n\t"
    "v_mfma_f32_16x16x32_bf16 v[64:67], v[248:251], a[192:195], v[64:67]\n\t"
    "buffer_load_dwordx4 a[188:191], v1, s[12:15], s55 offen offset:2048 sc1\n\t"
    "s_waitcnt lgkmcnt(7)\n\t"
    "v_mfma_f32_16x16x32_bf16 v[68:71], v[244:247], a[192:195], v[68:71]\n\t"
    "s_waitcnt lgkmcnt(6)\n\t"
    "v_mfma_f32_16x16x32_bf16 v[72:75], v[240:243], a[192:195], v[72:75]\n\t"
    "s_waitcnt lgkmcnt(5)\n\t"
    "v_mfma_f32_16x16x32_bf16 v[76:79], v[236:239], a[192:195], v[76:79]\n\t"
    "s_waitcnt vmcnt(12) lgkmcnt(4)\n\t"
    "v_mfma_f32_16x16x32_bf16 v[60:63], v[232:235], a[52:55], v[60:63]\n\t"
    "s_waitcnt lgkmcnt(3)\n\t"
    "v_mfma_f32_16x16x32_bf16 v[64:67], v[228:231], a[52:55], v[64:67]\n\t"
    "buffer_load_dwordx4 a[192:195], v1, s[12:15], s55 offen offset:3072 sc1\n\t"
    "s_waitcnt lgkmcnt(2)\n\t"
    "v_mfma_f32_16x16x32_bf16 v[68:71], v[224:227], a[52:55], v[68:71]\n\t"
    "s_waitcnt lgkmcnt(1)\n\t"
    "v_mfma_f32_16x16x32_bf16 v[72:75], v[220:223], a[52:55], v[72:75]\n\t"
    "s_waitcnt lgkmcnt(0)\n\t"
    "v_mfma_f32_16x16x32_bf16 v[76:79], v[216:219], a[52:55], v[76:79]\n\t"
    "s_waitcnt vmcnt(12)\n\t"
    "v_mfma_f32_16x16x32_bf16 v[80:83], a[76:79], a[56:59], 0\n\t"
    "v_mfma_f32_16x16x32_bf16 v[84:87], a[80:83], a[56:59], 0\n\t"
    "s_add_u32 s55, s55, 0x1000\n\t"
    "buffer_load_dwordx4 a[52:55], v1, s[12:15], s55 offen sc1\n\t"
    "v_mfma_f32_16x16x32_bf16 v[88:91], a[84:87], a[56:59], 0\n\t"
    "ds_write_b128 v58, v[60:63]\n\t"
    "v_mfma_f32_16x16x32_bf16 v[92:95], a[88:91], a[56:59], 0\n\t"
    "v_mfma_f32_16x16x32_bf16 v[96:99], a[92:95], a[56:59], 0\n\t"
    "s_waitcnt vmcnt(12)\n\t"
    "v_mfma_f32_16x16x32_bf16 v[80:83], a[96:99], a[60:63], v[80:83]\n\t"
    "v_mfma_f32_16x16x32_bf16 v[84:87], v[164:167], a[60:63], v[84:87]\n\t"
    "buffer_load_dwordx4 a[56:59], v1, s[20:23], s55 offen offset:1024 sc1\n\t"
    "v_mfma_f32_16x16x32_bf16 v[88:91], v[160:163], a[60:63], v[88:91]\n\t"
    "ds_write_b128 v58, v[64:67] offset:1024\n\t"
    "v_mfma_f32_16x16x32_bf16 v[92:95], v[156:159], a[60:63], v[92:95]\n\t"
    "v_mfma_f32_16x16x32_bf16 v[96:99], v[152:155], a[60:63], v[96:99]\n\t"
    "s_waitcnt vmcnt(12)\n\t"
    "v_mfma_f32_16x16x32_bf16 v[80:83], v[148:151], a[64:67], v[80:83]\n\t"
    "v_mfma_f32_16x16x32_bf16 v[84:87], v[144:147], a[64:67], v[84:87]\n\t"
    "ds_write_b128 v58, v[68:71] offset:2048\n\t"
    "v_mfma_f32_16x16x32_bf16 v[88:91], v[140:143], a[64:67], v[88:91]\n\t"
    "v_mfma_f32_16x16x32_bf16 v[92:95], v[128:131], a[64:67], v[92:95]\n\t"
    "v_mfma_f32_16x16x32_bf16 v[96:99], v[24:27], a[64:67], v[96:99]\n\t"
    "s_waitcnt vmcnt(11)\n\t"
    "v_mfma_f32_16x16x32_bf16 v[80:83], a[100:103], a[68:71], v[80:83]\n\t"
    "ds_write_b128 v58, v[72:75] offset:3072\n\t"
    "v_mfma_f32_16x16x32_bf16 v[84:87], a[104:107], a[68:71], v[84:87]\n\t"
    "v_mfma_f32_16x16x32_bf16 v[88:91], a[108:111], a[68:71], v[88:91]\n\t"
    "v_mfma_f32_16x16x32_bf16 v[92:95], a[112:115], a[68:71], v[92:95]\n\t"
    "v_mfma_f32_16x16x32_bf16 v[96:99], a[116:119], a[68:71], v[96:99]\n\t"
    "ds_write_b128 v58, v[76:79] offset:4096\n\t"
    "s_waitcnt vmcnt(10)\n\t"
    "v_mfma_f32_16x16x32_bf16 v[80:83], a[120:123], a[72:75], v[80:83]\n\t"
    "v_mfma_f32_16x16x32_bf16 v[84:87], a[124:127], a[72:75], v[84:87]\n\t"
    "v_mfma_f32_16x16x32_bf16 v[88:91], a[128:131], a[72:75], v[88:91]\n\t"
    "v_mfma_f32_16x16x32_bf16 v[92:95], a[132:135], a[72:75], v[92:95]\n\t"
    "v_lshlrev_b32 v16, 16, v4\n\t"
    "v_and_b32 v17, 0xffff0000, v4\n\t"
    "v_mfma_f32_16x16x32_bf16 v[96:99], a[136:139], a[72:75], v[96:99]\n\t"
    "v_lshlrev_b32 v18, 16, v5\n\t"
    "s_waitcnt vmcnt(9)\n\t"
    "v_mfma_f32_16x16x32_bf16 v[80:83], a[140:143], a[168:171], v[80:83]\n\t"
    "v_and_b32 v19, 0xffff0000, v5\n\t"
    "v_lshlrev_b32 v20, 16, v6\n\t"
    "v_mfma_f32_16x16x32_bf16 v[84:87], a[144:147], a[168:171], v[84:87]\n\t"
    "s_waitcnt vmcnt(5)\n\t"
    "v_cmp_ne_u64 vcc, v[2:3], v[104:105]\n\t"
    "s_nop 0\n\t"
    "s_cbranch_vccz L48_%=\n\t"
    "poll49_%=:\n\t"
    "s_sleep 2\n\t"
    "buffer_load_dwordx2 v[104:105], v15, s[12:15], s49 offen offset:384 sc1\n\t"
    "s_waitcnt vmcnt(0)\n\t"
    "v_cmp_ne_u64 vcc, v[2:3], v[104:105]\n\t"
    "s_nop 0\n\t"
    "s_cbranch_vccz L48_%=\n\t"
    "s_sub_u32 s37, s37, 1\n\t"
    "s_cbranch_scc0 poll49_%=\n\t"
    "s_mov_b32 s37, 0\n\t"
    "L48_%=:\n\t"
    "buffer_load_dwordx4 a[60:63], v1, s[12:15], s56 offen sc1\n\t"
    "v_mfma_f32_16x16x32_bf16 v[88:91], a[148:151], a[168:171], v[88:91]\n\t"
    "v_and_b32 v21, 0xffff0000, v6\n\t"
    "v_lshlrev_b32 v22, 16, v7\n\t"
    "v_mfma_f32_16x16x32_bf16 v[92:95], a[152:155], a[168:171], v[92:95]\n\t"
    "v_and_b32 v23, 0xffff0000, v7\n\t"
    "v_lshlrev_b32 v44, 16, v8\n\t"
    "v_mfma_f32_16x16x32_bf16 v[96:99], a[156:159], a[168:171], v[96:99]\n\t"
    "buffer_load_dwordx4 a[64:67], v1, s[12:15], s56 offen offset:1024 sc1\n\t"
    "v_mfma_f32_16x16x32_bf16 v[80:83], a[160:163], a[172:175], v[80:83]\n\t"
    "v_and_b32 v45, 0xffff0000, v8\n\t"
    "v_lshlrev_b32 v46, 16, v9\n\t"
    "v_mfma_f32_16x16x32_bf16 v[84:87], a[0:3], a[172:175], v[84:87]\n\t"
    "buffer_load_dwordx4 a[68:71], v1, s[12:15], s56 offen offset:2048 sc1\n\t"
    "v_mfma_f32_16x16x32_bf16 v[88:91], a[164:167], a[172:175], v[88:91]\n\t"
    "v_and_b32 v47, 0xffff0000, v9\n\t"
    "v_lshlrev_b32 v48, 16, v10\n\t"
    "v_mfma_f32_16x16x32_bf16 v[92:95], a[4:7], a[172:175], v[92:95]\n\t"
    "v_and_b32 v49, 0xffff0000, v10\n\t"
    "v_lshlrev_b32 v50, 16, v11\n\t"
    "v_mfma_f32_16x16x32_bf16 v[96:99], a[8:11], a[172:175], v[96:99]\n\t"
    "buffer_load_dwordx4 a[72:75], v1, s[12:15], s56 offen offset:3072 sc1\n\t"
    "v_mfma_f32_16x16x32_bf16 v[80:83], a[12:15], a[176:179], v[80:83]\n\t"
    "v_and_b32 v51, 0xffff0000, v11\n\t"
    "v_lshlrev_b32 v52, 16, v12\n\t"
    "v_mfma_f32_16x16x32_bf16 v[84:87], a[16:19], a[176:179], v[84:87]\n\t"
    "s_add_u32 s56, s56, 0x1000\n\t"
    "buffer_load_dwordx4 a[168:171], v1, s[12:15], s56 offen sc1\n\t"
    "v_mfma_f32_16x16x32_bf16 v[88:91], a[20:23], a[176:179], v[88:91]\n\t"
    "v_and_b32 v53, 0xffff0000, v12\n\t"
    "v_lshlrev_b32 v54, 16, v13\n\t"
    "v_mfma_f32_16x16x32_bf16 v[92:95], a[24:27], a[176:179], v[92:95]\n\t"
    "v_and_b32 v55, 0xffff0000, v13\n\t"
    "v_lshlrev_b32 v56, 16, v32\n\t"
    "v_mfma_f32_16x16x32_bf16 v[96:99], a[28:31], a[176:179], v[96:99]\n\t"
    "buffer_load_dwordx4 a[172:175], v1, s[12:15], s56 offen offset:1024 sc1\n\t"
    "v_mfma_f32_16x16x32_bf16 v[80:83], a[32:35], a[180:183], v[80:83]\n\t"
    "v_and_b32 v57, 0xffff0000, v32\n\t"
    "v_add_f32 v16, v16, v56\n\t"
    "v_mfma_f32_16x16x32_bf16 v[84:87], a[36:39], a[180:183], v[84:87]\n\t"
    "buffer_load_dwordx4 a[176:179], v1, s[12:15], s56 offen offset:2048 sc1\n\t"
    "v_mfma_f32_16x16x32_bf16 v[88:91], a[40:43], a[180:183], v[88:91]\n\t"
    "v_add_f32 v17, v17, v57\n\t"
    "v_lshlrev_b32 v56, 16, v33\n\t"
    "v_mfma_f32_16x16x32_bf16 v[92:95], a[44:47], a[180:183], v[92:95]\n\t"
    "v_and_b32 v57, 0xffff0000, v33\n\t"
    "v_add_f32 v18, v18, v56\n\t"
    "v_mfma_f32_16x16x32_bf16 v[96:99], a[48:51], a[180:183], v[96:99]\n\t"
    "v_add_f32 v19, v19, v57\n\t"
    "s_waitcnt vmcnt(11)\n\t"
    "v_mfma_f32_16x16x32_bf16 v[80:83], a[196:199], a[184:187], v[80:83]\n\t"
    "v_lshlrev_b32 v56, 16, v34\n\t"
    "v_and_b32 v57, 0xffff0000, v34\n\t"
    "v_mfma_f32_16x16x32_bf16 v[84:87], a[200:203], a[184:187], v[84:87]\n\t"
    "buffer_load_dwordx4 a[180:183], v1, s[12:15], s56 offen offset:3072 sc1\n\t"
    "v_mfma_f32_16x16x32_bf16 v[88:91], a[204:207], a[184:187], v[88:91]\n\t"
    "v_add_f32 v20, v20, v56\n\t"
    "v_add_f32 v21, v21, v57\n\t"
    "v_mfma_f32_16x16x32_bf16 v[92:95], a[208:211], a[184:187], v[92:95]\n\t"
    "v_lshlrev_b32 v56, 16, v35\n\t"
    "v_and_b32 v57, 0xffff0000, v35\n\t"
    "v_mfma_f32_16x16x32_bf16 v[96:99], a[212:215], a[184:187], v[96:99]\n\t"
    "v_add_f32 v22, v22, v56\n\t"
    "s_waitcnt vmcnt(11)\n\t"
    "v_mfma_f32_16x16x32_bf16 v[80:83], a[216:219], a[188:191], v[80:83]\n\t"
    "v_add_f32 v23, v23, v57\n\t"
    "v_lshlrev_b32 v56, 16, v36\n\t"
    "v_mfma_f32_16x16x32_bf16 v[84:87], a[220:223], a[188:191], v[84:87]\n\t"
    "s_add_u32 s56, s56, 0x1000\n\t"
    "buffer_load_dwordx4 a[184:187], v1, s[12:15], s56 offen sc1\n\t"
    "v_mfma_f32_16x16x32_bf16 v[88:91], a[224:227], a[188:191], v[88:91]\n\t"
    "v_and_b32 v57, 0xffff0000, v36\n\t"
    "v_add_f32 v44, v44, v56\n\t"
    "v_mfma_f32_16x16x32_bf16 v[92:95], a[228:231], a[188:191], v[92:95]\n\t"
    "v_add_f32 v45, v45, v57\n\t"
    "v_lshlrev_b32 v56, 16, v37\n\t"
    "v_mfma_f32_16x16x32_bf16 v[96:99], a[232:235], a[188:191], v[96:99]\n\t"
    "v_and_b32 v57, 0xffff0000, v37\n\t"
    "s_waitcnt vmcnt(11)\n\t"
    "v_mfma_f32_16x16x32_bf16 v[80:83], a[236:239], a[192:195], v[80:83]\n\t"
    "v_add_f32 v46, v46, v56\n\t"
    "v_add_f32 v47, v47, v57\n\t"
    "v_mfma_f32_16x16x32_bf16 v[84:87], a[240:243], a[192:195], v[84:87]\n\t"
    "buffer_load_dwordx4 a[188:191], v1, s[12:15], s56 offen offset:1024 sc1\n\t"
    "v_mfma_f32_16x16x32_bf16 v[88:91], a[244:247], a[192:195], v[88:91]\n\t"
    "v_lshlrev_b32 v56, 16, v38\n\t"
    "v_and_b32 v57, 0xffff0000, v38\n\t"
    "v_mfma_f32_16x16x32_bf16 v[92:95], a[248:251], a[192:195], v[92:95]\n\t"
    "v_add_f32 v48, v48, v56\n\t"
    "v_add_f32 v49, v49, v57\n\t"
    "v_mfma_f32_16x16x32_bf16 v[96:99], a[252:255], a[192:195], v[96:99]\n\t"
    "v_lshlrev_b32 v56, 16, v39\n\t"
    "s_waitcnt vmcnt(11)\n\t"
    "v_mfma_f32_16x16x32_bf16 v[80:83], v[252:255], a[52:55], v[80:83]\n\t"
    "v_and_b32 v57, 0xffff0000, v39\n\t"
    "v_add_f32 v50, v50, v56\n\t"
    "v_mfma_f32_16x16x32_bf16 v[84:87], v[248:251], a[52:55], v[84:87]\n\t"
    "buffer_load_dwordx4 a[192:195], v1, s[12:15], s56 offen offset:2048 sc1\n\t"
    "v_mfma_f32_16x16x32_bf16 v[88:91], v[244:247], a[52:55], v[88:91]\n\t"
    "v_add_f32 v51, v51, v57\n\t"
    "v_lshlrev_b32 v56, 16, v40\n\t"
    "v_mfma_f32_16x16x32_bf16 v[92:95], v[240:243], a[52:55], v[92:95]\n\t"
    "v_and_b32 v57, 0xffff0000, v40\n\t"
    "v_add_f32 v52, v52, v56\n\t"
    "v_mfma_f32_16x16x32_bf16 v[96:99], v[236:239], a[52:55], v[96:99]\n\t"
    "v_add_f32 v53, v53, v57\n\t"
    "s_waitcnt vmcnt(11)\n\t"
    "v_mfma_f32_16x16x32_bf16 v[80:83], v[232:235], a[56:59], v[80:83]\n\t"
    "v_lshlrev_b32 v56, 16, v41\n\t"
    "v_and_b32 v57, 0xffff0000, v41\n\t"
    "v_mfma_f32_16x16x32_bf16 v[84:87], v[228:231], a[56:59], v[84:87]\n\t"
    "buffer_load_dwordx4 a[52:55], v1, s[12:15], s56 offen offset:3072 sc1\n\t"
    "v_mfma_f32_16x16x32_bf16 v[88:91], v[224:227], a[56:59], v[88:91]\n\t"
    "v_add_f32 v54, v54, v56\n\t"
    "v_add_f32 v55, v55, v57\n\t"
    "v_mfma_f32_16x16x32_bf16 v[92:95], v[220:223], a[56:59], v[92:95]\n\t"
    "v_mfma_f32_16x16x32_bf16 v[96:99], v[216:219], a[56:59], v[96:99]\n\t"
    "s_waitcnt vmcnt(11)\n\t"
    "v_mfma_f32_16x16x32_bf16 v[60:63], a[76:79], a[60:63], 0\n\t"
    "v_mfma_f32_16x16x32_bf16 v[64:67], a[80:83], a[60:63], 0\n\t"
    "s_add_u32 s56, s56, 0x1000\n\t"
    "buffer_load_dwordx4 a[56:59], v1, s[12:15], s56 offen sc1\n\t"
    "v_mfma_f32_16x16x32_bf16 v[68:71], a[84:87], a[60:63], 0\n\t"
    "ds_write_b128 v58, v[80:83] offset:5120\n\t"
    "v_mfma_f32_16x16x32_bf16 v[72:75], a[88:91], a[60:63], 0\n\t"
    "v_mfma_f32_16x16x32_bf16 v[76:79], a[92:95], a[60:63], 0\n\t"
    "s_waitcnt vmcnt(11)\n\t"
    "v_mfma_f32_16x16x32_bf16 v[60:63], a[96:99], a[64:67], v[60:63]\n\t"
    "v_mfma_f32_16x16x32_bf16 v[64:67], v[164:167], a[64:67], v[64:67]\n\t"
    "buffer_load_dwordx4 a[60:63], v1, s[20:23], s56 offen offset:1024 sc1\n\t"
    "v_mfma_f32_16x16x32_bf16 v[68:71], v[160:163], a[64:67], v[68:71]\n\t"
    "ds_write_b128 v58, v[84:87] offset:6144\n\t"
    "v_mfma_f32_16x16x32_bf16 v[72:75], v[156:159], a[64:67], v[72:75]\n\t"
    "v_mfma_f32_16x16x32_bf16 v[76:79], v[152:155], a[64:67], v[76:79]\n\t"
    "s_waitcnt vmcnt(11)\n\t"
    "v_mfma_f32_16x16x32_bf16 v[60:63], v[148:151], a[68:71], v[60:63]\n\t"
    "v_mfma_f32_16x16x32_bf16 v[64:67], v[144:147], a[68:71], v[64:67]\n\t"
    "pad10_%=:\n\t"
    "s_cbranch_execz vb15_%=\n\t"
    "buffer_load_dwordx4 a[64:67], v1, s[12:15], s57 offen sc1\n\t"
    "v_mfma_f32_16x16x32_bf16 v[68:71], v[140:143], a[68:71], v[68:71]\n\t"
    "ds_write_b128 v58, v[88:91] offset:7168\n\t"
    "v_mfma_f32_16x16x32_bf16 v[72:75], v[128:131], a[68:71], v[72:75]\n\t"
    "v_mfma_f32_16x16x32_bf16 v[76:79], v[24:27], a[68:71], v[76:79]\n\t"
    "s_waitcnt vmcnt(11)\n\t"
    "v_mfma_f32_16x16x32_bf16 v[60:63], a[100:103], a[72:75], v[60:63]\n\t"
    "v_mfma_f32_16x16x32_bf16 v[64:67], a[104:107], a[72:75], v[64:67]\n\t"
    "buffer_load_dwordx4 a[68:71], v1, s[12:15], s57 offen offset:1024 sc1\n\t"
    "v_mfma_f32_16x16x32_bf16 v[68:71], a[108:111], a[72:75], v[68:71]\n\t"
    "ds_write_b128 v58, v[92:95] offset:8192\n\t"
    "v_mfma_f32_16x16x32_bf16 v[72:75], a[112:115], a[72:75], v[72:75]\n\t"
    "v_mfma_f32_16x16x32_bf16 v[76:79], a[116:119], a[72:75], v[76:79]\n\t"
    "s_waitcnt vmcnt(11)\n\t"
    "v_mfma_f32_16x16x32_bf16 v[60:63], a[120:123], a[168:171], v[60:63]\n\t"
    "v_mfma_f32_16x16x32_bf16 v[64:67], a[124:127], a[168:171], v[64:67]\n\t"
    "buffer_load_dwordx4 a[72:75], v1, s[12:15], s57 offen offset:2048 sc1\n\t"
    "v_mfma_f32_16x16x32_bf16 v[68:71], a[128:131], a[168:171], v[68:71]\n\t"
    "ds_write_b128 v58, v[96:99] offset:9216\n\t"
    "v_mfma_f32_16x16x32_bf16 v[72:75], a[132:135], a[168:171], v[72:75]\n\t"
    "v_mfma_f32_16x16x32_bf16 v[76:79], a[136:139], a[168:171], v[76:79]\n\t"
    "s_waitcnt vmcnt(11)\n\t"
    "v_mfma_f32_16x16x32_bf16 v[60:63], a[140:143], a[172:175], v[60:63]\n\t"
    "v_mfma_f32_16x16x32_bf16 v[64:67], a[144:147], a[172:175], v[64:67]\n\t"
    "buffer_load_dwordx4 a[168:171], v1, s[12:15], s57 offen offset:3072 sc1\n\t"
    "v_mfma_f32_16x16x32_bf16 v[68:71], a[148:151], a[172:175], v[68:71]\n\t"
    "v_mfma_f32_16x16x32_bf16 v[72:75], a[152:155], a[172:175], v[72:75]\n\t"
    "v_mfma_f32_16x16x32_bf16 v[76:79], a[156:159], a[172:175], v[76:79]\n\t"
    "s_waitcnt vmcnt(11)\n\t"
    "v_mfma_f32_16x16x32_bf16 v[60:63], a[160:163], a[176:179], v[60:63]\n\t"
    "v_mfma_f32_16x16x32_bf16 v[64:67], a[0:3], a[176:179], v[64:67]\n\t"
    "s_add_u32 s57, s57, 0x1000\n\t"
    "buffer_load_dwordx4 a[172:175], v1, s[12:15], s57 offen sc1\n\t"
    "v_mfma_f32_16x16x32_bf16 v[68:71], a[164:167], a[176:179], v[68:71]\n\t"
    "v_mfma_f32_16x16x32_bf16 v[72:75], a[4:7], a[176:179], v[72:75]\n\t"
    "v_mfma_f32_16x16x32_bf16 v[76:79], a[8:11], a[176:179], v[76:79]\n\t"
    "s_waitcnt vmcnt(11)\n\t"
    "v_mfma_f32_16x16x32_bf16 v[60:63], a[12:15], a[180:183], v[60:63]\n\t"
    "v_mfma_f32_16x16x32_bf16 v[64:67], a[16:19], a[180:183], v[64:67]\n\t"
    "buffer_load_dwordx4 a[176:179], v1, s[12:15], s57 offen offset:1024 sc1\n\t"
    "v_mfma_f32_16x16x32_bf16 v[68:71], a[20:23], a[180:183], v[68:71]\n\t"
    "v_mfma_f32_16x16x32_bf16 v[72:75], a[24:27], a[180:183], v[72:75]\n\t"
    "v_mfma_f32_16x16x32_bf16 v[76:79], a[28:31], a[180:183], v[76:79]\n\t"
    "s_waitcnt vmcnt(11)\n\t"
    "v_mfma_f32_16x16x32_bf16 v[60:63], a[32:35], a[184:187], v[60:63]\n\t"
    "v_mfma_f32_16x16x32_bf16 v[64:67], a[36:39], a[184:187], v[64:67]\n\t"
    "buffer_load_dwordx4 a[180:183], v1, s[12:15], s57 offen offset:2048 sc1\n\t"
    "v_mfma_f32_16x16x32_bf16 v[68:71], a[40:43], a[184:187], v[68:71]\n\t"
    "v_mfma_f32_16x16x32_bf16 v[72:75], a[44:47], a[184:187], v[72:75]\n\t"
    "v_mfma_f32_16x16x32_bf16 v[76:79], a[48:51], a[184:187], v[76:79]\n\t"
    "s_waitcnt vmcnt(11)\n\t"
    "v_mfma_f32_16x16x32_bf16 v[60:63], a[196:199], a[188:191], v[60:63]\n\t"
    "v_mfma_f32_16x16x32_bf16 v[64:67], a[200:203], a[188:191], v[64:67]\n\t"
    "buffer_load_dwordx4 a[184:187], v1, s[12:15], s57 offen offset:3072 sc1\n\t"
    "v_mfma_f32_16x16x32_bf16 v[68:71], a[204:207], a[188:191], v[68:71]\n\t"
    "v_mfma_f32_16x16x32_bf16 v[72:75], a[208:211], a[188:191], v[72:75]\n\t"
    "v_mfma_f32_16x16x32_bf16 v[76:79], a[212:215], a[188:191], v[76:79]\n\t"
    "s_waitcnt vmcnt(11)\n\t"
    "v_mfma_f32_16x16x32_bf16 v[60:63], a[216:219], a[192:195], v[60:63]\n\t"
    "v_mfma_f32_16x16x32_bf16 v[64:67], a[220:223], a[192:195], v[64:67]\n\t"
    "s_add_u32 s57, s57, 0x1000\n\t"
    "buffer_load_dwordx4 a[188:191], v1, s[12:15], s57 offen sc1\n\t"
    "v_mfma_f32_16x16x32_bf16 v[68:71], a[224:227], a[192:195], v[68:71]\n\t"
    "v_mfma_f32_16x16x32_bf16 v[72:75], a[228:231], a[192:195], v[72:75]\n\t"
    "v_mfma_f32_16x16x32_bf16 v[76:79], a[232:235], a[192:195], v[76:79]\n\t"
    "s_waitcnt vmcnt(11)\n\t"
    "v_mfma_f32_16x16x32_bf16 v[60:63], a[236:239], a[52:55], v[60:63]\n\t"
    "v_mfma_f32_16x16x32_bf16 v[64:67], a[240:243], a[52:55], v[64:67]\n\t"
    "buffer_load_dwordx4 a[192:195], v1, s[12:15], s57 offen offset:1024 sc1\n\t"
    "v_mfma_f32_16x16x32_bf16 v[68:71], a[244:247], a[52:55], v[68:71]\n\t"
    "v_mfma_f32_16x16x32_bf16 v[72:75], a[248:251], a[52:55], v[72:75]\n\t"
    "v_mfma_f32_16x16x32_bf16 v[76:79], a[252:255], a[52:55], v[76:79]\n\t"
    "s_waitcnt vmcnt(11)\n\t"
    "v_mfma_f32_16x16x32_bf16 v[60:63], v[252:255], a[56:59], v[60:63]\n\t"
    "v_mfma_f32_16x16x32_bf16 v[64:67], v[248:251], a[56:59], v[64:67]\n\t"
    "buffer_load_dwordx4 a[52:55], v1, s[12:15], s57 offen offset:2048 sc1\n\t"
    "v_mfma_f32_16x16x32_bf16 v[68:71], v[244:247], a[56:59], v[68:71]\n\t"
    "v_mfma_f32_16x16x32_bf16 v[72:75], v[240:243], a[56:59], v[72:75]\n\t"
    "v_mfma_f32_16x16x32_bf16 v[76:79], v[236:239], a[56:59], v[76:79]\n\t"
    "s_waitcnt vmcnt(11)\n\t"
    "v_mfma_f32_16x16x32_bf16 v[60:63], v[232:235], a[60:63], v[60:63]\n\t"
    "v_mfma_f32_16x16x32_bf16 v[64:67], v[228:231], a[60:63], v[64:67]\n\t"
    "buffer_load_dwordx4 a[56:59], v1, s[12:15], s57 offen offset:3072 sc1\n\t"
    "v_mfma_f32_16x16x32_bf16 v[68:71], v[224:227], a[60:63], v[68:71]\n\t"
    "v_mfma_f32_16x16x32_bf16 v[72:75], v[220:223], a[60:63], v[72:75]\n\t"
    "v_mfma_f32_16x16x32_bf16 v[76:79], v[216:219], a[60:63], v[76:79]\n\t"
    "s_waitcnt vmcnt(11)\n\t"
    "v_mfma_f32_16x16x32_bf16 v[16:19], a[76:79], a[64:67], v[16:19]\n\t"
    "v_mfma_f32_16x16x32_bf16 v[20:23], a[80:83], a[64:67], v[20:23]\n\t"
    "s_add_u32 s57, s57, 0x1000\n\t"
    "buffer_load_dwordx4 a[60:63], v1, s[12:15], s57 offen sc1\n\t"
    "v_mfma_f32_16x16x32_bf16 v[44:47], a[84:87], a[64:67], v[44:47]\n\t"
    "ds_write_b128 v58, v[60:63] offset:10240\n\t"
    "v_mfma_f32_16x16x32_bf16 v[48:51], a[88:91], a[64:67], v[48:51]\n\t"
    "v_mfma_f32_16x16x32_bf16 v[52:55], a[92:95], a[64:67], v[52:55]\n\t"
    "s_waitcnt vmcnt(11)\n\t"
    "v_mfma_f32_16x16x32_bf16 v[16:19], a[96:99], a[68:71], v[16:19]\n\t"
    "v_mfma_f32_16x16x32_bf16 v[20:23], v[164:167], a[68:71], v[20:23]\n\t"
    "buffer_load_dwordx4 a[64:67], v1, s[20:23], s57 offen offset:1024 sc1\n\t"
    "v_mfma_f32_16x16x32_bf16 v[44:47], v[160:163], a[68:71], v[44:47]\n\t"
    "ds_write_b128 v58, v[64:67] offset:11264\n\t"
    "v_mfma_f32_16x16x32_bf16 v[48:51], v[156:159], a[68:71], v[48:51]\n\t"
    "v_mfma_f32_16x16x32_bf16 v[52:55], v[152:155], a[68:71], v[52:55]\n\t"
    "s_waitcnt vmcnt(11)\n\t"
    "v_mfma_f32_16x16x32_bf16 v[16:19], v[148:151], a[72:75], v[16:19]\n\t"
    "v_mfma_f32_16x16x32_bf16 v[20:23], v[144:147], a[72:75], v[20:23]\n\t"
    "s_and_b32 s66, s34, 1\n\t"
    "s_mul_i32 s66, s66, 0xa000\n\t"
    "s_mul_i32 s32, s33, 0x2800\n\t"
    "s_add_u32 s66, s66, s32\n\t"
    "s_add_u32 s66, s66, 0x108000\n\t"
    "buffer_load_dwordx4 a[76:79], v1, s[8:11], s66 offen sc1\n\t"
    "v_mfma_f32_16x16x32_bf16 v[44:47], v[140:143], a[72:75], v[44:47]\n\t"
    "ds_write_b128 v58, v[68:71] offset:12288\n\t"
    "v_mfma_f32_16x16x32_bf16 v[48:51], v[128:131], a[72:75], v[48:51]\n\t"
    "v_mfma_f32_16x16x32_bf16 v[52:55], v[24:27], a[72:75], v[52:55]\n\t"
    "s_waitcnt vmcnt(11)\n\t"
    "v_mfma_f32_16x16x32_bf16 v[16:19], a[100:103], a[168:171], v[16:19]\n\t"
    "v_mfma_f32_16x16x32_bf16 v[20:23], a[104:107], a[168:171], v[20:23]\n\t"
    "buffer_load_dwordx4 a[80:83], v1, s[8:11], s66 offen offset:1024 sc1\n\t"
    "v_mfma_f32_16x16x32_bf16 v[44:47], a[108:111], a[168:171], v[44:47]\n\t"
    "ds_write_b128 v58, v[72:75] offset:13312\n\t"
    "v_mfma_f32_16x16x32_bf16 v[48:51], a[112:115], a[168:171], v[48:51]\n\t"
    "v_mfma_f32_16x16x32_bf16 v[52:55], a[116:119], a[168:171], v[52:55]\n\t"
    "s_waitcnt vmcnt(11)\n\t"
    "v_mfma_f32_16x16x32_bf16 v[16:19], a[120:123], a[172:175], v[16:19]\n\t"
    "v_mfma_f32_16x16x32_bf16 v[20:23], a[124:127], a[172:175], v[20:23]\n\t"
    "buffer_load_dwordx4 a[84:87], v1, s[8:11], s66 offen offset:2048 sc1\n\t"
    "v_mfma_f32_16x16x32_bf16 v[44:47], a[128:131], a[172:175], v[44:47]\n\t"
    "ds_write_b128 v58, v[76:79] offset:14336\n\t"
    "v_mfma_f32_16x16x32_bf16 v[48:51], a[132:135], a[172:175], v[48:51]\n\t"
    "v_mfma_f32_16x16x32_bf16 v[52:55], a[136:139], a[172:175], v[52:55]\n\t"
    "s_waitcnt vmcnt(11)\n\t"
    "v_mfma_f32_16x16x32_bf16 v[16:19], a[140:143], a[176:179], v[16:19]\n\t"
    "v_mfma_f32_16x16x32_bf16 v[20:23], a[144:147], a[176:179], v[20:23]\n\t"
    "buffer_load_dwordx4 a[88:91], v1, s[8:11], s66 offen offset:3072 sc1\n\t"
    "v_mfma_f32_16x16x32_bf16 v[44:47], a[148:151], a[176:179], v[44:47]\n\t"
    "v_mfma_f32_16x16x32_bf16 v[48:51], a[152:155], a[176:179], v[48:51]\n\t"
    "s_waitcnt lgkmcnt(0)\n\t"
    "s_barrier\n\t"
    "v_mfma_f32_16x16x32_bf16 v[52:55], a[156:159], a[176:179], v[52:55]\n\t"
    "ds_read_b128 v[24:27], v100\n\t"
    "s_waitcnt vmcnt(11)\n\t"
    "v_mfma_f32_16x16x32_bf16 v[16:19], a[160:163], a[180:183], v[16:19]\n\t"
    "ds_read_b128 v[32:35], v100 offset:1024\n\t"
    "v_mfma_f32_16x16x32_bf16 v[20:23], a[0:3], a[180:183], v[20:23]\n\t"
    "s_add_u32 s66, s66, 0x1000\n\t"
    "buffer_load_dwordx4 a[0:3], v1, s[8:11], s66 offen sc1\n\t"
    "v_mfma_f32_16x16x32_bf16 v[44:47], a[164:167], a[180:183], v[44:47]\n\t"
    "ds_read_b128 v[36:39], v100 offset:2048\n\t"
    "v_mfma_f32_16x16x32_bf16 v[48:51], a[4:7], a[180:183], v[48:51]\n\t"
    "ds_read_b128 v[40:43], v100 offset:3072\n\t"
    "v_mfma_f32_16x16x32_bf16 v[52:55], a[8:11], a[180:183], v[52:55]\n\t"
    "ds_read_b128 v[60:63], v100 offset:4096\n\t"
    "s_waitcnt vmcnt(11)\n\t"
    "v_mfma_f32_16x16x32_bf16 v[16:19], a[12:15], a[184:187], v[16:19]\n\t"
    "ds_read_b128 v[64:67], v101\n\t"
    "v_mfma_f32_16x16x32_bf16 v[20:23], a[16:19], a[184:187], v[20:23]\n\t"
    "buffer_load_dwordx4 a[4:7], v1, s[8:11], s66 offen offset:1024 sc1\n\t"
    "v_mfma_f32_16x16x32_bf16 v[44:47], a[20:23], a[184:187], v[44:47]\n\t"
    "ds_read_b128 v[68:71], v101 offset:1024\n\t"
    "v_mfma_f32_16x16x32_bf16 v[48:51], a[24:27], a[184:187], v[48:51]\n\t"
    "ds_read_b128 v[72:75], v101 offset:2048\n\t"
    "v_mfma_f32_16x16x32_bf16 v[52:55], a[28:31], a[184:187], v[52:55]\n\t"
    "ds_read_b128 v[76:79], v101 offset:3072\n\t"
    "s_waitcnt vmcnt(11)\n\t"
    "v_mfma_f32_16x16x32_bf16 v[16:19], a[32:35], a[188:191], v[16:19]\n\t"
    "ds_read_b128 v[80:83], v101 offset:4096\n\t"
    "v_mfma_f32_16x16x32_bf16 v[20:23], a[36:39], a[188:191], v[20:23]\n\t"
    "buffer_load_dwordx4 a[8:11], v1, s[8:11], s66 offen offset:2048 sc1\n\t"
    "v_mfma_f32_16x16x32_bf16 v[44:47], a[40:43], a[188:191], v[44:47]\n\t"
    "ds_read_b128 v[84:87], v102\n\t"
    "v_mfma_f32_16x16x32_bf16 v[48:51], a[44:47], a[188:191], v[48:51]\n\t"
    "ds_read_b128 v[88:91], v102 offset:1024\n\t"
    "v_mfma_f32_16x16x32_bf16 v[52:55], a[48:51], a[188:191], v[52:55]\n\t"
    "ds_read_b128 v[92:95], v102 offset:2048\n\t"
    "s_waitcnt vmcnt(11)\n\t"
    "v_mfma_f32_16x16x32_bf16 v[16:19], a[196:199], a[192:195], v[16:19]\n\t"
    "ds_read_b128 v[96:99], v102 offset:3072\n\t"
    "v_mfma_f32_16x16x32_bf16 v[20:23], a[200:203], a[192:195], v[20:23]\n\t"
    "buffer_load_dwordx4 a[12:15], v1, s[8:11], s66 offen offset:3072 sc1\n\t"
    "v_mfma_f32_16x16x32_bf16 v[44:47], a[204:207], a[192:195], v[44:47]\n\t"
    "ds_read_b128 v[104:107], v102 offset:4096\n\t"
    "v_mfma_f32_16x16x32_bf16 v[48:51], a[208:211], a[192:195], v[48:51]\n\t"
    "v_mfma_f32_16x16x32_bf16 v[52:55], a[212:215], a[192:195], v[52:55]\n\t"
    "s_waitcnt vmcnt(11)\n\t"
    "v_mfma_f32_16x16x32_bf16 v[16:19], a[216:219], a[52:55], v[16:19]\n\t"
    "v_mfma_f32_16x16x32_bf16 v[20:23], a[220:223], a[52:55], v[20:23]\n\t"
    "s_add_u32 s66, s66, 0x1000\n\t"
    "buffer_load_dwordx4 a[16:19], v1, s[8:11], s66 offen sc1\n\t"
    "v_mfma_f32_16x16x32_bf16 v[44:47], a[224:227], a[52:55], v[44:47]\n\t"
    "s_waitcnt lgkmcnt(9)\n\t"
    "v_add_f32 v24, v24, v64\n\t"
    "v_add_f32 v25, v25, v65\n\t"
    "v_mfma_f32_16x16x32_bf16 v[48:51], a[228:231], a[52:55], v[48:51]\n\t"
    "v_add_f32 v26, v26, v66\n\t"
    "v_add_f32 v27, v27, v67\n\t"
    "v_mfma_f32_16x16x32_bf16 v[52:55], a[232:235], a[52:55], v[52:55]\n\t"
    "s_waitcnt lgkmcnt(8)\n\t"
    "v_add_f32 v32, v32, v68\n\t"
    "v_add_f32 v33, v33, v69\n\t"
    "s_waitcnt vmcnt(11)\n\t"
    "v_mfma_f32_16x16x32_bf16 v[16:19], a[236:239], a[56:59], v[16:19]\n\t"
    "v_add_f32 v34, v34, v70\n\t"
    "v_add_f32 v35, v35, v71\n\t"
    "v_mfma_f32_16x16x32_bf16 v[20:23], a[240:243], a[56:59], v[20:23]\n\t"
    "buffer_load_dwordx4 a[20:23], v1, s[8:11], s66 offen offset:1024 sc1\n\t"
    "v_mfma_f32_16x16x32_bf16 v[44:47], a[244:247], a[56:59], v[44:47]\n\t"
    "s_waitcnt lgkmcnt(7)\n\t"
    "v_add_f32 v36, v36, v72\n\t"
    "v_add_f32 v37, v37, v73\n\t"
    "v_mfma_f32_16x16x32_bf16 v[48:51], a[248:251], a[56:59], v[48:51]\n\t"
    "v_add_f32 v38, v38, v74\n\t"
    "v_add_f32 v39, v39, v75\n\t"
    "v_mfma_f32_16x16x32_bf16 v[52:55], a[252:255], a[56:59], v[52:55]\n\t"
    "s_waitcnt lgkmcnt(6)\n\t"
    "v_add_f32 v40, v40, v76\n\t"
    "v_add_f32 v41, v41, v77\n\t"
    "s_waitcnt vmcnt(11)\n\t"
    "v_mfma_f32_16x16x32_bf16 v[16:19], v[252:255], a[60:63], v[16:19]\n\t"
    "v_add_f32 v42, v42, v78\n\t"
    "v_add_f32 v43, v43, v79\n\t"
    "v_mfma_f32_16x16x32_bf16 v[20:23], v[248:251], a[60:63], v[20:23]\n\t"
    "s_mul_i32 s66, s34, 0x19000\n\t"
    "s_mul_i32 s32, s33, 0x6400\n\t"
    "s_add_u32 s66, s66, s32\n\t"
    "s_add_u32 s66, s66, 0x20000\n\t"
    "buffer_load_dwordx4 a[24:27], v1, s[8:11], s66 offen sc1\n\t"
    "v_mfma_f32_16x16x32_bf16 v[44:47], v[244:247], a[60:63], v[44:47]\n\t"
    "s_waitcnt lgkmcnt(5)\n\t"
    "v_add_f32 v60, v60, v80\n\t"
    "v_add_f32 v61, v61, v81\n\t"
    "v_mfma_f32_16x16x32_bf16 v[48:51], v[240:243], a[60:63], v[48:51]\n\t"
    "v_add_f32 v62, v62, v82\n\t"
    "v_add_f32 v63, v63, v83\n\t"
    "v_mfma_f32_16x16x32_bf16 v[52:55], v[236:239], a[60:63], v[52:55]\n\t"
    "s_waitcnt lgkmcnt(4)\n\t"
    "v_add_f32 v24, v24, v84\n\t"
    "v_add_f32 v25, v25, v85\n\t"
    "s_waitcnt vmcnt(11)\n\t"
    "v_mfma_f32_16x16x32_bf16 v[16:19], v[232:235], a[64:67], v[16:19]\n\t"
    "v_add_f32 v26, v26, v86\n\t"
    "v_add_f32 v27, v27, v87\n\t"
    "v_mfma_f32_16x16x32_bf16 v[20:23], v[228:231], a[64:67], v[20:23]\n\t"
    "buffer_load_dwordx4 a[28:31], v1, s[8:11], s66 offen offset:1024 sc1\n\t"
    "v_mfma_f32_16x16x32_bf16 v[44:47], v[224:227], a[64:67], v[44:47]\n\t"
    "s_waitcnt lgkmcnt(3)\n\t"
    "v_add_f32 v32, v32, v88\n\t"
    "v_add_f32 v33, v33, v89\n\t"
    "v_mfma_f32_16x16x32_bf16 v[48:51], v[220:223], a[64:67], v[48:51]\n\t"
    "v_add_f32 v34, v34, v90\n\t"
    "v_add_f32 v35, v35, v91\n\t"
    "v_mfma_f32_16x16x32_bf16 v[52:55], v[216:219], a[64:67], v[52:55]\n\t"
    "s_waitcnt lgkmcnt(2)\n\t"
    "v_add_f32 v36, v36, v92\n\t"
    "v_add_f32 v37, v37, v93\n\t"
    "s_waitcnt lgkmcnt(0)\n\t"
    "v_pk_add_f32 v[38:39], v[38:39], v[94:95]\n\t"
    "v_pk_add_f32 v[40:41], v[40:41], v[96:97]\n\t"
    "v_pk_add_f32 v[42:43], v[42:43], v[98:99]\n\t"
    "v_pk_add_f32 v[60:61], v[60:61], v[104:105]\n\t"
    "v_pk_add_f32 v[62:63], v[62:63], v[106:107]\n\t"
    "v_pk_add_f32 v[16:17], v[16:17], v[24:25]\n\t"
    "v_pk_add_f32 v[18:19], v[18:19], v[26:27]\n\t"
    "v_pk_add_f32 v[20:21], v[20:21], v[32:33]\n\t"
    "v_pk_add_f32 v[22:23], v[22:23], v[34:35]\n\t"
    "v_pk_add_f32 v[44:45], v[44:45], v[36:37]\n\t"
    "v_pk_add_f32 v[46:47], v[46:47], v[38:39]\n\t"
    "v_pk_add_f32 v[48:49], v[48:49], v[40:41]\n\t"
    "v_pk_add_f32 v[50:51], v[50:51], v[42:43]\n\t"
    "v_pk_add_f32 v[52:53], v[52:53], v[60:61]\n\t"
    "v_pk_add_f32 v[54:55], v[54:55], v[62:63]\n\t"
    "v_cvt_pk_bf16_f32 v4, v16, v17\n\t"
    "v_cvt_pk_bf16_f32 v5, v18, v19\n\t"
    "v_cvt_pk_bf16_f32 v6, v20, v21\n\t"
    "v_cvt_pk_bf16_f32 v7, v22, v23\n\t"
    "v_cvt_pk_bf16_f32 v8, v44, v45\n\t"
    "v_cvt_pk_bf16_f32 v9, v46, v47\n\t"
    "v_cvt_pk_bf16_f32 v10, v48, v49\n\t"
    "v_cvt_pk_bf16_f32 v11, v50, v51\n\t"
    "v_cvt_pk_bf16_f32 v12, v52, v53\n\t"
    "v_cvt_pk_bf16_f32 v13, v54, v55\n\t"
    "s_cmp_eq_u32 s36, 0\n\t"
    "s_cbranch_scc0 L50_%=\n\t"
    "buffer_store_dwordx4 v[4:7], v1, s[12:15], s62 offen sc1\n\t"
    "buffer_store_dwordx4 v[8:11], v1, s[12:15], s62 offen offset:1024 sc1\n\t"
    "buffer_store_dwordx2 v[12:13], v14, s[12:15], s63 offen sc1\n\t"
    "v_dot2_f32_bf16 v16, v4, s65, 0\n\t"
    "v_dot2_f32_bf16 v17, v4, v4, 0\n\t"
    "v_dot2_f32_bf16 v16, v5, s65, v16\n\t"
    "v_dot2_f32_bf16 v17, v5, v5, v17\n\t"
    "v_dot2_f32_bf16 v16, v6, s65, v16\n\t"
    "v_dot2_f32_bf16 v17, v6, v6, v17\n\t"
    "v_dot2_f32_bf16 v16, v7, s65, v16\n\t"
    "v_dot2_f32_bf16 v17, v7, v7, v17\n\t"
    "v_dot2_f32_bf16 v16, v8, s65, v16\n\t"
    "v_dot2_f32_bf16 v17, v8, v8, v17\n\t"
    "v_dot2_f32_bf16 v16, v9, s65, v16\n\t"
    "v_dot2_f32_bf16 v17, v9, v9, v17\n\t"
    "v_dot2_f32_bf16 v16, v10, s65, v16\n\t"
    "v_dot2_f32_bf16 v17, v10, v10, v17\n\t"
    "v_dot2_f32_bf16 v16, v11, s65, v16\n\t"
    "v_dot2_f32_bf16 v17, v11, v11, v17\n\t"
    "v_dot2_f32_bf16 v16, v12, s65, v16\n\t"
    "v_dot2_f32_bf16 v17, v12, v12, v17\n\t"
    "v_dot2_f32_bf16 v16, v13, s65, v16\n\t"
    "v_dot2_f32_bf16 v17, v13, v13, v17\n\t"
    "s_nop 2\n\t"
    "v_permlane32_swap_b32 v16, v17\n\t"
    "v_add_f32 v16, v16, v17\n\t"
    "v_mov_b32 v17, v16\n\t"
    "s_nop 1\n\t"
    "v_permlane16_swap_b32 v16, v17\n\t"
    "v_add_f32 v16, v16, v17\n\t"
    "buffer_store_dword v16, v59, s[12:15], s64 offen sc1\n\t"
    "buffer_load_dwordx4 a[32:35], v1, s[8:11], s66 offen offset:2048 sc1\n\t"
    "s_nop 11\n\t"
    "buffer_load_dwordx4 a[36:39], v1, s[8:11], s66 offen offset:3072 sc1\n\t"
    "s_nop 11\n\t"
    "s_add_u32 s66, s66, 0x1000\n\t"
    "buffer_load_dwordx4 a[40:43], v1, s[8:11], s66 offen sc1\n\t"
    "s_nop 10\n\t"
    "buffer_load_dwordx4 a[44:47], v1, s[8:11], s66 offen offset:1024 sc1\n\t"
    "s_nop 11\n\t"
    "buffer_load_dwordx4 a[48:51], v1, s[8:11], s66 offen offset:2048 sc1\n\t"
    "s_nop 11\n\t"
    "buffer_load_dwordx4 a[52:55], v1, s[8:11], s66 offen offset:3072 sc1\n\t"
    "s_nop 11\n\t"
    "s_add_u32 s66, s66, 0x1000\n\t"
    "buffer_load_dwordx4 a[56:59], v1, s[8:11], s66 offen sc1\n\t"
    "s_nop 10\n\t"
    "buffer_load_dwordx4 a[60:63], v1, s[8:11], s66 offen offset:1024 sc1\n\t"
    "s_nop 11\n\t"
    "buffer_load_dwordx4 a[64:67], v1, s[8:11], s66 offen offset:2048 sc1\n\t"
    "s_nop 11\n\t"
    "buffer_load_dwordx4 a[68:71], v1, s[8:11], s66 offen offset:3072 sc1\n\t"
    "s_add_u32 s47, s47, 0x2a0000\n\t"
    "s_add_u32 s45, s45, 0x29000\n\t"
    "s_add_u32 s46, s46, 0x280000\n\t"
    "s_add_u32 s48, s48, 0x6c0000\n\t"
    "s_cmp_eq_u32 s33, 0\n\t"
    "s_waitcnt vmcnt(10)\n\t"
    "s_barrier\n\t"
    "s_cbranch_scc0 L51_%=\n\t"
    "s_mov_b64 exec, 1\n\t"
    "buffer_store_dwordx2 v[2:3], v1, s[12:15], s76 offen offset:640 sc1\n\t"
    "s_mov_b64 exec, -1\n\t"
    "L51_%=:\n\t"
    "s_mov_b32 s36, 1\n\t"
    "v_lshrrev_b32 v40, 4, v0\n\t"
    "v_lshlrev_b32 v40, 5, v40\n\t"
    "s_mul_i32 s51, s39, 0x80\n\t"
    "s_add_u32 s51, s51, 0x11c000\n\t"
    "buffer_load_dwordx4 v[32:35], v40, s[12:15], s51 offen sc1\n\t"
    "buffer_load_dwordx4 v[36:39], v40, s[12:15], s51 offen offset:16 sc1\n\t"
    "s_lshr_b32 s58, s34, 1\n\t"
    "s_add_u32 s50, s58, 0\n\t"
    "s_and_b32 s50, s50, 3\n\t"
    "s_mul_i32 s54, s50, 0x5000\n\t"
    "s_mul_i32 s53, s33, 0x1400\n\t"
    "s_add_u32 s54, s54, s53\n\t"
    "s_add_u32 s54, s54, s44\n\t"
    "s_add_u32 s50, s58, 1\n\t"
    "s_and_b32 s50, s50, 3\n\t"
    "s_mul_i32 s55, s50, 0x5000\n\t"
    "s_mul_i32 s53, s33, 0x1400\n\t"
    "s_add_u32 s55, s55, s53\n\t"
    "s_add_u32 s55, s55, s44\n\t"
    "s_add_u32 s50, s58, 2\n\t"
    "s_and_b32 s50, s50, 3\n\t"
    "s_mul_i32 s56, s50, 0x5000\n\t"
    "s_mul_i32 s53, s33, 0x1400\n\t"
    "s_add_u32 s56, s56, s53\n\t"
    "s_add_u32 s56, s56, s44\n\t"
    "s_add_u32 s50, s58, 3\n\t"
    "s_and_b32 s50, s50, 3\n\t"
    "s_mul_i32 s57, s50, 0x5000\n\t"
    "s_mul_i32 s53, s33, 0x1400\n\t"
    "s_add_u32 s57, s57, s53\n\t"
    "s_add_u32 s57, s57, s44\n\t"
    "s_mul_i32 s59, s33, 0x400\n\t"
    "s_add_u32 s59, s59, s44\n\t"
    "s_add_u32 s59, s59, 0x14000\n\t"
    "s_mul_i32 m0, s33, 0x400\n\t"
    "s_add_u32 m0, m0, 0x27000\n\t"
    "s_mul_i32 s50, s58, 0x80\n\t"
    "s_add_u32 s50, s50, 0x27000\n\t"
    "v_and_b32 v40, 15, v0\n\t"
    "v_lshlrev_b32 v40, 2, v40\n\t"
    "v_lshrrev_b32 v30, 4, v0\n\t"
    "v_lshl_add_u32 v40, v30, 10, v40\n\t"
    "v_add_u32 v40, s50, v40\n\t"
    "s_mul_i32 s50, s33, 0x4c00\n\t"
    "s_add_u32 s50, s50, 0x4400\n\t"
    "v_add_u32 v41, s50, v1\n\t"
    "s_cmp_eq_u32 s33, 0\n\t"
    "s_cbranch_scc0 L52_%=\n\t"
    "buffer_load_dwordx2 v[52:53], v15, s[12:15], s49 offen offset:640 sc1\n\t"
    "L52_%=:\n\t"
    "s_mul_i32 s52, s35, 0x1480\n\t"
    "s_add_u32 s52, s52, s45\n\t"
    "s_bitcmp1_b32 s34, 0\n\t"
    "s_cbranch_scc1 L53_%=\n\t"
    "s_lshr_b32 s50, s58, 1\n\t"
    "s_and_b32 s51, s58, 1\n\t"
    "s_mul_i32 s62, s50, 0x400\n\t"
    "s_mul_i32 s53, s51, 0x100\n\t"
    "s_add_u32 s62, s62, s53\n\t"
    "s_add_u32 s62, s62, s52\n\t"
    "s_mul_i32 s63, s58, 0x80\n\t"
    "s_add_u32 s63, s63, s52\n\t"
    "s_add_u32 s63, s63, 0x800\n\t"
    "v_and_b32 v44, 15, v0\n\t"
    "v_lshlrev_b32 v44, 4, v44\n\t"
    "v_lshrrev_b32 v30, 4, v0\n\t"
    "v_lshl_add_u32 v44, v30, 9, v44\n\t"
    "v_and_b32 v45, 15, v0\n\t"
    "v_lshlrev_b32 v45, 3, v45\n\t"
    "s_mul_i32 s50, s36, 0x4000\n\t"
    "s_mul_i32 s53, s35, 0x200\n\t"
    "s_add_u32 s50, s50, s53\n\t"
    "s_mul_i32 s53, s52, -1\n\t"
    "s_add_u32 s50, s50, s53\n\t"
    "s_add_u32 s50, s50, 0xe55800\n\t"
    "v_add_u32 v42, s50, v45\n\t"
    "v_cmp_lt_u32 vcc, 47, v0\n\t"
    "s_nop 1\n\t"
    "v_cndmask_b32 v45, v45, v42, vcc\n\t"
    "s_branch L54_%=\n\t"
    "L53_%=:\n\t"
    "s_mul_i32 s62, s58, 0x2a0\n\t"
    "s_add_u32 s62, s62, s52\n\t"
    "s_add_u32 s62, s62, 0xa00\n\t"
    "s_mov_b32 s63, 0x3f803f80\n\t"
    "v_and_b32 v44, 15, v0\n\t"
    "v_lshlrev_b32 v44, 6, v44\n\t"
    "v_lshrrev_b32 v30, 4, v0\n\t"
    "v_lshl_add_u32 v44, v30, 4, v44\n\t"
    "v_and_b32 v45, 3, v0\n\t"
    "v_lshlrev_b32 v45, 3, v45\n\t"
    "v_bfe_u32 v30, v0, 2, 2\n\t"
    "v_lshl_add_u32 v45, v30, 6, v45\n\t"
    "v_bfe_u32 v30, v0, 4, 1\n\t"
    "v_lshl_add_u32 v45, v30, 5, v45\n\t"
    "v_lshrrev_b32 v30, 5, v0\n\t"
    "v_lshl_add_u32 v45, v30, 9, v45\n\t"
    "v_and_b32 v46, 31, v0\n\t"
    "v_lshlrev_b32 v46, 4, v46\n\t"
    "v_lshrrev_b32 v30, 5, v0\n\t"
    "v_mul_u32_u24 v30, 0x140, v30\n\t"
    "v_add_u32 v46, v46, v30\n\t"
    "L54_%=:\n\t"
    "s_add_u32 s66, s66, 0x1000\n\t"
    "buffer_load_dwordx4 a[168:171], v1, s[8:11], s66 offen sc1\n\t"
    "s_nop 10\n\t"
    "buffer_load_dwordx4 a[172:175], v1, s[8:11], s66 offen offset:1024 sc1\n\t"
    "s_nop 11\n\t"
    "buffer_load_dwordx4 a[176:179], v1, s[8:11], s66 offen offset:2048 sc1\n\t"
    "s_nop 11\n\t"
    "buffer_load_dwordx4 a[180:183], v1, s[8:11], s66 offen offset:3072 sc1\n\t"
    "s_nop 11\n\t"
    "s_add_u32 s66, s66, 0x1000\n\t"
    "buffer_load_dwordx4 a[184:187], v1, s[8:11], s66 offen sc1\n\t"
    "s_nop 10\n\t"
    "buffer_load_dwordx4 a[188:191], v1, s[8:11], s66 offen offset:1024 sc1\n\t"
    "s_cmp_eq_u32 s33, 0\n\t"
    "s_cbranch_scc0 L55_%=\n\t"
    "s_waitcnt vmcnt(6)\n\t"
    "v_cmp_ne_u64 vcc, v[2:3], v[52:53]\n\t"
    "s_nop 0\n\t"
    "s_cbranch_vccz L56_%=\n\t"
    "poll57_%=:\n\t"
    "s_sleep 2\n\t"
    "buffer_load_dwordx2 v[52:53], v15, s[12:15], s49 offen offset:640 sc1\n\t"
    "s_waitcnt vmcnt(0)\n\t"
    "v_cmp_ne_u64 vcc, v[2:3], v[52:53]\n\t"
    "s_nop 0\n\t"
    "s_cbranch_vccz L56_%=\n\t"
    "s_sub_u32 s37, s37, 1\n\t"
    "s_cbranch_scc0 poll57_%=\n\t"
    "s_mov_b32 s37, 0\n\t"
    "L56_%=:\n\t"
    "L55_%=:\n\t"
    "s_barrier\n\t"
    "s_add_u32 s76, s76, 0x400\n\t"
    "s_add_u32 s49, s49, 0x400\n\t"
    "buffer_load_dwordx4 v1, s[12:15], s59 offen sc1 lds\n\t"
    "s_mul_i32 s61, s36, 0x4000\n\t"
    "s_mul_i32 s53, s35, 64\n\t"
    "s_add_u32 s61, s61, s53\n\t"
    "s_mul_i32 s53, s34, 8\n\t"
    "s_add_u32 s61, s61, s53\n\t"
    "s_add_u32 s61, s61, 0x10000\n\t"
    "v_and_b32 v50, 7, v0\n\t"
    "v_lshlrev_b32 v50, 11, v50\n\t"
    "s_mul_i32 s50, s33, 0x5000\n\t"
    "s_add_u32 s50, s50, 0x13000\n\t"
    "v_add_u32 v31, s50, v1\n\t"
    "s_mul_i32 s50, s33, 0x400\n\t"
    "buffer_load_dwordx4 a[72:75], v1, s[12:15], s54 offen sc1\n\t"
    "s_add_u32 s50, s50, 0x13000\n\t"
    "v_and_b32 v47, 15, v0\n\t"
    "v_lshlrev_b32 v47, 4, v47\n\t"
    "v_bfe_u32 v30, v0, 4, 1\n\t"
    "v_mul_u32_u24 v30, 0x1400, v30\n\t"
    "v_add_u32 v47, v47, v30\n\t"
    "v_lshrrev_b32 v30, 5, v0\n\t"
    "v_lshl_add_u32 v47, v30, 9, v47\n\t"
    "v_add_u32 v47, s50, v47\n\t"
    "v_add_u32 v48, 0x2800, v47\n\t"
    "s_mul_i32 s50, s33, 0x100\n\t"
    "s_add_u32 s50, s50, 0x14000\n\t"
    "buffer_load_dwordx4 a[92:95], v1, s[12:15], s54 offen offset:1024 sc1\n\t"
    "v_and_b32 v49, 15, v0\n\t"
    "v_lshlrev_b32 v49, 4, v49\n\t"
    "v_lshrrev_b32 v30, 4, v0\n\t"
    "v_mul_u32_u24 v30, 0x1400, v30\n\t"
    "v_add_u32 v49, v49, v30\n\t"
    "v_add_u32 v49, s50, v49\n\t"
    "s_mul_i32 s50, s58, 16\n\t"
    "s_add_u32 s50, s50, 16\n\t"
    "v_add_u32 v51, s50, v0\n\t"
    "v_and_b32 v51, 63, v51\n\t"
    "v_lshlrev_b32 v51, 3, v51\n\t"
    "v_mov_b32 v124, 0xdddddddd\n\t"
    "buffer_load_dwordx4 a[96:99], v1, s[12:15], s54 offen offset:2048 sc1\n\t"
    "v_mov_b32 v125, v1\n\t"
    "v_lshlrev_b32 v126, 3, v0\n\t"
    "v_and_b32 v129, 3, v0\n\t"
    "v_lshlrev_b32 v129, 4, v129\n\t"
    "v_bfe_u32 v30, v0, 2, 1\n\t"
    "v_lshl_add_u32 v129, v30, 7, v129\n\t"
    "v_bfe_u32 v30, v0, 3, 1\n\t"
    "v_lshl_add_u32 v129, v30, 6, v129\n\t"
    "v_bfe_u32 v30, v0, 4, 1\n\t"
    "v_lshl_add_u32 v129, v30, 8, v129\n\t"
    "v_lshrrev_b32 v30, 5, v0\n\t"
    "v_mul_u32_u24 v30, 0x140, v30\n\t"
    "buffer_load_dwordx4 a[100:103], v1, s[12:15], s54 offen offset:3072 sc1\n\t"
    "v_add_u32 v129, v129, v30\n\t"
    "v_and_b32 v128, 31, v0\n\t"
    "v_and_b32 v131, 27, v128\n\t"
    "v_mov_b32 v130, 0x290\n\t"
    "v_mov_b32 v132, 0x280\n\t"
    "v_cmp_eq_u32 vcc, 24, v131\n\t"
    "s_nop 1\n\t"
    "v_cndmask_b32 v130, v130, v132, vcc\n\t"
    "v_cmp_gt_u32 vcc, 20, v128\n\t"
    "s_nop 1\n\t"
    "v_cndmask_b32 v127, v130, v129, vcc\n\t"
    "s_add_u32 s54, s54, 0x1000\n\t"
    "buffer_load_dwordx4 a[104:107], v1, s[12:15], s54 offen sc1\n\t"
    "v_mov_b32 v128, 0\n\t"
    "v_mov_b32 v144, 0\n\t"
    "v_mov_b32 v129, 0\n\t"
    "v_mov_b32 v145, 0\n\t"
    "v_mov_b32 v130, 0\n\t"
    "v_mov_b32 v146, 0\n\t"
    "v_mov_b32 v131, 0\n\t"
    "v_mov_b32 v147, 0\n\t"
    "v_mov_b32 v132, 0\n\t"
    "v_mov_b32 v148, 0\n\t"
    "v_mov_b32 v133, 0\n\t"
    "buffer_load_dwordx4 a[192:195], v1, s[8:11], s66 offen offset:2048 sc1\n\t"
    "v_mov_b32 v149, 0\n\t"
    "v_mov_b32 v134, 0\n\t"
    "v_mov_b32 v150, 0\n\t"
    "v_mov_b32 v135, 0\n\t"
    "v_mov_b32 v151, 0\n\t"
    "v_mov_b32 v136, 0\n\t"
    "v_mov_b32 v152, 0\n\t"
    "v_mov_b32 v137, 0\n\t"
    "v_mov_b32 v153, 0\n\t"
    "v_mov_b32 v138, 0\n\t"
    "v_mov_b32 v154, 0\n\t"
    "v_mov_b32 v139, 0\n\t"
    "buffer_load_dwordx4 a[196:199], v1, s[8:11], s66 offen offset:3072 sc1\n\t"
    "v_mov_b32 v155, 0\n\t"
    "v_mov_b32 v140, 0\n\t"
    "v_mov_b32 v156, 0\n\t"
    "v_mov_b32 v141, 0\n\t"
    "v_mov_b32 v157, 0\n\t"
    "v_mov_b32 v142, 0\n\t"
    "v_mov_b32 v158, 0\n\t"
    "v_mov_b32 v143, 0\n\t"
    "v_mov_b32 v159, 0\n\t"
    "v_and_b32 v26, 15, v0\n\t"
    "v_lshlrev_b32 v26, 4, v26\n\t"
    "v_bfe_u32 v30, v0, 4, 1\n\t"
    "s_add_u32 s66, s66, 0x1000\n\t"
    "buffer_load_dwordx4 a[200:203], v1, s[8:11], s66 offen sc1\n\t"
    "v_lshl_add_u32 v26, v30, 14, v26\n\t"
    "v_lshrrev_b32 v30, 5, v0\n\t"
    "v_lshl_add_u32 v26, v30, 8, v26\n\t"
    "v_and_b32 v27, 15, v0\n\t"
    "v_lshlrev_b32 v27, 4, v27\n\t"
    "v_bfe_u32 v30, v0, 4, 1\n\t"
    "v_lshl_add_u32 v27, v30, 13, v27\n\t"
    "s_mul_i32 s78, s34, 0x800\n\t"
    "s_mul_i32 s53, s33, 0x200\n\t"
    "s_add_u32 s78, s78, s53\n\t"
    "s_add_u32 s78, s78, s46\n\t"
    "buffer_load_dwordx4 a[204:207], v1, s[8:11], s66 offen offset:1024 sc1\n\t"
    "s_add_u32 s79, s78, 0x8000\n\t"
    "s_mul_i32 s80, s34, 0x400\n\t"
    "s_mul_i32 s53, s33, 0x100\n\t"
    "s_add_u32 s80, s80, s53\n\t"
    "s_add_u32 s80, s80, s46\n\t"
    "s_add_u32 s80, s80, 0x10000\n\t"
    "s_waitcnt vmcnt(8)\n\t"
    "v_mfma_f32_16x16x32_bf16 v[52:55], a[76:79], a[72:75], 0\n\t"
    "v_mfma_f32_16x16x32_bf16 v[56:59], a[80:83], a[72:75], 0\n\t"
    "s_waitcnt vmcnt(7)\n\t"
    "v_mfma_f32_16x16x32_bf16 v[52:55], a[84:87], a[92:95], v[52:55]\n\t"
    "v_mfma_f32_16x16x32_bf16 v[56:59], a[88:91], a[92:95], v[56:59]\n\t"
    "s_waitcnt vmcnt(6)\n\t"
    "v_mfma_f32_16x16x32_bf16 v[52:55], a[0:3], a[96:99], v[52:55]\n\t"
    "v_mfma_f32_16x16x32_bf16 v[56:59], a[4:7], a[96:99], v[56:59]\n\t"
    "s_waitcnt vmcnt(5)\n\t"
    "v_mfma_f32_16x16x32_bf16 v[52:55], a[8:11], a[100:103], v[52:55]\n\t"
    "v_mfma_f32_16x16x32_bf16 v[56:59], a[12:15], a[100:103], v[56:59]\n\t"
    "s_waitcnt vmcnt(4)\n\t"
    "v_mfma_f32_16x16x32_bf16 v[52:55], a[16:19], a[104:107], v[52:55]\n\t"
    "v_mfma_f32_16x16x32_bf16 v[56:59], a[20:23], a[104:107], v[56:59]\n\t"
    "s_cmp_eq_u32 s33, 0\n\t"
    "s_cbranch_scc1 L58_%=\n\t"
    "s_nop 4\n\t"
    "ds_write_b128 v41, v[52:55]\n\t"
    "ds_write_b128 v41, v[56:59] offset:1024\n\t"
    "s_waitcnt lgkmcnt(0)\n\t"
    "s_barrier\n\t"
    "s_branch L59_%=\n\t"
    "L58_%=:\n\t"
    "s_barrier\n\t"
    "ds_read2_b32 v[88:89], v40 offset0:0 offset1:128\n\t"
    "ds_read2_b32 v[90:91], v40 offset0:16 offset1:144\n\t"
    "ds_read_b128 v[60:63], v41 offset:19456\n\t"
    "ds_read_b128 v[64:67], v41 offset:20480\n\t"
    "ds_read_b128 v[68:71], v41 offset:38912\n\t"
    "ds_read_b128 v[72:75], v41 offset:39936\n\t"
    "ds_read_b128 v[76:79], v41 offset:58368\n\t"
    "ds_read_b128 v[80:83], v41 offset:59392\n\t"
    "s_waitcnt lgkmcnt(6)\n\t"
    "v_add_f32 v88, v88, v89\n\t"
    "v_add_f32 v90, v90, v91\n\t"
    "v_mov_b32 v89, v88\n\t"
    "v_mov_b32 v91, v90\n\t"
    "s_nop 0\n\t"
    "v_permlane32_swap_b32 v88, v89\n\t"
    "v_permlane32_swap_b32 v90, v91\n\t"
    "v_add_f32 v88, v88, v89\n\t"
    "v_add_f32 v90, v90, v91\n\t"
    "v_mov_b32 v89, v88\n\t"
    "v_mov_b32 v91, v90\n\t"
    "s_nop 0\n\t"
    "v_permlane16_swap_b32 v88, v89\n\t"
    "v_permlane16_swap_b32 v90, v91\n\t"
    "v_add_f32 v88, v88, v89\n\t"
    "v_add_f32 v90, v90, v91\n\t"
    "pad11_%=:\n\t"
    "s_cbranch_execz vb15_%=\n\t"
    "v_mul_f32 v88, 0xbacccccd, v88\n\t"
    "v_mul_f32 v90, 0x3acccccd, v90\n\t"
    "v_fma_f32 v90, -v88, v88, v90\n\t"
    "v_max_f32 v90, 0, v90\n\t"
    "v_add_f32 v84, 0x3727c5ac, v90\n\t"
    "v_rsq_f32 v90, v84\n\t"
    "s_waitcnt lgkmcnt(0)\n\t"
    "v_mov_b32 v89, v88\n\t"
    "v_mov_b32 v91, v90\n\t"
    "v_pk_add_f32 v[52:53], v[52:53], v[60:61]\n\t"
    "v_pk_add_f32 v[54:55], v[54:55], v[62:63]\n\t"
    "v_pk_add_f32 v[56:57], v[56:57], v[64:65]\n\t"
    "v_pk_add_f32 v[58:59], v[58:59], v[66:67]\n\t"
    "v_pk_add_f32 v[52:53], v[52:53], v[68:69]\n\t"
    "v_pk_add_f32 v[54:55], v[54:55], v[70:71]\n\t"
    "v_pk_add_f32 v[56:57], v[56:57], v[72:73]\n\t"
    "v_pk_add_f32 v[58:59], v[58:59], v[74:75]\n\t"
    "v_pk_add_f32 v[52:53], v[52:53], v[76:77]\n\t"
    "v_pk_add_f32 v[54:55], v[54:55], v[78:79]\n\t"
    "v_pk_add_f32 v[56:57], v[56:57], v[80:81]\n\t"
    "v_pk_add_f32 v[58:59], v[58:59], v[82:83]\n\t"
    "v_pk_fma_f32 v[52:53], v[88:89], v[32:33], v[52:53]\n\t"
    "v_pk_fma_f32 v[54:55], v[88:89], v[34:35], v[54:55]\n\t"
    "v_pk_fma_f32 v[56:57], v[88:89], v[36:37], v[56:57]\n\t"
    "v_pk_fma_f32 v[58:59], v[88:89], v[38:39], v[58:59]\n\t"
    "v_pk_mul_f32 v[52:53], v[52:53], v[90:91]\n\t"
    "v_pk_mul_f32 v[54:55], v[54:55], v[90:91]\n\t"
    "v_pk_mul_f32 v[56:57], v[56:57], v[90:91]\n\t"
    "v_pk_mul_f32 v[58:59], v[58:59], v[90:91]\n\t"
    "v_cvt_pk_bf16_f32 v84, v52, v53\n\t"
    "v_cvt_pk_bf16_f32 v85, v54, v55\n\t"
    "v_cvt_pk_bf16_f32 v86, v56, v57\n\t"
    "v_cvt_pk_bf16_f32 v87, v58, v59\n\t"
    "s_bitcmp1_b32 s34, 0\n\t"
    "s_cbranch_scc1 L60_%=\n\t"
    "s_mov_b32 exec_hi, 0\n\t"
    "buffer_store_dwordx4 v[84:87], v44, s[12:15], s62 offen sc1\n\t"
    "s_mov_b32 exec_lo, 0\n\t"
    "s_mov_b32 exec_hi, 0xffff0000\n\t"
    "v_mov_b32 v84, v88\n\t"
    "v_mov_b32 v85, v90\n\t"
    "s_mov_b32 exec_hi, -1\n\t"
    "buffer_store_dwordx2 v[84:85], v45, s[12:15], s63 offen sc1\n\t"
    "s_branch L61_%=\n\t"
    "L60_%=:\n\t"
    "ds_write_b128 v44, v[84:87] offset:17408\n\t"
    "ds_read_b64_tr_b16 v[64:65], v45 offset:17408\n\t"
    "ds_read_b64_tr_b16 v[66:67], v45 offset:17664\n\t"
    "v_max3_f32 v60, |v52|, |v53|, |v54|\n\t"
    "v_max3_f32 v61, |v56|, |v57|, |v58|\n\t"
    "v_max_f32 v60, |v55|, v60\n\t"
    "v_max_f32 v61, |v59|, v61\n\t"
    "v_mov_b32 v62, 0x5d800000\n\t"
    "v_cmp_lt_f32 vcc, v62, v60\n\t"
    "v_cmp_lt_f32 s[42:43], v62, v61\n\t"
    "s_and_b32 s50, vcc_hi, 0xffff\n\t"
    "s_or_b32 s50, s50, vcc_lo\n\t"
    "s_or_b32 s50, s50, s42\n\t"
    "s_cselect_b32 s63, 0x7f807f80, s63\n\t"
    "s_mov_b32 exec_lo, 0xfffff\n\t"
    "s_mov_b32 exec_hi, 0x3fffff\n\t"
    "s_waitcnt lgkmcnt(0)\n\t"
    "v_writelane_b32 v64, s63, 52\n\t"
    "v_writelane_b32 v65, s63, 52\n\t"
    "v_writelane_b32 v66, s63, 52\n\t"
    "v_writelane_b32 v67, s63, 52\n\t"
    "v_writelane_b32 v64, 0, 53\n\t"
    "v_writelane_b32 v65, 0, 53\n\t"
    "v_writelane_b32 v66, 0, 53\n\t"
    "v_writelane_b32 v67, 0, 53\n\t"
    "buffer_store_dwordx4 v[64:67], v46, s[12:15], s62 offen sc1\n\t"
    "L61_%=:\n\t"
    "s_mov_b64 exec, -1\n\t"
    "L59_%=:\n\t"
    "buffer_load_dwordx4 a[208:211], v1, s[8:11], s66 offen offset:2048 sc1\n\t"
    "s_nop 11\n\t"
    "buffer_load_dwordx4 a[212:215], v1, s[8:11], s66 offen offset:3072 sc1\n\t"
    "s_nop 11\n\t"
    "s_add_u32 s66, s66, 0x1000\n\t"
    "buffer_load_dwordx4 a[216:219], v1, s[8:11], s66 offen sc1\n\t"
    "s_nop 10\n\t"
    "buffer_load_dwordx4 a[108:111], v1, s[12:15], s55 offen sc1\n\t"
    "s_nop 11\n\t"
    "buffer_load_dwordx4 a[112:115], v1, s[12:15], s55 offen offset:1024 sc1\n\t"
    "s_nop 11\n\t"
    "buffer_load_dwordx4 a[116:119], v1, s[12:15], s55 offen offset:2048 sc1\n\t"
    "s_nop 11\n\t"
    "buffer_load_dwordx4 a[120:123], v1, s[12:15], s55 offen offset:3072 sc1\n\t"
    "s_nop 11\n\t"
    "s_add_u32 s55, s55, 0x1000\n\t"
    "buffer_load_dwordx4 a[124:127], v1, s[12:15], s55 offen sc1\n\t"
    "v_mfma_f32_16x16x32_bf16 a[0:3], a[24:27], a[72:75], 0\n\t"
    "v_mfma_f32_16x16x32_bf16 a[4:7], a[28:31], a[72:75], 0\n\t"
    "v_mfma_f32_16x16x32_bf16 a[8:11], a[32:35], a[72:75], 0\n\t"
    "v_mfma_f32_16x16x32_bf16 a[12:15], a[36:39], a[72:75], 0\n\t"
    "v_mfma_f32_16x16x32_bf16 a[16:19], a[40:43], a[72:75], 0\n\t"
    "buffer_load_dwordx4 a[128:131], v1, s[12:15], s56 offen sc1\n\t"
    "v_mfma_f32_16x16x32_bf16 a[0:3], a[44:47], a[92:95], a[0:3]\n\t"
    "v_mfma_f32_16x16x32_bf16 a[4:7], a[48:51], a[92:95], a[4:7]\n\t"
    "v_mfma_f32_16x16x32_bf16 a[8:11], a[52:55], a[92:95], a[8:11]\n\t"
    "v_mfma_f32_16x16x32_bf16 a[12:15], a[56:59], a[92:95], a[12:15]\n\t"
    "v_mfma_f32_16x16x32_bf16 a[16:19], a[60:63], a[92:95], a[16:19]\n\t"
    "buffer_load_dwordx4 a[132:135], v1, s[12:15], s56 offen offset:1024 sc1\n\t"
    "v_mfma_f32_16x16x32_bf16 a[0:3], a[64:67], a[96:99], a[0:3]\n\t"
    "v_mfma_f32_16x16x32_bf16 a[4:7], a[68:71], a[96:99], a[4:7]\n\t"
    "v_mfma_f32_16x16x32_bf16 a[8:11], a[168:171], a[96:99], a[8:11]\n\t"
    "v_mfma_f32_16x16x32_bf16 a[12:15], a[172:175], a[96:99], a[12:15]\n\t"
    "v_mfma_f32_16x16x32_bf16 a[16:19], a[176:179], a[96:99], a[16:19]\n\t"
    "buffer_load_dwordx4 a[136:139], v1, s[12:15], s56 offen offset:2048 sc1\n\t"
    "v_mfma_f32_16x16x32_bf16 a[0:3], a[180:183], a[100:103], a[0:3]\n\t"
    "v_mfma_f32_16x16x32_bf16 a[4:7], a[184:187], a[100:103], a[4:7]\n\t"
    "v_mfma_f32_16x16x32_bf16 a[8:11], a[188:191], a[100:103], a[8:11]\n\t"
    "s_waitcnt vmcnt(14)\n\t"
    "v_mfma_f32_16x16x32_bf16 a[12:15], a[192:195], a[100:103], a[12:15]\n\t"
    "s_waitcnt vmcnt(13)\n\t"
    "v_mfma_f32_16x16x32_bf16 a[16:19], a[196:199], a[100:103], a[16:19]\n\t"
    "buffer_load_dwordx4 a[140:143], v1, s[12:15], s56 offen offset:3072 sc1\n\t"
    "s_waitcnt vmcnt(13)\n\t"
    "v_mfma_f32_16x16x32_bf16 a[0:3], a[200:203], a[104:107], a[0:3]\n\t"
    "s_waitcnt vmcnt(12)\n\t"
    "v_mfma_f32_16x16x32_bf16 a[4:7], a[204:207], a[104:107], a[4:7]\n\t"
    "s_waitcnt vmcnt(11)\n\t"
    "v_mfma_f32_16x16x32_bf16 a[8:11], a[208:211], a[104:107], a[8:11]\n\t"
    "s_waitcnt vmcnt(10)\n\t"
    "v_mfma_f32_16x16x32_bf16 a[12:15], a[212:215], a[104:107], a[12:15]\n\t"
    "s_waitcnt vmcnt(9)\n\t"
    "v_mfma_f32_16x16x32_bf16 a[16:19], a[216:219], a[104:107], a[16:19]\n\t"
    "s_cmp_eq_u32 s33, 0\n\t"
    "s_cbranch_scc0 L62_%=\n\t"
    "s_mov_b64 exec, 0xff\n\t"
    "buffer_store_dwordx2 v[2:3], v50, s[12:15], s61 offen sc1\n\t"
    "s_mov_b64 exec, -1\n\t"
    "L62_%=:\n\t"
    "s_add_u32 s56, s56, 0x1000\n\t"
    "buffer_load_dwordx4 a[144:147], v1, s[12:15], s56 offen sc1\n\t"
    "s_waitcnt vmcnt(9)\n\t"
    "v_mfma_f32_16x16x32_bf16 a[20:23], a[24:27], a[108:111], 0\n\t"
    "v_mfma_f32_16x16x32_bf16 a[76:79], a[28:31], a[108:111], 0\n\t"
    "v_mfma_f32_16x16x32_bf16 a[80:83], a[32:35], a[108:111], 0\n\t"
    "ds_write_b128 v31, a[0:3] offset:15360\n\t"
    "v_mfma_f32_16x16x32_bf16 a[84:87], a[36:39], a[108:111], 0\n\t"
    "v_mfma_f32_16x16x32_bf16 a[88:91], a[40:43], a[108:111], 0\n\t"
    "buffer_load_dwordx4 a[148:151], v1, s[12:15], s57 offen sc1\n\t"
    "s_waitcnt vmcnt(9)\n\t"
    "v_mfma_f32_16x16x32_bf16 a[20:23], a[44:47], a[112:115], a[20:23]\n\t"
    "ds_write_b128 v31, a[4:7] offset:16384\n\t"
    "v_mfma_f32_16x16x32_bf16 a[76:79], a[48:51], a[112:115], a[76:79]\n\t"
    "v_mfma_f32_16x16x32_bf16 a[80:83], a[52:55], a[112:115], a[80:83]\n\t"
    "v_mfma_f32_16x16x32_bf16 a[84:87], a[56:59], a[112:115], a[84:87]\n\t"
    "ds_write_b128 v31, a[8:11] offset:17408\n\t"
    "v_mfma_f32_16x16x32_bf16 a[88:91], a[60:63], a[112:115], a[88:91]\n\t"
    "buffer_load_dwordx4 a[152:155], v1, s[12:15], s57 offen offset:1024 sc1\n\t"
    "s_waitcnt vmcnt(9)\n\t"
    "v_mfma_f32_16x16x32_bf16 a[20:23], a[64:67], a[116:119], a[20:23]\n\t"
    "v_mfma_f32_16x16x32_bf16 a[76:79], a[68:71], a[116:119], a[76:79]\n\t"
    "ds_write_b128 v31, a[12:15] offset:18432\n\t"
    "v_mfma_f32_16x16x32_bf16 a[80:83], a[168:171], a[116:119], a[80:83]\n\t"
    "v_mfma_f32_16x16x32_bf16 a[84:87], a[172:175], a[116:119], a[84:87]\n\t"
    "v_mfma_f32_16x16x32_bf16 a[88:91], a[176:179], a[116:119], a[88:91]\n\t"
    "buffer_load_dwordx4 a[156:159], v1, s[12:15], s57 offen offset:2048 sc1\n\t"
    "s_waitcnt vmcnt(9)\n\t"
    "v_mfma_f32_16x16x32_bf16 a[20:23], a[180:183], a[120:123], a[20:23]\n\t"
    "ds_write_b128 v31, a[16:19] offset:19456\n\t"
    "v_mfma_f32_16x16x32_bf16 a[76:79], a[184:187], a[120:123], a[76:79]\n\t"
    "v_mfma_f32_16x16x32_bf16 a[80:83], a[188:191], a[120:123], a[80:83]\n\t"
    "v_mfma_f32_16x16x32_bf16 a[84:87], a[192:195], a[120:123], a[84:87]\n\t"
    "v_mfma_f32_16x16x32_bf16 a[88:91], a[196:199], a[120:123], a[88:91]\n\t"
    "buffer_load_dwordx4 a[160:163], v1, s[12:15], s57 offen offset:3072 sc1\n\t"
    "s_waitcnt vmcnt(9)\n\t"
    "v_mfma_f32_16x16x32_bf16 a[20:23], a[200:203], a[124:127], a[20:23]\n\t"
    "v_mfma_f32_16x16x32_bf16 a[76:79], a[204:207], a[124:127], a[76:79]\n\t"
    "v_mfma_f32_16x16x32_bf16 a[80:83], a[208:211], a[124:127], a[80:83]\n\t"
    "v_mfma_f32_16x16x32_bf16 a[84:87], a[212:215], a[124:127], a[84:87]\n\t"
    "v_mfma_f32_16x16x32_bf16 a[88:91], a[216:219], a[124:127], a[88:91]\n\t"
    "s_add_u32 s57, s57, 0x1000\n\t"
    "buffer_load_dwordx4 a[164:167], v1, s[12:15], s57 offen sc1\n\t"
    "s_waitcnt vmcnt(9)\n\t"
    "v_mfma_f32_16x16x32_bf16 a[0:3], a[24:27], a[128:131], 0\n\t"
    "v_mfma_f32_16x16x32_bf16 a[4:7], a[28:31], a[128:131], 0\n\t"
    "v_mfma_f32_16x16x32_bf16 a[8:11], a[32:35], a[128:131], 0\n\t"
    "ds_write_b128 v31, a[20:23]\n\t"
    "v_mfma_f32_16x16x32_bf16 a[12:15], a[36:39], a[128:131], 0\n\t"
    "v_mfma_f32_16x16x32_bf16 a[16:19], a[40:43], a[128:131], 0\n\t"
    "s_waitcnt vmcnt(8)\n\t"
    "v_mfma_f32_16x16x32_bf16 a[0:3], a[44:47], a[132:135], a[0:3]\n\t"
    "ds_write_b128 v31, a[76:79] offset:1024\n\t"
    "v_mfma_f32_16x16x32_bf16 a[4:7], a[48:51], a[132:135], a[4:7]\n\t"
    "v_mfma_f32_16x16x32_bf16 a[8:11], a[52:55], a[132:135], a[8:11]\n\t"
    "v_mfma_f32_16x16x32_bf16 a[12:15], a[56:59], a[132:135], a[12:15]\n\t"
    "ds_write_b128 v31, a[80:83] offset:2048\n\t"
    "v_mfma_f32_16x16x32_bf16 a[16:19], a[60:63], a[132:135], a[16:19]\n\t"
    "s_waitcnt vmcnt(7)\n\t"
    "v_mfma_f32_16x16x32_bf16 a[0:3], a[64:67], a[136:139], a[0:3]\n\t"
    "v_mfma_f32_16x16x32_bf16 a[4:7], a[68:71], a[136:139], a[4:7]\n\t"
    "ds_write_b128 v31, a[84:87] offset:3072\n\t"
    "v_mfma_f32_16x16x32_bf16 a[8:11], a[168:171], a[136:139], a[8:11]\n\t"
    "v_mfma_f32_16x16x32_bf16 a[12:15], a[172:175], a[136:139], a[12:15]\n\t"
    "v_mfma_f32_16x16x32_bf16 a[16:19], a[176:179], a[136:139], a[16:19]\n\t"
    "ds_write_b128 v31, a[88:91] offset:4096\n\t"
    "s_waitcnt vmcnt(6)\n\t"
    "v_mfma_f32_16x16x32_bf16 a[0:3], a[180:183], a[140:143], a[0:3]\n\t"
    "v_mfma_f32_16x16x32_bf16 a[4:7], a[184:187], a[140:143], a[4:7]\n\t"
    "v_mfma_f32_16x16x32_bf16 a[8:11], a[188:191], a[140:143], a[8:11]\n\t"
    "v_mfma_f32_16x16x32_bf16 a[12:15], a[192:195], a[140:143], a[12:15]\n\t"
    "v_mfma_f32_16x16x32_bf16 a[16:19], a[196:199], a[140:143], a[16:19]\n\t"
    "s_waitcnt vmcnt(5)\n\t"
    "v_mfma_f32_16x16x32_bf16 a[0:3], a[200:203], a[144:147], a[0:3]\n\t"
    "v_mfma_f32_16x16x32_bf16 a[4:7], a[204:207], a[144:147], a[4:7]\n\t"
    "v_mfma_f32_16x16x32_bf16 a[8:11], a[208:211], a[144:147], a[8:11]\n\t"
    "v_mfma_f32_16x16x32_bf16 a[12:15], a[212:215], a[144:147], a[12:15]\n\t"
    "v_mfma_f32_16x16x32_bf16 a[16:19], a[216:219], a[144:147], a[16:19]\n\t"
    "s_waitcnt vmcnt(4)\n\t"
    "v_mfma_f32_16x16x32_bf16 a[20:23], a[24:27], a[148:151], 0\n\t"
    "v_mfma_f32_16x16x32_bf16 a[76:79], a[28:31], a[148:151], 0\n\t"
    "v_mfma_f32_16x16x32_bf16 a[80:83], a[32:35], a[148:151], 0\n\t"
    "ds_write_b128 v31, a[0:3] offset:5120\n\t"
    "v_mfma_f32_16x16x32_bf16 a[84:87], a[36:39], a[148:151], 0\n\t"
    "v_mfma_f32_16x16x32_bf16 a[88:91], a[40:43], a[148:151], 0\n\t"
    "s_waitcnt vmcnt(3)\n\t"
    "v_mfma_f32_16x16x32_bf16 a[20:23], a[44:47], a[152:155], a[20:23]\n\t"
    "ds_write_b128 v31, a[4:7] offset:6144\n\t"
    "v_mfma_f32_16x16x32_bf16 a[76:79], a[48:51], a[152:155], a[76:79]\n\t"
    "v_mfma_f32_16x16x32_bf16 a[80:83], a[52:55], a[152:155], a[80:83]\n\t"
    "v_mfma_f32_16x16x32_bf16 a[84:87], a[56:59], a[152:155], a[84:87]\n\t"
    "ds_write_b128 v31, a[8:11] offset:7168\n\t"
    "v_mfma_f32_16x16x32_bf16 a[88:91], a[60:63], a[152:155], a[88:91]\n\t"
    "s_waitcnt vmcnt(2)\n\t"
    "v_mfma_f32_16x16x32_bf16 a[20:23], a[64:67], a[156:159], a[20:23]\n\t"
    "v_mfma_f32_16x16x32_bf16 a[76:79], a[68:71], a[156:159], a[76:79]\n\t"
    "ds_write_b128 v31, a[12:15] offset:8192\n\t"
    "v_mfma_f32_16x16x32_bf16 a[80:83], a[168:171], a[156:159], a[80:83]\n\t"
    "v_mfma_f32_16x16x32_bf16 a[84:87], a[172:175], a[156:159], a[84:87]\n\t"
    "v_mfma_f32_16x16x32_bf16 a[88:91], a[176:179], a[156:159], a[88:91]\n\t"
    "ds_write_b128 v31, a[16:19] offset:9216\n\t"
    "s_waitcnt vmcnt(1)\n\t"
    "v_mfma_f32_16x16x32_bf16 a[20:23], a[180:183], a[160:163], a[20:23]\n\t"
    "v_mfma_f32_16x16x32_bf16 a[76:79], a[184:187], a[160:163], a[76:79]\n\t"
    "v_mfma_f32_16x16x32_bf16 a[80:83], a[188:191], a[160:163], a[80:83]\n\t"
    "v_mfma_f32_16x16x32_bf16 a[84:87], a[192:195], a[160:163], a[84:87]\n\t"
    "v_mfma_f32_16x16x32_bf16 a[88:91], a[196:199], a[160:163], a[88:91]\n\t"
    "s_waitcnt vmcnt(0)\n\t"
    "v_mfma_f32_16x16x32_bf16 a[20:23], a[200:203], a[164:167], a[20:23]\n\t"
    "v_mfma_f32_16x16x32_bf16 a[76:79], a[204:207], a[164:167], a[76:79]\n\t"
    "v_mfma_f32_16x16x32_bf16 a[80:83], a[208:211], a[164:167], a[80:83]\n\t"
    "v_mfma_f32_16x16x32_bf16 a[84:87], a[212:215], a[164:167], a[84:87]\n\t"
    "v_mfma_f32_16x16x32_bf16 a[88:91], a[216:219], a[164:167], a[88:91]\n\t"
    "v_lshrrev_b32 v40, 5, v0\n\t"
    "v_lshlrev_b32 v40, 5, v40\n\t"
    "v_mov_b32 v41, 64\n\t"
    "s_mul_i32 s51, s39, 0x200\n\t"
    "s_mul_i32 s53, s33, 0x80\n\t"
    "s_add_u32 s51, s51, s53\n\t"
    "s_add_u32 s51, s51, 0xe8000\n\t"
    "buffer_load_dwordx4 v[220:223], v40, s[12:15], s51 offen sc1\n\t"
    "buffer_load_dwordx4 v[224:227], v40, s[12:15], s51 offen offset:16 sc1\n\t"
    "buffer_load_dwordx4 v[228:231], v41, s[12:15], s51 offen sc1\n\t"
    "s_mul_i32 s55, s33, 0xc00\n\t"
    "s_add_u32 s55, s55, s45\n\t"
    "s_mul_i32 s54, s33, 0x1000\n\t"
    "s_add_u32 s54, s54, s45\n\t"
    "s_add_u32 s54, s54, 0x3000\n\t"
    "ds_write_b128 v31, a[20:23] offset:10240\n\t"
    "ds_write_b128 v31, a[76:79] offset:11264\n\t"
    "ds_write_b128 v31, a[80:83] offset:12288\n\t"
    "ds_write_b128 v31, a[84:87] offset:13312\n\t"
    "ds_write_b128 v31, a[88:91] offset:14336\n\t"
    "s_mul_i32 m0, s33, 0xc00\n\t"
    "s_mul_i32 s57, s36, 0x4000\n\t"
    "s_mul_i32 s53, s35, 0x200\n\t"
    "s_add_u32 s57, s57, s53\n\t"
    "s_add_u32 s57, s57, 0xe56000\n\t"
    "s_waitcnt lgkmcnt(0)\n\t"
    "s_and_b32 s50, s39, 7\n\t"
    "s_mul_i32 s60, s36, 0x4000\n\t"
    "s_mul_i32 s53, s50, 0x800\n\t"
    "s_add_u32 s60, s60, s53\n\t"
    "s_add_u32 s60, s60, 0x10000\n\t"
    "s_cmp_eq_u32 s33, 0\n\t"
    "s_cbranch_scc0 L63_%=\n\t"
    "buffer_load_dwordx4 v[32:35], v1, s[12:15], s60 offen sc1\n\t"
    "buffer_load_dwordx4 v[36:39], v1, s[12:15], s60 offen offset:1024 sc1\n\t"
    "L63_%=:\n\t"
    "s_cmp_eq_u32 s33, 0\n\t"
    "s_cbranch_scc0 L64_%=\n\t"
    "s_waitcnt vmcnt(0)\n\t"
    "v_cmp_ne_u64 vcc, v[2:3], v[32:33]\n\t"
    "v_cmp_ne_u64 s[42:43], v[2:3], v[34:35]\n\t"
    "s_nop 0\n\t"
    "s_or_b64 vcc, vcc, s[42:43]\n\t"
    "v_cmp_ne_u64 s[42:43], v[2:3], v[36:37]\n\t"
    "s_nop 0\n\t"
    "s_or_b64 vcc, vcc, s[42:43]\n\t"
    "v_cmp_ne_u64 s[42:43], v[2:3], v[38:39]\n\t"
    "s_nop 0\n\t"
    "s_or_b64 vcc, vcc, s[42:43]\n\t"
    "s_cbranch_vccz L65_%=\n\t"
    "poll66_%=:\n\t"
    "s_sleep 2\n\t"
    "buffer_load_dwordx4 v[32:35], v1, s[12:15], s60 offen sc1\n\t"
    "buffer_load_dwordx4 v[36:39], v1, s[12:15], s60 offen offset:1024 sc1\n\t"
    "s_waitcnt vmcnt(0)\n\t"
    "v_cmp_ne_u64 vcc, v[2:3], v[32:33]\n\t"
    "v_cmp_ne_u64 s[42:43], v[2:3], v[34:35]\n\t"
    "s_nop 0\n\t"
    "s_or_b64 vcc, vcc, s[42:43]\n\t"
    "v_cmp_ne_u64 s[42:43], v[2:3], v[36:37]\n\t"
    "s_nop 0\n\t"
    "s_or_b64 vcc, vcc, s[42:43]\n\t"
    "v_cmp_ne_u64 s[42:43], v[2:3], v[38:39]\n\t"
    "s_nop 0\n\t"
    "s_or_b64 vcc, vcc, s[42:43]\n\t"
    "s_cbranch_vccz L65_%=\n\t"
    "s_sub_u32 s37, s37, 1\n\t"
    "s_cbranch_scc0 poll66_%=\n\t"
    "s_mov_b32 s37, 0\n\t"
    "L65_%=:\n\t"
    "L64_%=:\n\t"
    "s_barrier\n\t"
    "buffer_load_dwordx2 v[232:233], v51, s[12:15], s57 offen sc1\n\t"
    "ds_read_b128 v[200:203], v47\n\t"
    "ds_read_b128 v[204:207], v47 offset:256\n\t"
    "ds_read_b128 v[208:211], v48\n\t"
    "buffer_load_dwordx4 v1, s[12:15], s55 offen sc1 lds\n\t"
    "ds_read_b128 v[212:215], v48 offset:256\n\t"
    "ds_read_b128 v[216:219], v49\n\t"
    "ds_read_b128 v[32:35], v47 offset:20480\n\t"
    "buffer_load_dwordx4 v1, s[12:15], s55 offen offset:1024 sc1 lds\n\t"
    "ds_read_b128 v[36:39], v47 offset:20736\n\t"
    "ds_read_b128 v[52:55], v48 offset:20480\n\t"
    "ds_read_b128 v[56:59], v48 offset:20736\n\t"
    "buffer_load_dwordx4 v1, s[12:15], s55 offen offset:2048 sc1 lds\n\t"
    "ds_read_b128 v[60:63], v49 offset:20480\n\t"
    "ds_read_b128 v[64:67], v47 offset:40960\n\t"
    "ds_read_b128 v[68:71], v47 offset:41216\n\t"
    "s_lshl_b32 m0, s33, 12\n\t"
    "s_add_u32 m0, m0, 0x3000\n\t"
    "s_nop 0\n\t"
    "buffer_load_dwordx4 v1, s[12:15], s54 offen sc1 lds\n\t"
    "ds_read_b128 v[72:75], v48 offset:40960\n\t"
    "ds_read_b128 v[76:79], v48 offset:41216\n\t"
    "ds_read_b128 v[80:83], v49 offset:40960\n\t"
    "buffer_load_dwordx4 v1, s[12:15], s54 offen offset:1024 sc1 lds\n\t"
    "ds_read_b128 v[84:87], v47 offset:61440\n\t"
    "ds_read_b128 v[88:91], v47 offset:61696\n\t"
    "ds_read_b128 v[92:95], v48 offset:61440\n\t"
    "buffer_load_dwordx4 v1, s[12:15], s54 offen offset:2048 sc1 lds\n\t"
    "ds_read_b128 v[96:99], v48 offset:61696\n\t"
    "ds_read_b128 v[100:103], v49 offset:61440\n\t"
    "s_waitcnt lgkmcnt(10)\n\t"
    "v_pk_add_f32 v[200:201], v[200:201], v[32:33]\n\t"
    "v_pk_add_f32 v[202:203], v[202:203], v[34:35]\n\t"
    "v_pk_add_f32 v[204:205], v[204:205], v[36:37]\n\t"
    "buffer_load_dwordx4 v1, s[12:15], s54 offen offset:3072 sc1 lds\n\t"
    "v_pk_add_f32 v[206:207], v[206:207], v[38:39]\n\t"
    "v_pk_add_f32 v[208:209], v[208:209], v[52:53]\n\t"
    "v_pk_add_f32 v[210:211], v[210:211], v[54:55]\n\t"
    "v_pk_add_f32 v[212:213], v[212:213], v[56:57]\n\t"
    "v_pk_add_f32 v[214:215], v[214:215], v[58:59]\n\t"
    "v_pk_add_f32 v[216:217], v[216:217], v[60:61]\n\t"
    "v_pk_add_f32 v[218:219], v[218:219], v[62:63]\n\t"
    "s_waitcnt lgkmcnt(5)\n\t"
    "v_pk_add_f32 v[200:201], v[200:201], v[64:65]\n\t"
    "v_pk_add_f32 v[202:203], v[202:203], v[66:67]\n\t"
    "v_pk_add_f32 v[204:205], v[204:205], v[68:69]\n\t"
    "v_pk_add_f32 v[206:207], v[206:207], v[70:71]\n\t"
    "s_add_u32 m0, m0, 0x4000\n\t"
    "s_add_u32 s54, s54, 0x4000\n\t"
    "buffer_load_dwordx4 v1, s[12:15], s54 offen sc1 lds\n\t"
    "v_pk_add_f32 v[208:209], v[208:209], v[72:73]\n\t"
    "v_pk_add_f32 v[210:211], v[210:211], v[74:75]\n\t"
    "v_pk_add_f32 v[212:213], v[212:213], v[76:77]\n\t"
    "v_pk_add_f32 v[214:215], v[214:215], v[78:79]\n\t"
    "v_pk_add_f32 v[216:217], v[216:217], v[80:81]\n\t"
    "v_pk_add_f32 v[218:219], v[218:219], v[82:83]\n\t"
    "s_waitcnt lgkmcnt(0)\n\t"
    "v_pk_add_f32 v[200:201], v[200:201], v[84:85]\n\t"
    "v_pk_add_f32 v[202:203], v[202:203], v[86:87]\n\t"
    "v_pk_add_f32 v[204:205], v[204:205], v[88:89]\n\t"
    "buffer_load_dwordx4 v1, s[12:15], s54 offen offset:1024 sc1 lds\n\t"
    "v_pk_add_f32 v[206:207], v[206:207], v[90:91]\n\t"
    "v_pk_add_f32 v[208:209], v[208:209], v[92:93]\n\t"
    "v_pk_add_f32 v[210:211], v[210:211], v[94:95]\n\t"
    "v_pk_add_f32 v[212:213], v[212:213], v[96:97]\n\t"
    "v_pk_add_f32 v[214:215], v[214:215], v[98:99]\n\t"
    "v_pk_add_f32 v[216:217], v[216:217], v[100:101]\n\t"
    "v_pk_add_f32 v[218:219], v[218:219], v[102:103]\n\t"
    "s_nop 0\n\t"
    "buffer_load_dwordx4 v1, s[12:15], s54 offen offset:2048 sc1 lds\n\t"
    "s_nop 0\n\t"
    "buffer_load_dwordx4 v1, s[12:15], s54 offen offset:3072 sc1 lds\n\t"
    "s_mov_b32 s10, 0\n\t"
    "s_waitcnt vmcnt(11)\n\t"
    "v_mul_f32 v234, 0x3ea52b5c, v233\n\t"
    "v_mov_b32 v233, v232\n\t"
    "v_mov_b32 v236, v232\n\t"
    "v_mov_b32 v240, v232\n\t"
    "v_mov_b32 v238, v234\n\t"
    "v_mov_b32 v242, v234\n\t"
    "v_mov_b32 v235, v234\n\t"
    "v_permlane32_swap_b32 v236, v240\n\t"
    "v_permlane32_swap_b32 v238, v242\n\t"
    "v_mov_b32 v237, v236\n\t"
    "v_mov_b32 v241, v240\n\t"
    "v_mov_b32 v239, v238\n\t"
    "v_mov_b32 v243, v242\n\t"
    "s_waitcnt vmcnt(8)\n\t"
    "s_barrier\n\t"
    "ds_read_b128 v[96:99], v125\n\t"
    "ds_read_b64 v[120:121], v126 offset:2048\n\t"
    "ds_read_b128 v[100:103], v125 offset:1024\n\t"
    "ds_read_b128 v[104:107], v127 offset:2560\n\t"
    "ds_read_b128 v[112:115], v127 offset:3232\n\t"
    "v_pk_fma_f32 v[200:201], v[236:237], v[220:221], v[200:201]\n\t"
    "v_pk_fma_f32 v[202:203], v[236:237], v[222:223], v[202:203]\n\t"
    "v_pk_fma_f32 v[204:205], v[236:237], v[224:225], v[204:205]\n\t"
    "v_pk_fma_f32 v[206:207], v[236:237], v[226:227], v[206:207]\n\t"
    "v_pk_fma_f32 v[208:209], v[240:241], v[220:221], v[208:209]\n\t"
    "v_pk_fma_f32 v[210:211], v[240:241], v[222:223], v[210:211]\n\t"
    "v_pk_fma_f32 v[212:213], v[240:241], v[224:225], v[212:213]\n\t"
    "v_pk_fma_f32 v[214:215], v[240:241], v[226:227], v[214:215]\n\t"
    "v_pk_fma_f32 v[216:217], v[232:233], v[228:229], v[216:217]\n\t"
    "v_pk_fma_f32 v[218:219], v[232:233], v[230:231], v[218:219]\n\t"
    "v_pk_mul_f32 v[200:201], v[200:201], v[238:239]\n\t"
    "v_pk_mul_f32 v[202:203], v[202:203], v[238:239]\n\t"
    "v_pk_mul_f32 v[204:205], v[204:205], v[238:239]\n\t"
    "v_pk_mul_f32 v[206:207], v[206:207], v[238:239]\n\t"
    "v_pk_mul_f32 v[208:209], v[208:209], v[242:243]\n\t"
    "v_pk_mul_f32 v[210:211], v[210:211], v[242:243]\n\t"
    "v_pk_mul_f32 v[212:213], v[212:213], v[242:243]\n\t"
    "v_pk_mul_f32 v[214:215], v[214:215], v[242:243]\n\t"
    "v_pk_mul_f32 v[216:217], v[216:217], v[234:235]\n\t"
    "v_pk_mul_f32 v[218:219], v[218:219], v[234:235]\n\t"
    "v_cvt_pk_bf16_f32 v16, v200, v201\n\t"
    "v_cvt_pk_bf16_f32 v17, v202, v203\n\t"
    "v_cvt_pk_bf16_f32 v18, v204, v205\n\t"
    "v_cvt_pk_bf16_f32 v19, v206, v207\n\t"
    "v_cvt_pk_bf16_f32 v20, v208, v209\n\t"
    "v_cvt_pk_bf16_f32 v21, v210, v211\n\t"
    "v_cvt_pk_bf16_f32 v22, v212, v213\n\t"
    "v_cvt_pk_bf16_f32 v23, v214, v215\n\t"
    "v_cvt_pk_bf16_f32 v24, v216, v217\n\t"
    "v_cvt_pk_bf16_f32 v25, v218, v219\n\t"
    "s_waitcnt lgkmcnt(4)\n\t"
    "v_mfma_f32_32x32x16_bf16 v[32:47], v[96:99], v[16:19], 0\n\t"
    "v_mfma_f32_32x32x16_bf16 v[48:63], v[96:99], v[20:23], 0\n\t"
    "s_add_u32 m0, m0, 0x4000\n\t"
    "s_add_u32 s54, s54, 0x4000\n\t"
    "buffer_load_dwordx4 v1, s[12:15], s54 offen sc1 lds\n\t"
    "s_waitcnt lgkmcnt(3)\n\t"
    "s_nop 5\n\t"
    "v_mfma_f32_32x32x4_2b_bf16 v[32:63], v[120:121], v[24:25], v[32:63] cbsz:1 abid:0\n\t"
    "buffer_load_dwordx4 v1, s[12:15], s54 offen offset:1024 sc1 lds\n\t"
    "s_nop 15\n\t"
    "s_nop 2\n\t"
    "v_exp_f32 v32, v32\n\t"
    "v_exp_f32 v33, v33\n\t"
    "s_waitcnt lgkmcnt(0)\n\t"
    "ds_read_b128 v[96:99], v125 offset:5248\n\t"
    "v_exp_f32 v34, v34\n\t"
    "v_exp_f32 v35, v35\n\t"
    "v_exp_f32 v36, v36\n\t"
    "v_exp_f32 v37, v37\n\t"
    "v_mfma_f32_32x32x4_2b_bf16 v[64:95], v[120:121], v[24:25], 0 cbsz:1 abid:1\n\t"
    "ds_read_b128 v[108:111], v127 offset:3904\n\t"
    "v_exp_f32 v38, v38\n\t"
    "v_exp_f32 v39, v39\n\t"
    "v_exp_f32 v40, v40\n\t"
    "v_exp_f32 v41, v41\n\t"
    "v_exp_f32 v42, v42\n\t"
    "v_exp_f32 v43, v43\n\t"
    "v_exp_f32 v44, v44\n\t"
    "v_exp_f32 v45, v45\n\t"
    "v_smfmac_f32_32x32x32_bf16 v[128:143], v[104:107], v[32:39], v124\n\t"
    "ds_read_b128 v[116:119], v127 offset:4576\n\t"
    "buffer_load_dwordx4 v1, s[12:15], s54 offen offset:2048 sc1 lds\n\t"
    "v_exp_f32 v46, v46\n\t"
    "v_exp_f32 v47, v47\n\t"
    "v_exp_f32 v48, v48\n\t"
    "v_exp_f32 v49, v49\n\t"
    "v_smfmac_f32_32x32x32_bf16 v[128:143], v[112:115], v[40:47], v124\n\t"
    "ds_read_b64 v[122:123], v126 offset:7296\n\t"
    "v_exp_f32 v50, v50\n\t"
    "v_exp_f32 v51, v51\n\t"
    "v_exp_f32 v52, v52\n\t"
    "v_exp_f32 v53, v53\n\t"
    "v_mfma_f32_32x32x16_bf16 v[64:79], v[100:103], v[16:19], v[64:79]\n\t"
    "buffer_load_dwordx4 v1, s[12:15], s54 offen offset:3072 sc1 lds\n\t"
    "v_exp_f32 v54, v54\n\t"
    "v_exp_f32 v55, v55\n\t"
    "v_exp_f32 v56, v56\n\t"
    "v_exp_f32 v57, v57\n\t"
    "v_smfmac_f32_32x32x32_bf16 v[144:159], v[104:107], v[48:55], v124\n\t"
    "v_exp_f32 v58, v58\n\t"
    "v_exp_f32 v59, v59\n\t"
    "v_exp_f32 v60, v60\n\t"
    "v_exp_f32 v61, v61\n\t"
    "v_mfma_f32_32x32x16_bf16 v[80:95], v[100:103], v[20:23], v[80:95]\n\t"
    "s_add_u32 m0, m0, 0x4000\n\t"
    "s_add_u32 s54, s54, 0x4000\n\t"
    "buffer_load_dwordx4 v1, s[12:15], s54 offen sc1 lds\n\t"
    "v_exp_f32 v62, v62\n\t"
    "v_exp_f32 v63, v63\n\t"
    "v_exp_f32 v64, v64\n\t"
    "v_exp_f32 v65, v65\n\t"
    "v_smfmac_f32_32x32x32_bf16 v[144:159], v[112:115], v[56:63], v124\n\t"
    "s_waitcnt lgkmcnt(0)\n\t"
    "ds_read_b128 v[100:103], v125 offset:6272\n\t"
    "v_exp_f32 v66, v66\n\t"
    "v_exp_f32 v67, v67\n\t"
    "v_exp_f32 v68, v68\n\t"
    "v_exp_f32 v69, v69\n\t"
    "v_mfma_f32_32x32x4_2b_bf16 v[32:63], v[122:123], v[24:25], 0 cbsz:1 abid:0\n\t"
    "ds_read_b128 v[104:107], v127 offset:7808\n\t"
    "v_exp_f32 v70, v70\n\t"
    "v_exp_f32 v71, v71\n\t"
    "v_exp_f32 v72, v72\n\t"
    "v_exp_f32 v73, v73\n\t"
    "v_exp_f32 v74, v74\n\t"
    "v_exp_f32 v75, v75\n\t"
    "v_exp_f32 v76, v76\n\t"
    "v_exp_f32 v77, v77\n\t"
    "v_smfmac_f32_32x32x32_bf16 v[128:143], v[108:111], v[64:71], v124\n\t"
    "ds_read_b128 v[112:115], v127 offset:8480\n\t"
    "buffer_load_dwordx4 v1, s[12:15], s54 offen offset:1024 sc1 lds\n\t"
    "v_exp_f32 v78, v78\n\t"
    "v_exp_f32 v79, v79\n\t"
    "v_exp_f32 v80, v80\n\t"
    "v_exp_f32 v81, v81\n\t"
    "v_smfmac_f32_32x32x32_bf16 v[128:143], v[116:119], v[72:79], v124\n\t"
    "v_exp_f32 v82, v82\n\t"
    "v_exp_f32 v83, v83\n\t"
    "v_exp_f32 v84, v84\n\t"
    "v_exp_f32 v85, v85\n\t"
    "v_mfma_f32_32x32x16_bf16 v[32:47], v[96:99], v[16:19], v[32:47]\n\t"
    "buffer_load_dwordx4 v1, s[12:15], s54 offen offset:2048 sc1 lds\n\t"
    "v_exp_f32 v86, v86\n\t"
    "v_exp_f32 v87, v87\n\t"
    "v_exp_f32 v88, v88\n\t"
    "v_exp_f32 v89, v89\n\t"
    "v_smfmac_f32_32x32x32_bf16 v[144:159], v[108:111], v[80:87], v124\n\t"
    "v_exp_f32 v90, v90\n\t"
    "v_exp_f32 v91, v91\n\t"
    "v_exp_f32 v92, v92\n\t"
    "v_exp_f32 v93, v93\n\t"
    "v_mfma_f32_32x32x16_bf16 v[48:63], v[96:99], v[20:23], v[48:63]\n\t"
    "buffer_load_dwordx4 v1, s[12:15], s54 offen offset:3072 sc1 lds\n\t"
    "v_exp_f32 v94, v94\n\t"
    "v_exp_f32 v95, v95\n\t"
    "s_waitcnt vmcnt(8)\n\t"
    "s_barrier\n\t"
    "v_exp_f32 v32, v32\n\t"
    "v_exp_f32 v33, v33\n\t"
    "v_smfmac_f32_32x32x32_bf16 v[144:159], v[116:119], v[88:95], v124\n\t"
    "s_waitcnt lgkmcnt(0)\n\t"
    "ds_read_b128 v[96:99], v125 offset:10496\n\t"
    "v_exp_f32 v34, v34\n\t"
    "v_exp_f32 v35, v35\n\t"
    "v_exp_f32 v36, v36\n\t"
    "v_exp_f32 v37, v37\n\t"
    "v_mfma_f32_32x32x4_2b_bf16 v[64:95], v[122:123], v[24:25], 0 cbsz:1 abid:1\n\t"
    "ds_read_b128 v[108:111], v127 offset:9152\n\t"
    "v_exp_f32 v38, v38\n\t"
    "v_exp_f32 v39, v39\n\t"
    "v_exp_f32 v40, v40\n\t"
    "v_exp_f32 v41, v41\n\t"
    "v_exp_f32 v42, v42\n\t"
    "v_exp_f32 v43, v43\n\t"
    "v_exp_f32 v44, v44\n\t"
    "v_exp_f32 v45, v45\n\t"
    "v_smfmac_f32_32x32x32_bf16 v[128:143], v[104:107], v[32:39], v124\n\t"
    "ds_read_b128 v[116:119], v127 offset:9824\n\t"
    "s_add_u32 m0, m0, 0x4000\n\t"
    "s_add_u32 s54, s54, 0x4000\n\t"
    "buffer_load_dwordx4 v1, s[12:15], s54 offen sc1 lds\n\t"
    "v_exp_f32 v46, v46\n\t"
    "v_exp_f32 v47, v47\n\t"
    "v_exp_f32 v48, v48\n\t"
    "v_exp_f32 v49, v49\n\t"
    "v_smfmac_f32_32x32x32_bf16 v[128:143], v[112:115], v[40:47], v124\n\t"
    "ds_read_b64 v[120:121], v126 offset:12544\n\t"
    "v_exp_f32 v50, v50\n\t"
    "v_exp_f32 v51, v51\n\t"
    "v_exp_f32 v52, v52\n\t"
    "v_exp_f32 v53, v53\n\t"
    "v_mfma_f32_32x32x16_bf16 v[64:79], v[100:103], v[16:19], v[64:79]\n\t"
    "buffer_load_dwordx4 v1, s[12:15], s54 offen offset:1024 sc1 lds\n\t"
    "v_exp_f32 v54, v54\n\t"
    "v_exp_f32 v55, v55\n\t"
    "v_exp_f32 v56, v56\n\t"
    "v_exp_f32 v57, v57\n\t"
    "v_smfmac_f32_32x32x32_bf16 v[144:159], v[104:107], v[48:55], v124\n\t"
    "v_exp_f32 v58, v58\n\t"
    "v_exp_f32 v59, v59\n\t"
    "v_exp_f32 v60, v60\n\t"
    "v_exp_f32 v61, v61\n\t"
    "v_mfma_f32_32x32x16_bf16 v[80:95], v[100:103], v[20:23], v[80:95]\n\t"
    "buffer_load_dwordx4 v1, s[12:15], s54 offen offset:2048 sc1 lds\n\t"
    "v_exp_f32 v62, v62\n\t"
    "v_exp_f32 v63, v63\n\t"
    "v_exp_f32 v64, v64\n\t"
    "v_exp_f32 v65, v65\n\t"
    "v_smfmac_f32_32x32x32_bf16 v[144:159], v[112:115], v[56:63], v124\n\t"
    "s_waitcnt lgkmcnt(0)\n\t"
    "ds_read_b128 v[100:103], v125 offset:11520\n\t"
    "v_exp_f32 v66, v66\n\t"
    "v_exp_f32 v67, v67\n\t"
    "v_exp_f32 v68, v68\n\t"
    "v_exp_f32 v69, v69\n\t"
    "v_mfma_f32_32x32x4_2b_bf16 v[32:63], v[120:121], v[24:25], 0 cbsz:1 abid:0\n\t"
    "ds_read_b128 v[104:107], v127 offset:13056\n\t"
    "v_exp_f32 v70, v70\n\t"
    "v_exp_f32 v71, v71\n\t"
    "v_exp_f32 v72, v72\n\t"
    "v_exp_f32 v73, v73\n\t"
    "v_exp_f32 v74, v74\n\t"
    "v_exp_f32 v75, v75\n\t"
    "v_exp_f32 v76, v76\n\t"
    "v_exp_f32 v77, v77\n\t"
    "v_smfmac_f32_32x32x32_bf16 v[128:143], v[108:111], v[64:71], v124\n\t"
    "ds_read_b128 v[112:115], v127 offset:13728\n\t"
    "buffer_load_dwordx4 v1, s[12:15], s54 offen offset:3072 sc1 lds\n\t"
    "v_exp_f32 v78, v78\n\t"
    "v_exp_f32 v79, v79\n\t"
    "v_exp_f32 v80, v80\n\t"
    "v_exp_f32 v81, v81\n\t"
    "v_smfmac_f32_32x32x32_bf16 v[128:143], v[116:119], v[72:79], v124\n\t"
    "v_exp_f32 v82, v82\n\t"
    "v_exp_f32 v83, v83\n\t"
    "v_exp_f32 v84, v84\n\t"
    "v_exp_f32 v85, v85\n\t"
    "v_mfma_f32_32x32x16_bf16 v[32:47], v[96:99], v[16:19], v[32:47]\n\t"
    "s_add_u32 m0, m0, 0x4000\n\t"
    "s_add_u32 s54, s54, 0x4000\n\t"
    "buffer_load_dwordx4 v1, s[12:15], s54 offen sc1 lds\n\t"
    "v_exp_f32 v86, v86\n\t"
    "v_exp_f32 v87, v87\n\t"
    "v_exp_f32 v88, v88\n\t"
    "v_exp_f32 v89, v89\n\t"
    "v_smfmac_f32_32x32x32_bf16 v[144:159], v[108:111], v[80:87], v124\n\t"
    "v_exp_f32 v90, v90\n\t"
    "v_exp_f32 v91, v91\n\t"
    "v_exp_f32 v92, v92\n\t"
    "v_exp_f32 v93, v93\n\t"
    "pad12_%=:\n\t"
    "s_cbranch_execz vb15_%=\n\t"
    "v_mfma_f32_32x32x16_bf16 v[48:63], v[96:99], v[20:23], v[48:63]\n\t"
    "buffer_load_dwordx4 v1, s[12:15], s54 offen offset:1024 sc1 lds\n\t"
    "v_exp_f32 v94, v94\n\t"
    "v_exp_f32 v95, v95\n\t"
    "v_exp_f32 v32, v32\n\t"
    "v_exp_f32 v33, v33\n\t"
    "v_smfmac_f32_32x32x32_bf16 v[144:159], v[116:119], v[88:95], v124\n\t"
    "s_waitcnt lgkmcnt(0)\n\t"
    "ds_read_b128 v[96:99], v125 offset:15744\n\t"
    "v_exp_f32 v34, v34\n\t"
    "v_exp_f32 v35, v35\n\t"
    "v_exp_f32 v36, v36\n\t"
    "v_exp_f32 v37, v37\n\t"
    "v_mfma_f32_32x32x4_2b_bf16 v[64:95], v[120:121], v[24:25], 0 cbsz:1 abid:1\n\t"
    "ds_read_b128 v[108:111], v127 offset:14400\n\t"
    "v_exp_f32 v38, v38\n\t"
    "v_exp_f32 v39, v39\n\t"
    "v_exp_f32 v40, v40\n\t"
    "v_exp_f32 v41, v41\n\t"
    "v_exp_f32 v42, v42\n\t"
    "v_exp_f32 v43, v43\n\t"
    "v_exp_f32 v44, v44\n\t"
    "v_exp_f32 v45, v45\n\t"
    "v_smfmac_f32_32x32x32_bf16 v[128:143], v[104:107], v[32:39], v124\n\t"
    "ds_read_b128 v[116:119], v127 offset:15072\n\t"
    "buffer_load_dwordx4 v1, s[12:15], s54 offen offset:2048 sc1 lds\n\t"
    "v_exp_f32 v46, v46\n\t"
    "v_exp_f32 v47, v47\n\t"
    "v_exp_f32 v48, v48\n\t"
    "v_exp_f32 v49, v49\n\t"
    "v_smfmac_f32_32x32x32_bf16 v[128:143], v[112:115], v[40:47], v124\n\t"
    "ds_read_b64 v[122:123], v126 offset:17792\n\t"
    "v_exp_f32 v50, v50\n\t"
    "v_exp_f32 v51, v51\n\t"
    "v_exp_f32 v52, v52\n\t"
    "v_exp_f32 v53, v53\n\t"
    "v_mfma_f32_32x32x16_bf16 v[64:79], v[100:103], v[16:19], v[64:79]\n\t"
    "buffer_load_dwordx4 v1, s[12:15], s54 offen offset:3072 sc1 lds\n\t"
    "v_exp_f32 v54, v54\n\t"
    "v_exp_f32 v55, v55\n\t"
    "v_exp_f32 v56, v56\n\t"
    "v_exp_f32 v57, v57\n\t"
    "v_smfmac_f32_32x32x32_bf16 v[144:159], v[104:107], v[48:55], v124\n\t"
    "v_exp_f32 v58, v58\n\t"
    "v_exp_f32 v59, v59\n\t"
    "v_exp_f32 v60, v60\n\t"
    "v_exp_f32 v61, v61\n\t"
    "v_mfma_f32_32x32x16_bf16 v[80:95], v[100:103], v[20:23], v[80:95]\n\t"
    "s_add_u32 m0, m0, 0x4000\n\t"
    "s_add_u32 s54, s54, 0x4000\n\t"
    "buffer_load_dwordx4 v1, s[12:15], s54 offen sc1 lds\n\t"
    "v_exp_f32 v62, v62\n\t"
    "v_exp_f32 v63, v63\n\t"
    "v_exp_f32 v64, v64\n\t"
    "v_exp_f32 v65, v65\n\t"
    "v_smfmac_f32_32x32x32_bf16 v[144:159], v[112:115], v[56:63], v124\n\t"
    "s_waitcnt lgkmcnt(0)\n\t"
    "ds_read_b128 v[100:103], v125 offset:16768\n\t"
    "v_exp_f32 v66, v66\n\t"
    "v_exp_f32 v67, v67\n\t"
    "v_exp_f32 v68, v68\n\t"
    "v_exp_f32 v69, v69\n\t"
    "v_mfma_f32_32x32x4_2b_bf16 v[32:63], v[122:123], v[24:25], 0 cbsz:1 abid:0\n\t"
    "ds_read_b128 v[104:107], v127 offset:18304\n\t"
    "v_exp_f32 v70, v70\n\t"
    "v_exp_f32 v71, v71\n\t"
    "v_exp_f32 v72, v72\n\t"
    "v_exp_f32 v73, v73\n\t"
    "v_exp_f32 v74, v74\n\t"
    "v_exp_f32 v75, v75\n\t"
    "v_exp_f32 v76, v76\n\t"
    "v_exp_f32 v77, v77\n\t"
    "v_smfmac_f32_32x32x32_bf16 v[128:143], v[108:111], v[64:71], v124\n\t"
    "ds_read_b128 v[112:115], v127 offset:18976\n\t"
    "buffer_load_dwordx4 v1, s[12:15], s54 offen offset:1024 sc1 lds\n\t"
    "v_exp_f32 v78, v78\n\t"
    "v_exp_f32 v79, v79\n\t"
    "v_exp_f32 v80, v80\n\t"
    "v_exp_f32 v81, v81\n\t"
    "v_smfmac_f32_32x32x32_bf16 v[128:143], v[116:119], v[72:79], v124\n\t"
    "v_exp_f32 v82, v82\n\t"
    "v_exp_f32 v83, v83\n\t"
    "v_exp_f32 v84, v84\n\t"
    "v_exp_f32 v85, v85\n\t"
    "v_mfma_f32_32x32x16_bf16 v[32:47], v[96:99], v[16:19], v[32:47]\n\t"
    "buffer_load_dwordx4 v1, s[12:15], s54 offen offset:2048 sc1 lds\n\t"
    "v_exp_f32 v86, v86\n\t"
    "v_exp_f32 v87, v87\n\t"
    "v_exp_f32 v88, v88\n\t"
    "v_exp_f32 v89, v89\n\t"
    "v_smfmac_f32_32x32x32_bf16 v[144:159], v[108:111], v[80:87], v124\n\t"
    "v_exp_f32 v90, v90\n\t"
    "v_exp_f32 v91, v91\n\t"
    "v_exp_f32 v92, v92\n\t"
    "v_exp_f32 v93, v93\n\t"
    "v_mfma_f32_32x32x16_bf16 v[48:63], v[96:99], v[20:23], v[48:63]\n\t"
    "buffer_load_dwordx4 v1, s[12:15], s54 offen offset:3072 sc1 lds\n\t"
    "v_exp_f32 v94, v94\n\t"
    "v_exp_f32 v95, v95\n\t"
    "v_exp_f32 v32, v32\n\t"
    "v_exp_f32 v33, v33\n\t"
    "v_smfmac_f32_32x32x32_bf16 v[144:159], v[116:119], v[88:95], v124\n\t"
    "s_waitcnt lgkmcnt(0)\n\t"
    "ds_read_b128 v[96:99], v125 offset:20992\n\t"
    "v_exp_f32 v34, v34\n\t"
    "v_exp_f32 v35, v35\n\t"
    "v_exp_f32 v36, v36\n\t"
    "v_exp_f32 v37, v37\n\t"
    "v_mfma_f32_32x32x4_2b_bf16 v[64:95], v[122:123], v[24:25], 0 cbsz:1 abid:1\n\t"
    "ds_read_b128 v[108:111], v127 offset:19648\n\t"
    "v_exp_f32 v38, v38\n\t"
    "v_exp_f32 v39, v39\n\t"
    "v_exp_f32 v40, v40\n\t"
    "v_exp_f32 v41, v41\n\t"
    "v_exp_f32 v42, v42\n\t"
    "v_exp_f32 v43, v43\n\t"
    "v_exp_f32 v44, v44\n\t"
    "v_exp_f32 v45, v45\n\t"
    "v_smfmac_f32_32x32x32_bf16 v[128:143], v[104:107], v[32:39], v124\n\t"
    "ds_read_b128 v[116:119], v127 offset:20320\n\t"
    "s_add_u32 m0, m0, 0x4000\n\t"
    "s_add_u32 s54, s54, 0x4000\n\t"
    "buffer_load_dwordx4 v1, s[12:15], s54 offen sc1 lds\n\t"
    "v_exp_f32 v46, v46\n\t"
    "v_exp_f32 v47, v47\n\t"
    "v_exp_f32 v48, v48\n\t"
    "v_exp_f32 v49, v49\n\t"
    "v_smfmac_f32_32x32x32_bf16 v[128:143], v[112:115], v[40:47], v124\n\t"
    "ds_read_b64 v[120:121], v126 offset:23040\n\t"
    "v_exp_f32 v50, v50\n\t"
    "v_exp_f32 v51, v51\n\t"
    "v_exp_f32 v52, v52\n\t"
    "v_exp_f32 v53, v53\n\t"
    "v_mfma_f32_32x32x16_bf16 v[64:79], v[100:103], v[16:19], v[64:79]\n\t"
    "buffer_load_dwordx4 v1, s[12:15], s54 offen offset:1024 sc1 lds\n\t"
    "v_exp_f32 v54, v54\n\t"
    "v_exp_f32 v55, v55\n\t"
    "v_exp_f32 v56, v56\n\t"
    "v_exp_f32 v57, v57\n\t"
    "v_smfmac_f32_32x32x32_bf16 v[144:159], v[104:107], v[48:55], v124\n\t"
    "v_exp_f32 v58, v58\n\t"
    "v_exp_f32 v59, v59\n\t"
    "v_exp_f32 v60, v60\n\t"
    "v_exp_f32 v61, v61\n\t"
    "v_mfma_f32_32x32x16_bf16 v[80:95], v[100:103], v[20:23], v[80:95]\n\t"
    "buffer_load_dwordx4 v1, s[12:15], s54 offen offset:2048 sc1 lds\n\t"
    "v_exp_f32 v62, v62\n\t"
    "v_exp_f32 v63, v63\n\t"
    "v_exp_f32 v64, v64\n\t"
    "v_exp_f32 v65, v65\n\t"
    "v_smfmac_f32_32x32x32_bf16 v[144:159], v[112:115], v[56:63], v124\n\t"
    "s_waitcnt lgkmcnt(0)\n\t"
    "ds_read_b128 v[100:103], v125 offset:22016\n\t"
    "v_exp_f32 v66, v66\n\t"
    "v_exp_f32 v67, v67\n\t"
    "v_exp_f32 v68, v68\n\t"
    "v_exp_f32 v69, v69\n\t"
    "v_mfma_f32_32x32x4_2b_bf16 v[32:63], v[120:121], v[24:25], 0 cbsz:1 abid:0\n\t"
    "ds_read_b128 v[104:107], v127 offset:23552\n\t"
    "v_exp_f32 v70, v70\n\t"
    "v_exp_f32 v71, v71\n\t"
    "v_exp_f32 v72, v72\n\t"
    "v_exp_f32 v73, v73\n\t"
    "v_exp_f32 v74, v74\n\t"
    "v_exp_f32 v75, v75\n\t"
    "v_exp_f32 v76, v76\n\t"
    "v_exp_f32 v77, v77\n\t"
    "v_smfmac_f32_32x32x32_bf16 v[128:143], v[108:111], v[64:71], v124\n\t"
    "ds_read_b128 v[112:115], v127 offset:24224\n\t"
    "buffer_load_dwordx4 v1, s[12:15], s54 offen offset:3072 sc1 lds\n\t"
    "v_exp_f32 v78, v78\n\t"
    "v_exp_f32 v79, v79\n\t"
    "v_exp_f32 v80, v80\n\t"
    "v_exp_f32 v81, v81\n\t"
    "v_smfmac_f32_32x32x32_bf16 v[128:143], v[116:119], v[72:79], v124\n\t"
    "v_exp_f32 v82, v82\n\t"
    "v_exp_f32 v83, v83\n\t"
    "v_exp_f32 v84, v84\n\t"
    "v_exp_f32 v85, v85\n\t"
    "v_mfma_f32_32x32x16_bf16 v[32:47], v[96:99], v[16:19], v[32:47]\n\t"
    "s_add_u32 m0, m0, 0x4000\n\t"
    "s_add_u32 s54, s54, 0x4000\n\t"
    "buffer_load_dwordx4 v1, s[12:15], s54 offen sc1 lds\n\t"
    "v_exp_f32 v86, v86\n\t"
    "v_exp_f32 v87, v87\n\t"
    "v_exp_f32 v88, v88\n\t"
    "v_exp_f32 v89, v89\n\t"
    "v_smfmac_f32_32x32x32_bf16 v[144:159], v[108:111], v[80:87], v124\n\t"
    "v_exp_f32 v90, v90\n\t"
    "v_exp_f32 v91, v91\n\t"
    "v_exp_f32 v92, v92\n\t"
    "v_exp_f32 v93, v93\n\t"
    "v_mfma_f32_32x32x16_bf16 v[48:63], v[96:99], v[20:23], v[48:63]\n\t"
    "buffer_load_dwordx4 v1, s[12:15], s54 offen offset:1024 sc1 lds\n\t"
    "v_exp_f32 v94, v94\n\t"
    "v_exp_f32 v95, v95\n\t"
    "v_exp_f32 v32, v32\n\t"
    "v_exp_f32 v33, v33\n\t"
    "v_smfmac_f32_32x32x32_bf16 v[144:159], v[116:119], v[88:95], v124\n\t"
    "s_waitcnt lgkmcnt(0)\n\t"
    "ds_read_b128 v[96:99], v125 offset:26240\n\t"
    "v_exp_f32 v34, v34\n\t"
    "v_exp_f32 v35, v35\n\t"
    "v_exp_f32 v36, v36\n\t"
    "v_exp_f32 v37, v37\n\t"
    "v_mfma_f32_32x32x4_2b_bf16 v[64:95], v[120:121], v[24:25], 0 cbsz:1 abid:1\n\t"
    "ds_read_b128 v[108:111], v127 offset:24896\n\t"
    "v_exp_f32 v38, v38\n\t"
    "v_exp_f32 v39, v39\n\t"
    "v_exp_f32 v40, v40\n\t"
    "v_exp_f32 v41, v41\n\t"
    "v_exp_f32 v42, v42\n\t"
    "v_exp_f32 v43, v43\n\t"
    "v_exp_f32 v44, v44\n\t"
    "v_exp_f32 v45, v45\n\t"
    "v_smfmac_f32_32x32x32_bf16 v[128:143], v[104:107], v[32:39], v124\n\t"
    "ds_read_b128 v[116:119], v127 offset:25568\n\t"
    "buffer_load_dwordx4 v1, s[12:15], s54 offen offset:2048 sc1 lds\n\t"
    "v_exp_f32 v46, v46\n\t"
    "v_exp_f32 v47, v47\n\t"
    "v_exp_f32 v48, v48\n\t"
    "v_exp_f32 v49, v49\n\t"
    "v_smfmac_f32_32x32x32_bf16 v[128:143], v[112:115], v[40:47], v124\n\t"
    "ds_read_b64 v[122:123], v126 offset:28288\n\t"
    "v_exp_f32 v50, v50\n\t"
    "v_exp_f32 v51, v51\n\t"
    "v_exp_f32 v52, v52\n\t"
    "v_exp_f32 v53, v53\n\t"
    "v_mfma_f32_32x32x16_bf16 v[64:79], v[100:103], v[16:19], v[64:79]\n\t"
    "buffer_load_dwordx4 v1, s[12:15], s54 offen offset:3072 sc1 lds\n\t"
    "v_exp_f32 v54, v54\n\t"
    "v_exp_f32 v55, v55\n\t"
    "v_exp_f32 v56, v56\n\t"
    "v_exp_f32 v57, v57\n\t"
    "v_smfmac_f32_32x32x32_bf16 v[144:159], v[104:107], v[48:55], v124\n\t"
    "v_exp_f32 v58, v58\n\t"
    "v_exp_f32 v59, v59\n\t"
    "v_exp_f32 v60, v60\n\t"
    "v_exp_f32 v61, v61\n\t"
    "v_mfma_f32_32x32x16_bf16 v[80:95], v[100:103], v[20:23], v[80:95]\n\t"
    "s_mul_i32 m0, s33, 0x400\n\t"
    "s_add_u32 m0, m0, 0x27000\n\t"
    "s_add_u32 s50, m0, s45\n\t"
    "buffer_load_dwordx4 v1, s[12:15], s50 offen sc1 lds\n\t"
    "v_exp_f32 v62, v62\n\t"
    "v_exp_f32 v63, v63\n\t"
    "v_exp_f32 v64, v64\n\t"
    "v_exp_f32 v65, v65\n\t"
    "v_smfmac_f32_32x32x32_bf16 v[144:159], v[112:115], v[56:63], v124\n\t"
    "s_waitcnt lgkmcnt(0)\n\t"
    "ds_read_b128 v[100:103], v125 offset:27264\n\t"
    "v_exp_f32 v66, v66\n\t"
    "v_exp_f32 v67, v67\n\t"
    "v_exp_f32 v68, v68\n\t"
    "v_exp_f32 v69, v69\n\t"
    "v_mfma_f32_32x32x4_2b_bf16 v[32:63], v[122:123], v[24:25], 0 cbsz:1 abid:0\n\t"
    "ds_read_b128 v[104:107], v127 offset:28800\n\t"
    "v_exp_f32 v70, v70\n\t"
    "v_exp_f32 v71, v71\n\t"
    "v_exp_f32 v72, v72\n\t"
    "v_exp_f32 v73, v73\n\t"
    "v_exp_f32 v74, v74\n\t"
    "v_exp_f32 v75, v75\n\t"
    "v_exp_f32 v76, v76\n\t"
    "v_exp_f32 v77, v77\n\t"
    "v_smfmac_f32_32x32x32_bf16 v[128:143], v[108:111], v[64:71], v124\n\t"
    "ds_read_b128 v[112:115], v127 offset:29472\n\t"
    "v_exp_f32 v78, v78\n\t"
    "v_exp_f32 v79, v79\n\t"
    "v_exp_f32 v80, v80\n\t"
    "v_exp_f32 v81, v81\n\t"
    "v_smfmac_f32_32x32x32_bf16 v[128:143], v[116:119], v[72:79], v124\n\t"
    "v_exp_f32 v82, v82\n\t"
    "v_exp_f32 v83, v83\n\t"
    "v_exp_f32 v84, v84\n\t"
    "v_exp_f32 v85, v85\n\t"
    "v_mfma_f32_32x32x16_bf16 v[32:47], v[96:99], v[16:19], v[32:47]\n\t"
    "v_exp_f32 v86, v86\n\t"
    "v_exp_f32 v87, v87\n\t"
    "v_exp_f32 v88, v88\n\t"
    "v_exp_f32 v89, v89\n\t"
    "v_smfmac_f32_32x32x32_bf16 v[144:159], v[108:111], v[80:87], v124\n\t"
    "v_exp_f32 v90, v90\n\t"
    "v_exp_f32 v91, v91\n\t"
    "v_exp_f32 v92, v92\n\t"
    "v_exp_f32 v93, v93\n\t"
    "v_mfma_f32_32x32x16_bf16 v[48:63], v[96:99], v[20:23], v[48:63]\n\t"
    "v_exp_f32 v94, v94\n\t"
    "v_exp_f32 v95, v95\n\t"
    "v_exp_f32 v32, v32\n\t"
    "v_exp_f32 v33, v33\n\t"
    "v_smfmac_f32_32x32x32_bf16 v[144:159], v[116:119], v[88:95], v124\n\t"
    "s_waitcnt lgkmcnt(0)\n\t"
    "ds_read_b128 v[96:99], v125 offset:31488\n\t"
    "v_exp_f32 v34, v34\n\t"
    "v_exp_f32 v35, v35\n\t"
    "v_exp_f32 v36, v36\n\t"
    "v_exp_f32 v37, v37\n\t"
    "v_mfma_f32_32x32x4_2b_bf16 v[64:95], v[122:123], v[24:25], 0 cbsz:1 abid:1\n\t"
    "ds_read_b128 v[108:111], v127 offset:30144\n\t"
    "v_exp_f32 v38, v38\n\t"
    "v_exp_f32 v39, v39\n\t"
    "v_exp_f32 v40, v40\n\t"
    "v_exp_f32 v41, v41\n\t"
    "v_exp_f32 v42, v42\n\t"
    "v_exp_f32 v43, v43\n\t"
    "v_exp_f32 v44, v44\n\t"
    "v_exp_f32 v45, v45\n\t"
    "v_smfmac_f32_32x32x32_bf16 v[128:143], v[104:107], v[32:39], v124\n\t"
    "ds_read_b128 v[116:119], v127 offset:30816\n\t"
    "v_exp_f32 v46, v46\n\t"
    "v_exp_f32 v47, v47\n\t"
    "v_exp_f32 v48, v48\n\t"
    "v_exp_f32 v49, v49\n\t"
    "v_smfmac_f32_32x32x32_bf16 v[128:143], v[112:115], v[40:47], v124\n\t"
    "ds_read_b64 v[120:121], v126 offset:33536\n\t"
    "v_exp_f32 v50, v50\n\t"
    "v_exp_f32 v51, v51\n\t"
    "v_exp_f32 v52, v52\n\t"
    "v_exp_f32 v53, v53\n\t"
    "v_mfma_f32_32x32x16_bf16 v[64:79], v[100:103], v[16:19], v[64:79]\n\t"
    "v_exp_f32 v54, v54\n\t"
    "v_exp_f32 v55, v55\n\t"
    "v_exp_f32 v56, v56\n\t"
    "v_exp_f32 v57, v57\n\t"
    "v_smfmac_f32_32x32x32_bf16 v[144:159], v[104:107], v[48:55], v124\n\t"
    "v_exp_f32 v58, v58\n\t"
    "v_exp_f32 v59, v59\n\t"
    "v_exp_f32 v60, v60\n\t"
    "v_exp_f32 v61, v61\n\t"
    "v_mfma_f32_32x32x16_bf16 v[80:95], v[100:103], v[20:23], v[80:95]\n\t"
    "v_exp_f32 v62, v62\n\t"
    "v_exp_f32 v63, v63\n\t"
    "v_exp_f32 v64, v64\n\t"
    "v_exp_f32 v65, v65\n\t"
    "v_smfmac_f32_32x32x32_bf16 v[144:159], v[112:115], v[56:63], v124\n\t"
    "s_waitcnt lgkmcnt(0)\n\t"
    "ds_read_b128 v[100:103], v125 offset:32512\n\t"
    "v_exp_f32 v66, v66\n\t"
    "v_exp_f32 v67, v67\n\t"
    "v_exp_f32 v68, v68\n\t"
    "v_exp_f32 v69, v69\n\t"
    "v_mfma_f32_32x32x4_2b_bf16 v[32:63], v[120:121], v[24:25], 0 cbsz:1 abid:0\n\t"
    "ds_read_b128 v[104:107], v127 offset:34048\n\t"
    "v_exp_f32 v70, v70\n\t"
    "v_exp_f32 v71, v71\n\t"
    "v_exp_f32 v72, v72\n\t"
    "v_exp_f32 v73, v73\n\t"
    "v_exp_f32 v74, v74\n\t"
    "v_exp_f32 v75, v75\n\t"
    "v_exp_f32 v76, v76\n\t"
    "v_exp_f32 v77, v77\n\t"
    "v_smfmac_f32_32x32x32_bf16 v[128:143], v[108:111], v[64:71], v124\n\t"
    "ds_read_b128 v[112:115], v127 offset:34720\n\t"
    "v_exp_f32 v78, v78\n\t"
    "v_exp_f32 v79, v79\n\t"
    "v_exp_f32 v80, v80\n\t"
    "v_exp_f32 v81, v81\n\t"
    "v_smfmac_f32_32x32x32_bf16 v[128:143], v[116:119], v[72:79], v124\n\t"
    "v_exp_f32 v82, v82\n\t"
    "v_exp_f32 v83, v83\n\t"
    "v_exp_f32 v84, v84\n\t"
    "v_exp_f32 v85, v85\n\t"
    "v_mfma_f32_32x32x16_bf16 v[32:47], v[96:99], v[16:19], v[32:47]\n\t"
    "v_exp_f32 v86, v86\n\t"
    "v_exp_f32 v87, v87\n\t"
    "v_exp_f32 v88, v88\n\t"
    "v_exp_f32 v89, v89\n\t"
    "v_smfmac_f32_32x32x32_bf16 v[144:159], v[108:111], v[80:87], v124\n\t"
    "v_exp_f32 v90, v90\n\t"
    "v_exp_f32 v91, v91\n\t"
    "v_exp_f32 v92, v92\n\t"
    "v_exp_f32 v93, v93\n\t"
    "v_mfma_f32_32x32x16_bf16 v[48:63], v[96:99], v[20:23], v[48:63]\n\t"
    "v_exp_f32 v94, v94\n\t"
    "v_exp_f32 v95, v95\n\t"
    "v_exp_f32 v32, v32\n\t"
    "v_exp_f32 v33, v33\n\t"
    "v_smfmac_f32_32x32x32_bf16 v[144:159], v[116:119], v[88:95], v124\n\t"
    "s_waitcnt lgkmcnt(0)\n\t"
    "ds_read_b128 v[96:99], v125 offset:36736\n\t"
    "v_exp_f32 v34, v34\n\t"
    "v_exp_f32 v35, v35\n\t"
    "v_exp_f32 v36, v36\n\t"
    "v_exp_f32 v37, v37\n\t"
    "v_mfma_f32_32x32x4_2b_bf16 v[64:95], v[120:121], v[24:25], 0 cbsz:1 abid:1\n\t"
    "ds_read_b128 v[108:111], v127 offset:35392\n\t"
    "v_exp_f32 v38, v38\n\t"
    "v_exp_f32 v39, v39\n\t"
    "v_exp_f32 v40, v40\n\t"
    "v_exp_f32 v41, v41\n\t"
    "v_exp_f32 v42, v42\n\t"
    "v_exp_f32 v43, v43\n\t"
    "v_exp_f32 v44, v44\n\t"
    "v_exp_f32 v45, v45\n\t"
    "v_smfmac_f32_32x32x32_bf16 v[128:143], v[104:107], v[32:39], v124\n\t"
    "ds_read_b128 v[116:119], v127 offset:36064\n\t"
    "v_exp_f32 v46, v46\n\t"
    "v_exp_f32 v47, v47\n\t"
    "v_exp_f32 v48, v48\n\t"
    "v_exp_f32 v49, v49\n\t"
    "v_smfmac_f32_32x32x32_bf16 v[128:143], v[112:115], v[40:47], v124\n\t"
    "ds_read_b64 v[122:123], v126 offset:38784\n\t"
    "v_exp_f32 v50, v50\n\t"
    "v_exp_f32 v51, v51\n\t"
    "v_exp_f32 v52, v52\n\t"
    "v_exp_f32 v53, v53\n\t"
    "v_mfma_f32_32x32x16_bf16 v[64:79], v[100:103], v[16:19], v[64:79]\n\t"
    "v_exp_f32 v54, v54\n\t"
    "v_exp_f32 v55, v55\n\t"
    "v_exp_f32 v56, v56\n\t"
    "v_exp_f32 v57, v57\n\t"
    "v_smfmac_f32_32x32x32_bf16 v[144:159], v[104:107], v[48:55], v124\n\t"
    "v_exp_f32 v58, v58\n\t"
    "v_exp_f32 v59, v59\n\t"
    "v_exp_f32 v60, v60\n\t"
    "v_exp_f32 v61, v61\n\t"
    "v_mfma_f32_32x32x16_bf16 v[80:95], v[100:103], v[20:23], v[80:95]\n\t"
    "v_exp_f32 v62, v62\n\t"
    "v_exp_f32 v63, v63\n\t"
    "v_exp_f32 v64, v64\n\t"
    "v_exp_f32 v65, v65\n\t"
    "v_smfmac_f32_32x32x32_bf16 v[144:159], v[112:115], v[56:63], v124\n\t"
    "s_waitcnt lgkmcnt(0)\n\t"
    "ds_read_b128 v[100:103], v125 offset:37760\n\t"
    "v_exp_f32 v66, v66\n\t"
    "v_exp_f32 v67, v67\n\t"
    "v_exp_f32 v68, v68\n\t"
    "v_exp_f32 v69, v69\n\t"
    "v_mfma_f32_32x32x4_2b_bf16 v[32:63], v[122:123], v[24:25], 0 cbsz:1 abid:0\n\t"
    "ds_read_b128 v[104:107], v127 offset:39296\n\t"
    "v_exp_f32 v70, v70\n\t"
    "v_exp_f32 v71, v71\n\t"
    "v_exp_f32 v72, v72\n\t"
    "v_exp_f32 v73, v73\n\t"
    "v_exp_f32 v74, v74\n\t"
    "v_exp_f32 v75, v75\n\t"
    "v_exp_f32 v76, v76\n\t"
    "v_exp_f32 v77, v77\n\t"
    "v_smfmac_f32_32x32x32_bf16 v[128:143], v[108:111], v[64:71], v124\n\t"
    "ds_read_b128 v[112:115], v127 offset:39968\n\t"
    "v_exp_f32 v78, v78\n\t"
    "v_exp_f32 v79, v79\n\t"
    "v_exp_f32 v80, v80\n\t"
    "v_exp_f32 v81, v81\n\t"
    "v_smfmac_f32_32x32x32_bf16 v[128:143], v[116:119], v[72:79], v124\n\t"
    "v_exp_f32 v82, v82\n\t"
    "v_exp_f32 v83, v83\n\t"
    "v_exp_f32 v84, v84\n\t"
    "v_exp_f32 v85, v85\n\t"
    "v_mfma_f32_32x32x16_bf16 v[32:47], v[96:99], v[16:19], v[32:47]\n\t"
    "v_exp_f32 v86, v86\n\t"
    "v_exp_f32 v87, v87\n\t"
    "v_exp_f32 v88, v88\n\t"
    "v_exp_f32 v89, v89\n\t"
    "v_smfmac_f32_32x32x32_bf16 v[144:159], v[108:111], v[80:87], v124\n\t"
    "v_exp_f32 v90, v90\n\t"
    "v_exp_f32 v91, v91\n\t"
    "v_exp_f32 v92, v92\n\t"
    "v_exp_f32 v93, v93\n\t"
    "v_mfma_f32_32x32x16_bf16 v[48:63], v[96:99], v[20:23], v[48:63]\n\t"
    "v_exp_f32 v94, v94\n\t"
    "v_exp_f32 v95, v95\n\t"
    "s_waitcnt vmcnt(9)\n\t"
    "s_barrier\n\t"
    "v_exp_f32 v32, v32\n\t"
    "v_exp_f32 v33, v33\n\t"
    "v_smfmac_f32_32x32x32_bf16 v[144:159], v[116:119], v[88:95], v124\n\t"
    "s_waitcnt lgkmcnt(0)\n\t"
    "ds_read_b128 v[96:99], v125 offset:41984\n\t"
    "v_exp_f32 v34, v34\n\t"
    "v_exp_f32 v35, v35\n\t"
    "v_exp_f32 v36, v36\n\t"
    "v_exp_f32 v37, v37\n\t"
    "v_mfma_f32_32x32x4_2b_bf16 v[64:95], v[122:123], v[24:25], 0 cbsz:1 abid:1\n\t"
    "ds_read_b128 v[108:111], v127 offset:40640\n\t"
    "v_exp_f32 v38, v38\n\t"
    "v_exp_f32 v39, v39\n\t"
    "v_exp_f32 v40, v40\n\t"
    "v_exp_f32 v41, v41\n\t"
    "v_exp_f32 v42, v42\n\t"
    "v_exp_f32 v43, v43\n\t"
    "v_exp_f32 v44, v44\n\t"
    "v_exp_f32 v45, v45\n\t"
    "v_smfmac_f32_32x32x32_bf16 v[128:143], v[104:107], v[32:39], v124\n\t"
    "ds_read_b128 v[116:119], v127 offset:41312\n\t"
    "v_exp_f32 v46, v46\n\t"
    "v_exp_f32 v47, v47\n\t"
    "v_exp_f32 v48, v48\n\t"
    "v_exp_f32 v49, v49\n\t"
    "v_smfmac_f32_32x32x32_bf16 v[128:143], v[112:115], v[40:47], v124\n\t"
    "ds_read_b64 v[120:121], v126 offset:44032\n\t"
    "v_exp_f32 v50, v50\n\t"
    "v_exp_f32 v51, v51\n\t"
    "v_exp_f32 v52, v52\n\t"
    "v_exp_f32 v53, v53\n\t"
    "v_mfma_f32_32x32x16_bf16 v[64:79], v[100:103], v[16:19], v[64:79]\n\t"
    "v_exp_f32 v54, v54\n\t"
    "v_exp_f32 v55, v55\n\t"
    "v_exp_f32 v56, v56\n\t"
    "v_exp_f32 v57, v57\n\t"
    "v_smfmac_f32_32x32x32_bf16 v[144:159], v[104:107], v[48:55], v124\n\t"
    "v_exp_f32 v58, v58\n\t"
    "v_exp_f32 v59, v59\n\t"
    "v_exp_f32 v60, v60\n\t"
    "v_exp_f32 v61, v61\n\t"
    "v_mfma_f32_32x32x16_bf16 v[80:95], v[100:103], v[20:23], v[80:95]\n\t"
    "v_exp_f32 v62, v62\n\t"
    "v_exp_f32 v63, v63\n\t"
    "v_exp_f32 v64, v64\n\t"
    "v_exp_f32 v65, v65\n\t"
    "v_smfmac_f32_32x32x32_bf16 v[144:159], v[112:115], v[56:63], v124\n\t"
    "s_waitcnt lgkmcnt(0)\n\t"
    "ds_read_b128 v[100:103], v125 offset:43008\n\t"
    "v_exp_f32 v66, v66\n\t"
    "v_exp_f32 v67, v67\n\t"
    "v_exp_f32 v68, v68\n\t"
    "v_exp_f32 v69, v69\n\t"
    "v_mfma_f32_32x32x4_2b_bf16 v[32:63], v[120:121], v[24:25], 0 cbsz:1 abid:0\n\t"
    "ds_read_b128 v[104:107], v127 offset:44544\n\t"
    "v_exp_f32 v70, v70\n\t"
    "v_exp_f32 v71, v71\n\t"
    "v_exp_f32 v72, v72\n\t"
    "v_exp_f32 v73, v73\n\t"
    "v_exp_f32 v74, v74\n\t"
    "v_exp_f32 v75, v75\n\t"
    "v_exp_f32 v76, v76\n\t"
    "v_exp_f32 v77, v77\n\t"
    "v_smfmac_f32_32x32x32_bf16 v[128:143], v[108:111], v[64:71], v124\n\t"
    "ds_read_b128 v[112:115], v127 offset:45216\n\t"
    "v_exp_f32 v78, v78\n\t"
    "v_exp_f32 v79, v79\n\t"
    "v_exp_f32 v80, v80\n\t"
    "v_exp_f32 v81, v81\n\t"
    "v_smfmac_f32_32x32x32_bf16 v[128:143], v[116:119], v[72:79], v124\n\t"
    "v_exp_f32 v82, v82\n\t"
    "v_exp_f32 v83, v83\n\t"
    "v_exp_f32 v84, v84\n\t"
    "v_exp_f32 v85, v85\n\t"
    "v_mfma_f32_32x32x16_bf16 v[32:47], v[96:99], v[16:19], v[32:47]\n\t"
    "v_exp_f32 v86, v86\n\t"
    "v_exp_f32 v87, v87\n\t"
    "v_exp_f32 v88, v88\n\t"
    "v_exp_f32 v89, v89\n\t"
    "v_smfmac_f32_32x32x32_bf16 v[144:159], v[108:111], v[80:87], v124\n\t"
    "v_exp_f32 v90, v90\n\t"
    "v_exp_f32 v91, v91\n\t"
    "v_exp_f32 v92, v92\n\t"
    "v_exp_f32 v93, v93\n\t"
    "v_mfma_f32_32x32x16_bf16 v[48:63], v[96:99], v[20:23], v[48:63]\n\t"
    "v_exp_f32 v94, v94\n\t"
    "v_exp_f32 v95, v95\n\t"
    "v_exp_f32 v32, v32\n\t"
    "v_exp_f32 v33, v33\n\t"
    "v_smfmac_f32_32x32x32_bf16 v[144:159], v[116:119], v[88:95], v124\n\t"
    "s_waitcnt lgkmcnt(0)\n\t"
    "ds_read_b128 v[96:99], v125 offset:47232\n\t"
    "v_exp_f32 v34, v34\n\t"
    "v_exp_f32 v35, v35\n\t"
    "v_exp_f32 v36, v36\n\t"
    "v_exp_f32 v37, v37\n\t"
    "v_mfma_f32_32x32x4_2b_bf16 v[64:95], v[120:121], v[24:25], 0 cbsz:1 abid:1\n\t"
    "ds_read_b128 v[108:111], v127 offset:45888\n\t"
    "v_exp_f32 v38, v38\n\t"
    "v_exp_f32 v39, v39\n\t"
    "v_exp_f32 v40, v40\n\t"
    "v_exp_f32 v41, v41\n\t"
    "v_exp_f32 v42, v42\n\t"
    "v_exp_f32 v43, v43\n\t"
    "v_exp_f32 v44, v44\n\t"
    "v_exp_f32 v45, v45\n\t"
    "v_smfmac_f32_32x32x32_bf16 v[128:143], v[104:107], v[32:39], v124\n\t"
    "ds_read_b128 v[116:119], v127 offset:46560\n\t"
    "v_exp_f32 v46, v46\n\t"
    "v_exp_f32 v47, v47\n\t"
    "v_exp_f32 v48, v48\n\t"
    "v_exp_f32 v49, v49\n\t"
    "v_smfmac_f32_32x32x32_bf16 v[128:143], v[112:115], v[40:47], v124\n\t"
    "ds_read_b64 v[122:123], v126 offset:49280\n\t"
    "v_exp_f32 v50, v50\n\t"
    "v_exp_f32 v51, v51\n\t"
    "v_exp_f32 v52, v52\n\t"
    "v_exp_f32 v53, v53\n\t"
    "v_mfma_f32_32x32x16_bf16 v[64:79], v[100:103], v[16:19], v[64:79]\n\t"
    "v_exp_f32 v54, v54\n\t"
    "v_exp_f32 v55, v55\n\t"
    "v_exp_f32 v56, v56\n\t"
    "v_exp_f32 v57, v57\n\t"
    "v_smfmac_f32_32x32x32_bf16 v[144:159], v[104:107], v[48:55], v124\n\t"
    "v_exp_f32 v58, v58\n\t"
    "v_exp_f32 v59, v59\n\t"
    "v_exp_f32 v60, v60\n\t"
    "v_exp_f32 v61, v61\n\t"
    "v_mfma_f32_32x32x16_bf16 v[80:95], v[100:103], v[20:23], v[80:95]\n\t"
    "v_exp_f32 v62, v62\n\t"
    "v_exp_f32 v63, v63\n\t"
    "v_exp_f32 v64, v64\n\t"
    "v_exp_f32 v65, v65\n\t"
    "v_smfmac_f32_32x32x32_bf16 v[144:159], v[112:115], v[56:63], v124\n\t"
    "s_waitcnt lgkmcnt(0)\n\t"
    "ds_read_b128 v[100:103], v125 offset:48256\n\t"
    "v_exp_f32 v66, v66\n\t"
    "v_exp_f32 v67, v67\n\t"
    "v_exp_f32 v68, v68\n\t"
    "v_exp_f32 v69, v69\n\t"
    "v_mfma_f32_32x32x4_2b_bf16 v[32:63], v[122:123], v[24:25], 0 cbsz:1 abid:0\n\t"
    "ds_read_b128 v[104:107], v127 offset:49792\n\t"
    "v_exp_f32 v70, v70\n\t"
    "v_exp_f32 v71, v71\n\t"
    "v_exp_f32 v72, v72\n\t"
    "v_exp_f32 v73, v73\n\t"
    "v_exp_f32 v74, v74\n\t"
    "v_exp_f32 v75, v75\n\t"
    "v_exp_f32 v76, v76\n\t"
    "v_exp_f32 v77, v77\n\t"
    "v_smfmac_f32_32x32x32_bf16 v[128:143], v[108:111], v[64:71], v124\n\t"
    "ds_read_b128 v[112:115], v127 offset:50464\n\t"
    "v_exp_f32 v78, v78\n\t"
    "v_exp_f32 v79, v79\n\t"
    "v_exp_f32 v80, v80\n\t"
    "v_exp_f32 v81, v81\n\t"
    "v_smfmac_f32_32x32x32_bf16 v[128:143], v[116:119], v[72:79], v124\n\t"
    "v_exp_f32 v82, v82\n\t"
    "v_exp_f32 v83, v83\n\t"
    "v_exp_f32 v84, v84\n\t"
    "v_exp_f32 v85, v85\n\t"
    "v_mfma_f32_32x32x16_bf16 v[32:47], v[96:99], v[16:19], v[32:47]\n\t"
    "v_exp_f32 v86, v86\n\t"
    "v_exp_f32 v87, v87\n\t"
    "v_exp_f32 v88, v88\n\t"
    "v_exp_f32 v89, v89\n\t"
    "v_smfmac_f32_32x32x32_bf16 v[144:159], v[108:111], v[80:87], v124\n\t"
    "v_exp_f32 v90, v90\n\t"
    "v_exp_f32 v91, v91\n\t"
    "v_exp_f32 v92, v92\n\t"
    "v_exp_f32 v93, v93\n\t"
    "v_mfma_f32_32x32x16_bf16 v[48:63], v[96:99], v[20:23], v[48:63]\n\t"
    "v_exp_f32 v94, v94\n\t"
    "v_exp_f32 v95, v95\n\t"
    "v_exp_f32 v32, v32\n\t"
    "v_exp_f32 v33, v33\n\t"
    "v_smfmac_f32_32x32x32_bf16 v[144:159], v[116:119], v[88:95], v124\n\t"
    "s_waitcnt lgkmcnt(0)\n\t"
    "ds_read_b128 v[96:99], v125 offset:52480\n\t"
    "v_exp_f32 v34, v34\n\t"
    "v_exp_f32 v35, v35\n\t"
    "v_exp_f32 v36, v36\n\t"
    "v_exp_f32 v37, v37\n\t"
    "v_mfma_f32_32x32x4_2b_bf16 v[64:95], v[122:123], v[24:25], 0 cbsz:1 abid:1\n\t"
    "ds_read_b128 v[108:111], v127 offset:51136\n\t"
    "v_exp_f32 v38, v38\n\t"
    "v_exp_f32 v39, v39\n\t"
    "v_exp_f32 v40, v40\n\t"
    "v_exp_f32 v41, v41\n\t"
    "v_exp_f32 v42, v42\n\t"
    "v_exp_f32 v43, v43\n\t"
    "v_exp_f32 v44, v44\n\t"
    "v_exp_f32 v45, v45\n\t"
    "v_smfmac_f32_32x32x32_bf16 v[128:143], v[104:107], v[32:39], v124\n\t"
    "ds_read_b128 v[116:119], v127 offset:51808\n\t"
    "v_exp_f32 v46, v46\n\t"
    "v_exp_f32 v47, v47\n\t"
    "v_exp_f32 v48, v48\n\t"
    "v_exp_f32 v49, v49\n\t"
    "v_smfmac_f32_32x32x32_bf16 v[128:143], v[112:115], v[40:47], v124\n\t"
    "ds_read_b64 v[120:121], v126 offset:54528\n\t"
    "v_exp_f32 v50, v50\n\t"
    "v_exp_f32 v51, v51\n\t"
    "v_exp_f32 v52, v52\n\t"
    "v_exp_f32 v53, v53\n\t"
    "v_mfma_f32_32x32x16_bf16 v[64:79], v[100:103], v[16:19], v[64:79]\n\t"
    "v_exp_f32 v54, v54\n\t"
    "v_exp_f32 v55, v55\n\t"
    "v_exp_f32 v56, v56\n\t"
    "v_exp_f32 v57, v57\n\t"
    "v_smfmac_f32_32x32x32_bf16 v[144:159], v[104:107], v[48:55], v124\n\t"
    "v_exp_f32 v58, v58\n\t"
    "v_exp_f32 v59, v59\n\t"
    "v_exp_f32 v60, v60\n\t"
    "v_exp_f32 v61, v61\n\t"
    "v_mfma_f32_32x32x16_bf16 v[80:95], v[100:103], v[20:23], v[80:95]\n\t"
    "v_exp_f32 v62, v62\n\t"
    "v_exp_f32 v63, v63\n\t"
    "v_exp_f32 v64, v64\n\t"
    "v_exp_f32 v65, v65\n\t"
    "v_smfmac_f32_32x32x32_bf16 v[144:159], v[112:115], v[56:63], v124\n\t"
    "s_waitcnt lgkmcnt(0)\n\t"
    "ds_read_b128 v[100:103], v125 offset:53504\n\t"
    "v_exp_f32 v66, v66\n\t"
    "v_exp_f32 v67, v67\n\t"
    "v_exp_f32 v68, v68\n\t"
    "v_exp_f32 v69, v69\n\t"
    "v_mfma_f32_32x32x4_2b_bf16 v[32:63], v[120:121], v[24:25], 0 cbsz:1 abid:0\n\t"
    "ds_read_b128 v[104:107], v127 offset:55040\n\t"
    "v_exp_f32 v70, v70\n\t"
    "v_exp_f32 v71, v71\n\t"
    "v_exp_f32 v72, v72\n\t"
    "v_exp_f32 v73, v73\n\t"
    "v_exp_f32 v74, v74\n\t"
    "v_exp_f32 v75, v75\n\t"
    "v_exp_f32 v76, v76\n\t"
    "v_exp_f32 v77, v77\n\t"
    "v_smfmac_f32_32x32x32_bf16 v[128:143], v[108:111], v[64:71], v124\n\t"
    "ds_read_b128 v[112:115], v127 offset:55712\n\t"
    "v_exp_f32 v78, v78\n\t"
    "v_exp_f32 v79, v79\n\t"
    "v_exp_f32 v80, v80\n\t"
    "v_exp_f32 v81, v81\n\t"
    "v_smfmac_f32_32x32x32_bf16 v[128:143], v[116:119], v[72:79], v124\n\t"
    "v_exp_f32 v82, v82\n\t"
    "v_exp_f32 v83, v83\n\t"
    "v_exp_f32 v84, v84\n\t"
    "v_exp_f32 v85, v85\n\t"
    "v_mfma_f32_32x32x16_bf16 v[32:47], v[96:99], v[16:19], v[32:47]\n\t"
    "v_exp_f32 v86, v86\n\t"
    "v_exp_f32 v87, v87\n\t"
    "v_exp_f32 v88, v88\n\t"
    "v_exp_f32 v89, v89\n\t"
    "v_smfmac_f32_32x32x32_bf16 v[144:159], v[108:111], v[80:87], v124\n\t"
    "v_exp_f32 v90, v90\n\t"
    "v_exp_f32 v91, v91\n\t"
    "v_exp_f32 v92, v92\n\t"
    "v_exp_f32 v93, v93\n\t"
    "v_mfma_f32_32x32x16_bf16 v[48:63], v[96:99], v[20:23], v[48:63]\n\t"
    "v_exp_f32 v94, v94\n\t"
    "v_exp_f32 v95, v95\n\t"
    "v_exp_f32 v32, v32\n\t"
    "v_exp_f32 v33, v33\n\t"
    "v_smfmac_f32_32x32x32_bf16 v[144:159], v[116:119], v[88:95], v124\n\t"
    "s_waitcnt lgkmcnt(0)\n\t"
    "ds_read_b128 v[96:99], v125 offset:57728\n\t"
    "v_exp_f32 v34, v34\n\t"
    "v_exp_f32 v35, v35\n\t"
    "v_exp_f32 v36, v36\n\t"
    "v_exp_f32 v37, v37\n\t"
    "v_mfma_f32_32x32x4_2b_bf16 v[64:95], v[120:121], v[24:25], 0 cbsz:1 abid:1\n\t"
    "ds_read_b128 v[108:111], v127 offset:56384\n\t"
    "v_exp_f32 v38, v38\n\t"
    "v_exp_f32 v39, v39\n\t"
    "v_exp_f32 v40, v40\n\t"
    "v_exp_f32 v41, v41\n\t"
    "v_exp_f32 v42, v42\n\t"
    "v_exp_f32 v43, v43\n\t"
    "v_exp_f32 v44, v44\n\t"
    "v_exp_f32 v45, v45\n\t"
    "v_smfmac_f32_32x32x32_bf16 v[128:143], v[104:107], v[32:39], v124\n\t"
    "ds_read_b128 v[116:119], v127 offset:57056\n\t"
    "v_exp_f32 v46, v46\n\t"
    "v_exp_f32 v47, v47\n\t"
    "v_exp_f32 v48, v48\n\t"
    "v_exp_f32 v49, v49\n\t"
    "v_smfmac_f32_32x32x32_bf16 v[128:143], v[112:115], v[40:47], v124\n\t"
    "ds_read_b64 v[122:123], v126 offset:59776\n\t"
    "v_exp_f32 v50, v50\n\t"
    "v_exp_f32 v51, v51\n\t"
    "v_exp_f32 v52, v52\n\t"
    "v_exp_f32 v53, v53\n\t"
    "v_mfma_f32_32x32x16_bf16 v[64:79], v[100:103], v[16:19], v[64:79]\n\t"
    "v_exp_f32 v54, v54\n\t"
    "v_exp_f32 v55, v55\n\t"
    "v_exp_f32 v56, v56\n\t"
    "v_exp_f32 v57, v57\n\t"
    "v_smfmac_f32_32x32x32_bf16 v[144:159], v[104:107], v[48:55], v124\n\t"
    "v_exp_f32 v58, v58\n\t"
    "v_exp_f32 v59, v59\n\t"
    "v_exp_f32 v60, v60\n\t"
    "v_exp_f32 v61, v61\n\t"
    "v_mfma_f32_32x32x16_bf16 v[80:95], v[100:103], v[20:23], v[80:95]\n\t"
    "v_exp_f32 v62, v62\n\t"
    "v_exp_f32 v63, v63\n\t"
    "v_exp_f32 v64, v64\n\t"
    "v_exp_f32 v65, v65\n\t"
    "v_smfmac_f32_32x32x32_bf16 v[144:159], v[112:115], v[56:63], v124\n\t"
    "s_waitcnt lgkmcnt(0)\n\t"
    "ds_read_b128 v[100:103], v125 offset:58752\n\t"
    "v_exp_f32 v66, v66\n\t"
    "v_exp_f32 v67, v67\n\t"
    "v_exp_f32 v68, v68\n\t"
    "v_exp_f32 v69, v69\n\t"
    "v_mfma_f32_32x32x4_2b_bf16 v[32:63], v[122:123], v[24:25], 0 cbsz:1 abid:0\n\t"
    "ds_read_b128 v[104:107], v127 offset:60288\n\t"
    "v_exp_f32 v70, v70\n\t"
    "v_exp_f32 v71, v71\n\t"
    "v_exp_f32 v72, v72\n\t"
    "v_exp_f32 v73, v73\n\t"
    "v_exp_f32 v74, v74\n\t"
    "v_exp_f32 v75, v75\n\t"
    "v_exp_f32 v76, v76\n\t"
    "v_exp_f32 v77, v77\n\t"
    "v_smfmac_f32_32x32x32_bf16 v[128:143], v[108:111], v[64:71], v124\n\t"
    "ds_read_b128 v[112:115], v127 offset:60960\n\t"
    "v_exp_f32 v78, v78\n\t"
    "v_exp_f32 v79, v79\n\t"
    "v_exp_f32 v80, v80\n\t"
    "v_exp_f32 v81, v81\n\t"
    "v_smfmac_f32_32x32x32_bf16 v[128:143], v[116:119], v[72:79], v124\n\t"
    "v_exp_f32 v82, v82\n\t"
    "v_exp_f32 v83, v83\n\t"
    "v_exp_f32 v84, v84\n\t"
    "v_exp_f32 v85, v85\n\t"
    "v_mfma_f32_32x32x16_bf16 v[32:47], v[96:99], v[16:19], v[32:47]\n\t"
    "v_exp_f32 v86, v86\n\t"
    "v_exp_f32 v87, v87\n\t"
    "v_exp_f32 v88, v88\n\t"
    "v_exp_f32 v89, v89\n\t"
    "v_smfmac_f32_32x32x32_bf16 v[144:159], v[108:111], v[80:87], v124\n\t"
    "v_exp_f32 v90, v90\n\t"
    "v_exp_f32 v91, v91\n\t"
    "v_exp_f32 v92, v92\n\t"
    "v_exp_f32 v93, v93\n\t"
    "pad13_%=:\n\t"
    "s_cbranch_execz vb15_%=\n\t"
    "v_mfma_f32_32x32x16_bf16 v[48:63], v[96:99], v[20:23], v[48:63]\n\t"
    "v_exp_f32 v94, v94\n\t"
    "v_exp_f32 v95, v95\n\t"
    "v_exp_f32 v32, v32\n\t"
    "v_exp_f32 v33, v33\n\t"
    "v_smfmac_f32_32x32x32_bf16 v[144:159], v[116:119], v[88:95], v124\n\t"
    "s_waitcnt lgkmcnt(0)\n\t"
    "v_add_u32 v125, 0xf600, v125\n\t"
    "ds_read_b128 v[96:99], v125\n\t"
    "v_exp_f32 v34, v34\n\t"
    "v_exp_f32 v35, v35\n\t"
    "v_exp_f32 v36, v36\n\t"
    "v_exp_f32 v37, v37\n\t"
    "v_mfma_f32_32x32x4_2b_bf16 v[64:95], v[122:123], v[24:25], 0 cbsz:1 abid:1\n\t"
    "ds_read_b128 v[108:111], v127 offset:61632\n\t"
    "v_exp_f32 v38, v38\n\t"
    "v_exp_f32 v39, v39\n\t"
    "v_exp_f32 v40, v40\n\t"
    "v_exp_f32 v41, v41\n\t"
    "v_exp_f32 v42, v42\n\t"
    "v_exp_f32 v43, v43\n\t"
    "v_exp_f32 v44, v44\n\t"
    "v_exp_f32 v45, v45\n\t"
    "v_smfmac_f32_32x32x32_bf16 v[128:143], v[104:107], v[32:39], v124\n\t"
    "ds_read_b128 v[116:119], v127 offset:62304\n\t"
    "v_exp_f32 v46, v46\n\t"
    "v_exp_f32 v47, v47\n\t"
    "v_exp_f32 v48, v48\n\t"
    "v_exp_f32 v49, v49\n\t"
    "v_smfmac_f32_32x32x32_bf16 v[128:143], v[112:115], v[40:47], v124\n\t"
    "v_add_u32 v126, 0xf600, v126\n\t"
    "ds_read_b64 v[120:121], v126 offset:2048\n\t"
    "v_exp_f32 v50, v50\n\t"
    "v_exp_f32 v51, v51\n\t"
    "v_exp_f32 v52, v52\n\t"
    "v_exp_f32 v53, v53\n\t"
    "v_mfma_f32_32x32x16_bf16 v[64:79], v[100:103], v[16:19], v[64:79]\n\t"
    "v_exp_f32 v54, v54\n\t"
    "v_exp_f32 v55, v55\n\t"
    "v_exp_f32 v56, v56\n\t"
    "v_exp_f32 v57, v57\n\t"
    "v_smfmac_f32_32x32x32_bf16 v[144:159], v[104:107], v[48:55], v124\n\t"
    "v_exp_f32 v58, v58\n\t"
    "v_exp_f32 v59, v59\n\t"
    "v_exp_f32 v60, v60\n\t"
    "v_exp_f32 v61, v61\n\t"
    "v_mfma_f32_32x32x16_bf16 v[80:95], v[100:103], v[20:23], v[80:95]\n\t"
    "v_exp_f32 v62, v62\n\t"
    "v_exp_f32 v63, v63\n\t"
    "v_exp_f32 v64, v64\n\t"
    "v_exp_f32 v65, v65\n\t"
    "v_smfmac_f32_32x32x32_bf16 v[144:159], v[112:115], v[56:63], v124\n\t"
    "s_waitcnt lgkmcnt(0)\n\t"
    "ds_read_b128 v[100:103], v125 offset:1024\n\t"
    "v_exp_f32 v66, v66\n\t"
    "v_exp_f32 v67, v67\n\t"
    "v_exp_f32 v68, v68\n\t"
    "v_exp_f32 v69, v69\n\t"
    "v_mfma_f32_32x32x4_2b_bf16 v[32:63], v[120:121], v[24:25], 0 cbsz:1 abid:0\n\t"
    "v_add_u32 v127, 0xf600, v127\n\t"
    "ds_read_b128 v[104:107], v127 offset:2560\n\t"
    "v_exp_f32 v70, v70\n\t"
    "v_exp_f32 v71, v71\n\t"
    "v_exp_f32 v72, v72\n\t"
    "v_exp_f32 v73, v73\n\t"
    "v_exp_f32 v74, v74\n\t"
    "v_exp_f32 v75, v75\n\t"
    "v_exp_f32 v76, v76\n\t"
    "v_exp_f32 v77, v77\n\t"
    "v_smfmac_f32_32x32x32_bf16 v[128:143], v[108:111], v[64:71], v124\n\t"
    "ds_read_b128 v[112:115], v127 offset:3232\n\t"
    "v_exp_f32 v78, v78\n\t"
    "v_exp_f32 v79, v79\n\t"
    "v_exp_f32 v80, v80\n\t"
    "v_exp_f32 v81, v81\n\t"
    "v_smfmac_f32_32x32x32_bf16 v[128:143], v[116:119], v[72:79], v124\n\t"
    "v_exp_f32 v82, v82\n\t"
    "v_exp_f32 v83, v83\n\t"
    "v_exp_f32 v84, v84\n\t"
    "v_exp_f32 v85, v85\n\t"
    "v_mfma_f32_32x32x16_bf16 v[32:47], v[96:99], v[16:19], v[32:47]\n\t"
    "v_exp_f32 v86, v86\n\t"
    "v_exp_f32 v87, v87\n\t"
    "v_exp_f32 v88, v88\n\t"
    "v_exp_f32 v89, v89\n\t"
    "v_smfmac_f32_32x32x32_bf16 v[144:159], v[108:111], v[80:87], v124\n\t"
    "v_exp_f32 v90, v90\n\t"
    "v_exp_f32 v91, v91\n\t"
    "v_exp_f32 v92, v92\n\t"
    "v_exp_f32 v93, v93\n\t"
    "v_mfma_f32_32x32x16_bf16 v[48:63], v[96:99], v[20:23], v[48:63]\n\t"
    "v_exp_f32 v94, v94\n\t"
    "v_exp_f32 v95, v95\n\t"
    "v_exp_f32 v32, v32\n\t"
    "v_exp_f32 v33, v33\n\t"
    "v_smfmac_f32_32x32x32_bf16 v[144:159], v[116:119], v[88:95], v124\n\t"
    "s_waitcnt lgkmcnt(0)\n\t"
    "ds_read_b128 v[96:99], v125 offset:5248\n\t"
    "v_exp_f32 v34, v34\n\t"
    "v_exp_f32 v35, v35\n\t"
    "v_exp_f32 v36, v36\n\t"
    "v_exp_f32 v37, v37\n\t"
    "v_mfma_f32_32x32x4_2b_bf16 v[64:95], v[120:121], v[24:25], 0 cbsz:1 abid:1\n\t"
    "ds_read_b128 v[108:111], v127 offset:3904\n\t"
    "v_exp_f32 v38, v38\n\t"
    "v_exp_f32 v39, v39\n\t"
    "v_exp_f32 v40, v40\n\t"
    "v_exp_f32 v41, v41\n\t"
    "v_exp_f32 v42, v42\n\t"
    "v_exp_f32 v43, v43\n\t"
    "v_exp_f32 v44, v44\n\t"
    "v_exp_f32 v45, v45\n\t"
    "v_smfmac_f32_32x32x32_bf16 v[128:143], v[104:107], v[32:39], v124\n\t"
    "ds_read_b128 v[116:119], v127 offset:4576\n\t"
    "v_exp_f32 v46, v46\n\t"
    "v_exp_f32 v47, v47\n\t"
    "v_exp_f32 v48, v48\n\t"
    "v_exp_f32 v49, v49\n\t"
    "v_smfmac_f32_32x32x32_bf16 v[128:143], v[112:115], v[40:47], v124\n\t"
    "ds_read_b64 v[122:123], v126 offset:7296\n\t"
    "v_exp_f32 v50, v50\n\t"
    "v_exp_f32 v51, v51\n\t"
    "v_exp_f32 v52, v52\n\t"
    "v_exp_f32 v53, v53\n\t"
    "v_mfma_f32_32x32x16_bf16 v[64:79], v[100:103], v[16:19], v[64:79]\n\t"
    "v_exp_f32 v54, v54\n\t"
    "v_exp_f32 v55, v55\n\t"
    "v_exp_f32 v56, v56\n\t"
    "v_exp_f32 v57, v57\n\t"
    "v_smfmac_f32_32x32x32_bf16 v[144:159], v[104:107], v[48:55], v124\n\t"
    "v_exp_f32 v58, v58\n\t"
    "v_exp_f32 v59, v59\n\t"
    "v_exp_f32 v60, v60\n\t"
    "v_exp_f32 v61, v61\n\t"
    "v_mfma_f32_32x32x16_bf16 v[80:95], v[100:103], v[20:23], v[80:95]\n\t"
    "v_exp_f32 v62, v62\n\t"
    "v_exp_f32 v63, v63\n\t"
    "v_exp_f32 v64, v64\n\t"
    "v_exp_f32 v65, v65\n\t"
    "v_smfmac_f32_32x32x32_bf16 v[144:159], v[112:115], v[56:63], v124\n\t"
    "s_waitcnt lgkmcnt(0)\n\t"
    "ds_read_b128 v[100:103], v125 offset:6272\n\t"
    "v_exp_f32 v66, v66\n\t"
    "v_exp_f32 v67, v67\n\t"
    "v_exp_f32 v68, v68\n\t"
    "v_exp_f32 v69, v69\n\t"
    "v_mfma_f32_32x32x4_2b_bf16 v[32:63], v[122:123], v[24:25], 0 cbsz:1 abid:0\n\t"
    "ds_read_b128 v[104:107], v127 offset:7808\n\t"
    "v_exp_f32 v70, v70\n\t"
    "v_exp_f32 v71, v71\n\t"
    "v_exp_f32 v72, v72\n\t"
    "v_exp_f32 v73, v73\n\t"
    "v_exp_f32 v74, v74\n\t"
    "v_exp_f32 v75, v75\n\t"
    "v_exp_f32 v76, v76\n\t"
    "v_exp_f32 v77, v77\n\t"
    "v_smfmac_f32_32x32x32_bf16 v[128:143], v[108:111], v[64:71], v124\n\t"
    "ds_read_b128 v[112:115], v127 offset:8480\n\t"
    "v_exp_f32 v78, v78\n\t"
    "v_exp_f32 v79, v79\n\t"
    "v_exp_f32 v80, v80\n\t"
    "v_exp_f32 v81, v81\n\t"
    "v_smfmac_f32_32x32x32_bf16 v[128:143], v[116:119], v[72:79], v124\n\t"
    "v_exp_f32 v82, v82\n\t"
    "v_exp_f32 v83, v83\n\t"
    "v_exp_f32 v84, v84\n\t"
    "v_exp_f32 v85, v85\n\t"
    "v_mfma_f32_32x32x16_bf16 v[32:47], v[96:99], v[16:19], v[32:47]\n\t"
    "v_exp_f32 v86, v86\n\t"
    "v_exp_f32 v87, v87\n\t"
    "v_exp_f32 v88, v88\n\t"
    "v_exp_f32 v89, v89\n\t"
    "v_smfmac_f32_32x32x32_bf16 v[144:159], v[108:111], v[80:87], v124\n\t"
    "v_exp_f32 v90, v90\n\t"
    "v_exp_f32 v91, v91\n\t"
    "v_exp_f32 v92, v92\n\t"
    "v_exp_f32 v93, v93\n\t"
    "v_mfma_f32_32x32x16_bf16 v[48:63], v[96:99], v[20:23], v[48:63]\n\t"
    "v_exp_f32 v94, v94\n\t"
    "v_exp_f32 v95, v95\n\t"
    "s_branch layer27_%=\n\t"
    "L50_%=:\n\t"
    "v_and_b32 v168, 15, v0\n\t"
    "v_mul_u32_u24 v168, 0x500, v168\n\t"
    "v_lshrrev_b32 v30, 4, v0\n\t"
    "v_lshl_add_u32 v168, v30, 4, v168\n\t"
    "v_and_b32 v169, 15, v0\n\t"
    "v_mul_u32_u24 v169, 0x500, v169\n\t"
    "v_lshrrev_b32 v30, 4, v0\n\t"
    "v_lshl_add_u32 v169, v30, 3, v169\n\t"
    "s_mul_i32 s50, s35, 0x14000\n\t"
    "s_mul_i32 s53, s33, 0x5000\n\t"
    "s_add_u32 s50, s50, s53\n\t"
    "s_add_u32 s51, s50, s71\n\t"
    "s_add_u32 s50, s50, s70\n\t"
    "buffer_store_dwordx4 v[4:7], v168, s[24:27], s50 offen\n\t"
    "buffer_store_dwordx4 v[8:11], v168, s[24:27], s50 offen offset:64\n\t"
    "buffer_store_dwordx2 v[12:13], v169, s[24:27], s51 offen\n\t"
    "s_endpgm\n\t"
    "kend_%=:\n\t"
    "slow28_%=:\n\t"
    "s_waitcnt vmcnt(0) lgkmcnt(0)\n\t"
    "v_lshlrev_b32 v126, 3, v0\n\t"
    "v_add_u32 v127, 0xffff0a00, v127\n\t"
    "v_mov_b32 v30, 0x280\n\t"
    "v_cmp_eq_u32 s[42:43], v30, v127\n\t"
    "v_mov_b32 v125, 0x3f803f80\n\t"
    "v_mov_b32 v64, 0xff800000\n\t"
    "v_mov_b32 v80, 0xff800000\n\t"
    "s_mov_b32 s50, s45\n\t"
    "s_mov_b32 s52, 32\n\t"
    "s_nop 15\n\t"
    "s_nop 3\n\t"
    "fb29_%=:\n\t"
    "s_add_u32 s51, s50, 0xa00\n\t"
    "buffer_load_dwordx4 v[96:99], v1, s[12:15], s50 offen sc1\n\t"
    "buffer_load_dwordx4 v[100:103], v1, s[12:15], s50 offen offset:1024 sc1\n\t"
    "buffer_load_dwordx2 v[120:121], v126, s[12:15], s50 offen offset:2048 sc1\n\t"
    "s_waitcnt vmcnt(2)\n\t"
    "v_mfma_f32_32x32x16_bf16 v[32:47], v[96:99], v[16:19], 0\n\t"
    "v_mfma_f32_32x32x16_bf16 v[48:63], v[96:99], v[20:23], 0\n\t"
    "s_waitcnt vmcnt(0)\n\t"
    "s_nop 8\n\t"
    "v_mfma_f32_32x32x4_2b_bf16 v[32:63], v[120:121], v[24:25], v[32:63] cbsz:1 abid:0\n\t"
    "s_nop 15\n\t"
    "s_nop 3\n\t"
    "v_max3_f32 v64, v64, v32, v33\n\t"
    "v_max3_f32 v64, v64, v34, v35\n\t"
    "v_max3_f32 v64, v64, v36, v37\n\t"
    "v_max3_f32 v64, v64, v38, v39\n\t"
    "v_max3_f32 v64, v64, v40, v41\n\t"
    "v_max3_f32 v64, v64, v42, v43\n\t"
    "v_max3_f32 v64, v64, v44, v45\n\t"
    "v_max3_f32 v64, v64, v46, v47\n\t"
    "v_max3_f32 v80, v80, v48, v49\n\t"
    "v_max3_f32 v80, v80, v50, v51\n\t"
    "v_max3_f32 v80, v80, v52, v53\n\t"
    "v_max3_f32 v80, v80, v54, v55\n\t"
    "v_max3_f32 v80, v80, v56, v57\n\t"
    "v_max3_f32 v80, v80, v58, v59\n\t"
    "v_max3_f32 v80, v80, v60, v61\n\t"
    "v_max3_f32 v80, v80, v62, v63\n\t"
    "v_mfma_f32_32x32x16_bf16 v[32:47], v[100:103], v[16:19], 0\n\t"
    "v_mfma_f32_32x32x16_bf16 v[48:63], v[100:103], v[20:23], 0\n\t"
    "s_nop 9\n\t"
    "v_mfma_f32_32x32x4_2b_bf16 v[32:63], v[120:121], v[24:25], v[32:63] cbsz:1 abid:1\n\t"
    "s_nop 15\n\t"
    "s_nop 3\n\t"
    "v_max3_f32 v64, v64, v32, v33\n\t"
    "v_max3_f32 v64, v64, v34, v35\n\t"
    "v_max3_f32 v64, v64, v36, v37\n\t"
    "v_max3_f32 v64, v64, v38, v39\n\t"
    "v_max3_f32 v64, v64, v40, v41\n\t"
    "v_max3_f32 v64, v64, v42, v43\n\t"
    "v_max3_f32 v64, v64, v44, v45\n\t"
    "v_max3_f32 v64, v64, v46, v47\n\t"
    "v_max3_f32 v80, v80, v48, v49\n\t"
    "v_max3_f32 v80, v80, v50, v51\n\t"
    "v_max3_f32 v80, v80, v52, v53\n\t"
    "v_max3_f32 v80, v80, v54, v55\n\t"
    "v_max3_f32 v80, v80, v56, v57\n\t"
    "v_max3_f32 v80, v80, v58, v59\n\t"
    "v_max3_f32 v80, v80, v60, v61\n\t"
    "v_max3_f32 v80, v80, v62, v63\n\t"
    "s_add_u32 s50, s50, 0x1480\n\t"
    "s_sub_u32 s52, s52, 1\n\t"
    "s_cmp_lg_u32 s52, 0\n\t"
    "s_nop 15\n\t"
    "s_nop 3\n\t"
    "s_cbranch_scc1 fb29_%=\n\t"
    "v_mov_b32 v65, v64\n\t"
    "s_nop 1\n\t"
    "v_permlane32_swap_b32 v64, v65\n\t"
    "v_max_f32 v64, v64, v65\n\t"
    "v_xor_b32 v64, 0x80000000, v64\n\t"
    "v_mov_b32 v65, v64\n\t"
    "v_mov_b32 v66, v64\n\t"
    "v_mov_b32 v67, v64\n\t"
    "v_mov_b32 v68, v64\n\t"
    "v_mov_b32 v69, v64\n\t"
    "v_mov_b32 v70, v64\n\t"
    "v_mov_b32 v71, v64\n\t"
    "v_mov_b32 v72, v64\n\t"
    "v_mov_b32 v73, v64\n\t"
    "v_mov_b32 v74, v64\n\t"
    "v_mov_b32 v75, v64\n\t"
    "v_mov_b32 v76, v64\n\t"
    "v_mov_b32 v77, v64\n\t"
    "v_mov_b32 v78, v64\n\t"
    "v_mov_b32 v79, v64\n\t"
    "v_mov_b32 v128, 0\n\t"
    "v_mov_b32 v129, 0\n\t"
    "v_mov_b32 v130, 0\n\t"
    "v_mov_b32 v131, 0\n\t"
    "v_mov_b32 v132, 0\n\t"
    "v_mov_b32 v133, 0\n\t"
    "v_mov_b32 v134, 0\n\t"
    "v_mov_b32 v135, 0\n\t"
    "v_mov_b32 v136, 0\n\t"
    "v_mov_b32 v137, 0\n\t"
    "v_mov_b32 v138, 0\n\t"
    "v_mov_b32 v139, 0\n\t"
    "v_mov_b32 v140, 0\n\t"
    "v_mov_b32 v141, 0\n\t"
    "v_mov_b32 v142, 0\n\t"
    "v_mov_b32 v143, 0\n\t"
    "v_mov_b32 v81, v80\n\t"
    "s_nop 1\n\t"
    "v_permlane32_swap_b32 v80, v81\n\t"
    "v_max_f32 v80, v80, v81\n\t"
    "v_xor_b32 v80, 0x80000000, v80\n\t"
    "v_mov_b32 v81, v80\n\t"
    "v_mov_b32 v82, v80\n\t"
    "v_mov_b32 v83, v80\n\t"
    "v_mov_b32 v84, v80\n\t"
    "v_mov_b32 v85, v80\n\t"
    "v_mov_b32 v86, v80\n\t"
    "v_mov_b32 v87, v80\n\t"
    "v_mov_b32 v88, v80\n\t"
    "v_mov_b32 v89, v80\n\t"
    "v_mov_b32 v90, v80\n\t"
    "v_mov_b32 v91, v80\n\t"
    "v_mov_b32 v92, v80\n\t"
    "v_mov_b32 v93, v80\n\t"
    "v_mov_b32 v94, v80\n\t"
    "v_mov_b32 v95, v80\n\t"
    "v_mov_b32 v144, 0\n\t"
    "v_mov_b32 v145, 0\n\t"
    "v_mov_b32 v146, 0\n\t"
    "v_mov_b32 v147, 0\n\t"
    "v_mov_b32 v148, 0\n\t"
    "v_mov_b32 v149, 0\n\t"
    "v_mov_b32 v150, 0\n\t"
    "v_mov_b32 v151, 0\n\t"
    "v_mov_b32 v152, 0\n\t"
    "v_mov_b32 v153, 0\n\t"
    "v_mov_b32 v154, 0\n\t"
    "v_mov_b32 v155, 0\n\t"
    "v_mov_b32 v156, 0\n\t"
    "v_mov_b32 v157, 0\n\t"
    "v_mov_b32 v158, 0\n\t"
    "v_mov_b32 v159, 0\n\t"
    "s_mov_b32 s50, s45\n\t"
    "s_mov_b32 s52, 32\n\t"
    "s_nop 15\n\t"
    "s_nop 3\n\t"
    "fb30_%=:\n\t"
    "s_add_u32 s51, s50, 0xa00\n\t"
    "buffer_load_dwordx4 v[96:99], v1, s[12:15], s50 offen sc1\n\t"
    "buffer_load_dwordx4 v[100:103], v1, s[12:15], s50 offen offset:1024 sc1\n\t"
    "buffer_load_dwordx2 v[120:121], v126, s[12:15], s50 offen offset:2048 sc1\n\t"
    "buffer_load_dwordx4 v[104:107], v127, s[12:15], s51 offen sc1\n\t"
    "buffer_load_dwordx4 v[112:115], v127, s[12:15], s51 offen offset:672 sc1\n\t"
    "buffer_load_dwordx4 v[108:111], v127, s[12:15], s51 offen offset:1344 sc1\n\t"
    "buffer_load_dwordx4 v[116:119], v127, s[12:15], s51 offen offset:2016 sc1\n\t"
    "s_waitcnt vmcnt(3)\n\t"
    "v_cndmask_b32 v104, v104, v125, s[42:43]\n\t"
    "v_cndmask_b32 v105, v105, v125, s[42:43]\n\t"
    "v_cndmask_b32 v106, v106, v125, s[42:43]\n\t"
    "v_cndmask_b32 v107, v107, v125, s[42:43]\n\t"
    "s_waitcnt vmcnt(2)\n\t"
    "v_cndmask_b32 v112, v112, v125, s[42:43]\n\t"
    "v_cndmask_b32 v113, v113, v125, s[42:43]\n\t"
    "v_cndmask_b32 v114, v114, v125, s[42:43]\n\t"
    "v_cndmask_b32 v115, v115, v125, s[42:43]\n\t"
    "s_waitcnt vmcnt(1)\n\t"
    "v_cndmask_b32 v108, v108, v125, s[42:43]\n\t"
    "v_cndmask_b32 v109, v109, v125, s[42:43]\n\t"
    "v_cndmask_b32 v110, v110, v125, s[42:43]\n\t"
    "v_cndmask_b32 v111, v111, v125, s[42:43]\n\t"
    "s_waitcnt vmcnt(0)\n\t"
    "v_cndmask_b32 v116, v116, v125, s[42:43]\n\t"
    "v_cndmask_b32 v117, v117, v125, s[42:43]\n\t"
    "v_cndmask_b32 v118, v118, v125, s[42:43]\n\t"
    "v_cndmask_b32 v119, v119, v125, s[42:43]\n\t"
    "v_mfma_f32_32x32x16_bf16 v[32:47], v[96:99], v[16:19], v[64:79]\n\t"
    "v_mfma_f32_32x32x16_bf16 v[48:63], v[96:99], v[20:23], v[80:95]\n\t"
    "s_nop 9\n\t"
    "v_mfma_f32_32x32x4_2b_bf16 v[32:63], v[120:121], v[24:25], v[32:63] cbsz:1 abid:0\n\t"
    "s_nop 15\n\t"
    "s_nop 3\n\t"
    "v_exp_f32 v32, v32\n\t"
    "v_exp_f32 v33, v33\n\t"
    "v_exp_f32 v34, v34\n\t"
    "v_exp_f32 v35, v35\n\t"
    "v_exp_f32 v36, v36\n\t"
    "v_exp_f32 v37, v37\n\t"
    "v_exp_f32 v38, v38\n\t"
    "v_exp_f32 v39, v39\n\t"
    "v_exp_f32 v40, v40\n\t"
    "v_exp_f32 v41, v41\n\t"
    "v_exp_f32 v42, v42\n\t"
    "v_exp_f32 v43, v43\n\t"
    "v_exp_f32 v44, v44\n\t"
    "v_exp_f32 v45, v45\n\t"
    "v_exp_f32 v46, v46\n\t"
    "v_exp_f32 v47, v47\n\t"
    "v_exp_f32 v48, v48\n\t"
    "v_exp_f32 v49, v49\n\t"
    "v_exp_f32 v50, v50\n\t"
    "v_exp_f32 v51, v51\n\t"
    "v_exp_f32 v52, v52\n\t"
    "v_exp_f32 v53, v53\n\t"
    "v_exp_f32 v54, v54\n\t"
    "v_exp_f32 v55, v55\n\t"
    "v_exp_f32 v56, v56\n\t"
    "v_exp_f32 v57, v57\n\t"
    "v_exp_f32 v58, v58\n\t"
    "v_exp_f32 v59, v59\n\t"
    "v_exp_f32 v60, v60\n\t"
    "v_exp_f32 v61, v61\n\t"
    "v_exp_f32 v62, v62\n\t"
    "v_exp_f32 v63, v63\n\t"
    "v_smfmac_f32_32x32x32_bf16 v[128:143], v[104:107], v[32:39], v124\n\t"
    "v_smfmac_f32_32x32x32_bf16 v[128:143], v[112:115], v[40:47], v124\n\t"
    "v_smfmac_f32_32x32x32_bf16 v[144:159], v[104:107], v[48:55], v124\n\t"
    "v_smfmac_f32_32x32x32_bf16 v[144:159], v[112:115], v[56:63], v124\n\t"
    "v_mfma_f32_32x32x16_bf16 v[32:47], v[100:103], v[16:19], v[64:79]\n\t"
    "v_mfma_f32_32x32x16_bf16 v[48:63], v[100:103], v[20:23], v[80:95]\n\t"
    "s_nop 9\n\t"
    "v_mfma_f32_32x32x4_2b_bf16 v[32:63], v[120:121], v[24:25], v[32:63] cbsz:1 abid:1\n\t"
    "s_nop 15\n\t"
    "s_nop 3\n\t"
    "v_exp_f32 v32, v32\n\t"
    "v_exp_f32 v33, v33\n\t"
    "v_exp_f32 v34, v34\n\t"
    "v_exp_f32 v35, v35\n\t"
    "v_exp_f32 v36, v36\n\t"
    "v_exp_f32 v37, v37\n\t"
    "v_exp_f32 v38, v38\n\t"
    "v_exp_f32 v39, v39\n\t"
    "v_exp_f32 v40, v40\n\t"
    "v_exp_f32 v41, v41\n\t"
    "v_exp_f32 v42, v42\n\t"
    "v_exp_f32 v43, v43\n\t"
    "v_exp_f32 v44, v44\n\t"
    "v_exp_f32 v45, v45\n\t"
    "v_exp_f32 v46, v46\n\t"
    "v_exp_f32 v47, v47\n\t"
    "v_exp_f32 v48, v48\n\t"
    "v_exp_f32 v49, v49\n\t"
    "v_exp_f32 v50, v50\n\t"
    "v_exp_f32 v51, v51\n\t"
    "v_exp_f32 v52, v52\n\t"
    "v_exp_f32 v53, v53\n\t"
    "v_exp_f32 v54, v54\n\t"
    "v_exp_f32 v55, v55\n\t"
    "v_exp_f32 v56, v56\n\t"
    "v_exp_f32 v57, v57\n\t"
    "v_exp_f32 v58, v58\n\t"
    "v_exp_f32 v59, v59\n\t"
    "v_exp_f32 v60, v60\n\t"
    "v_exp_f32 v61, v61\n\t"
    "v_exp_f32 v62, v62\n\t"
    "v_exp_f32 v63, v63\n\t"
    "v_smfmac_f32_32x32x32_bf16 v[128:143], v[108:111], v[32:39], v124\n\t"
    "v_smfmac_f32_32x32x32_bf16 v[128:143], v[116:119], v[40:47], v124\n\t"
    "v_smfmac_f32_32x32x32_bf16 v[144:159], v[108:111], v[48:55], v124\n\t"
    "v_smfmac_f32_32x32x32_bf16 v[144:159], v[116:119], v[56:63], v124\n\t"
    "s_add_u32 s50, s50, 0x1480\n\t"
    "s_sub_u32 s52, s52, 1\n\t"
    "s_cmp_lg_u32 s52, 0\n\t"
    "s_nop 15\n\t"
    "s_nop 3\n\t"
    "s_cbranch_scc1 fb30_%=\n\t"
    "v_rcp_f32 v36, v140\n\t"
    "v_add_u32 v32, 0xcf800000, v140\n\t"
    "v_mul_f32 v128, v128, v36\n\t"
    "v_mul_f32 v129, v129, v36\n\t"
    "v_mul_f32 v130, v130, v36\n\t"
    "v_mul_f32 v131, v131, v36\n\t"
    "v_mul_f32 v132, v132, v36\n\t"
    "v_mul_f32 v133, v133, v36\n\t"
    "v_mul_f32 v134, v134, v36\n\t"
    "v_mul_f32 v135, v135, v36\n\t"
    "v_mul_f32 v136, v136, v36\n\t"
    "v_mul_f32 v137, v137, v36\n\t"
    "v_mul_f32 v138, v138, v36\n\t"
    "v_mul_f32 v139, v139, v36\n\t"
    "v_cvt_pk_bf16_f32 v40, v128, v129\n\t"
    "v_cvt_pk_bf16_f32 v41, v130, v131\n\t"
    "v_cvt_pk_bf16_f32 v42, v132, v133\n\t"
    "v_cvt_pk_bf16_f32 v43, v134, v135\n\t"
    "buffer_store_dwordx4 v[40:43], v26, s[12:15], s78 offen sc1\n\t"
    "v_cvt_pk_bf16_f32 v48, v136, v137\n\t"
    "v_cvt_pk_bf16_f32 v49, v138, v139\n\t"
    "s_branch back31_%=\n\t"
    ".p2align 12\n\t"

    :: "s"(__builtin_amdgcn_kernarg_segment_ptr()), "s"(w), "s"(blockIdx.x), "s"(ldsb) : "v0","v1","v2","v3","v4","v5","v6","v7","v8","v9","v10","v11","v12","v13","v14","v15","v16","v17","v18","v19","v20","v21","v22","v23","v24","v25","v26","v27","v28","v29","v30","v31","v32","v33","v34","v35","v36","v37","v38","v39","v40","v41","v42","v43","v44","v45","v46","v47","v48","v49","v50","v51","v52","v53","v54","v55","v56","v57","v58","v59","v60","v61","v62","v63","v64","v65","v66","v67","v68","v69","v70","v71","v72","v73","v74","v75","v76","v77","v78","v79","v80","v81","v82","v83","v84","v85","v86","v87","v88","v89","v90","v91","v92","v93","v94","v95","v96","v97","v98","v99","v100","v101","v102","v103","v104","v105","v106","v107","v108","v109","v110","v111","v112","v113","v114","v115","v116","v117","v118","v119","v120","v121","v122","v123","v124","v125","v126","v127","v128","v129","v130","v131","v132","v133","v134","v135","v136","v137","v138","v139","v140","v141","v142","v143","v144","v145","v146","v147","v148","v149","v150","v151","v152","v153","v154","v155","v156","v157","v158","v159","v160","v161","v162","v163","v164","v165","v166","v167","v168","v169","v170","v171","v172","v173","v174","v175","v176","v177","v178","v179","v180","v181","v182","v183","v184","v185","v186","v187","v188","v189","v190","v191","v192","v193","v194","v195","v196","v197","v198","v199","v200","v201","v202","v203","v204","v205","v206","v207","v208","v209","v210","v211","v212","v213","v214","v215","v216","v217","v218","v219","v220","v221","v222","v223","v224","v225","v226","v227","v228","v229","v230","v231","v232","v233","v234","v235","v236","v237","v238","v239","v240","v241","v242","v243","v244","v245","v246","v247","v248","v249","v250","v251","v252","v253","v254","v255","a0","a1","a2","a3","a4","a5","a6","a7","a8","a9","a10","a11","a12","a13","a14","a15","a16","a17","a18","a19","a20","a21","a22","a23","a24","a25","a26","a27","a28","a29","a30","a31","a32","a33","a34","a35","a36","a37","a38","a39","a40","a41","a42","a43","a44","a45","a46","a47","a48","a49","a50","a51","a52","a53","a54","a55","a56","a57","a58","a59","a60","a61","a62","a63","a64","a65","a66","a67","a68","a69","a70","a71","a72","a73","a74","a75","a76","a77","a78","a79","a80","a81","a82","a83","a84","a85","a86","a87","a88","a89","a90","a91","a92","a93","a94","a95","a96","a97","a98","a99","a100","a101","a102","a103","a104","a105","a106","a107","a108","a109","a110","a111","a112","a113","a114","a115","a116","a117","a118","a119","a120","a121","a122","a123","a124","a125","a126","a127","a128","a129","a130","a131","a132","a133","a134","a135","a136","a137","a138","a139","a140","a141","a142","a143","a144","a145","a146","a147","a148","a149","a150","a151","a152","a153","a154","a155","a156","a157","a158","a159","a160","a161","a162","a163","a164","a165","a166","a167","a168","a169","a170","a171","a172","a173","a174","a175","a176","a177","a178","a179","a180","a181","a182","a183","a184","a185","a186","a187","a188","a189","a190","a191","a192","a193","a194","a195","a196","a197","a198","a199","a200","a201","a202","a203","a204","a205","a206","a207","a208","a209","a210","a211","a212","a213","a214","a215","a216","a217","a218","a219","a220","a221","a222","a223","a224","a225","a226","a227","a228","a229","a230","a231","a232","a233","a234","a235","a236","a237","a238","a239","a240","a241","a242","a243","a244","a245","a246","a247","a248","a249","a250","a251","a252","a253","a254","a255","s12","s13","s14","s15","s16","s17","s18","s19","s20","s21","s22","s23","s24","s25","s26","s27","s28","s29","s30","s31","s33","s34","s35","s36","s37","s38","s39","s40","s41","s42","s43","s44","s45","s46","s47","s48","s49","s50","s51","s52","s53","s54","s55","s56","s57","s58","s59","s60","s61","s62","s63","s64","s65","s66","s67","s68","s69","s70","s71","s72","s73","s74","s75","s76","s77","s78","s79","s80","s81","s82","s83","s84","s85","s86","s87","s88","s89","s90","s91","s92","s93","s94","s95","s96","s97","s98","s99","vcc","scc","memory");
}

extern "C" void kernel_launch(void* const* d_in, const int* in_sizes, int n_in, void* d_out, int out_size, void* d_ws, size_t ws_size,
                              hipStream_t stream) {
  k_main<<<256, 256, 0, stream>>>(d_ws, d_in[0], d_in[1], d_out, d_in[9], d_in[2], d_in[3], d_in[4], d_in[6], d_in[8], d_in[7]);
}
```
